```python
import torch

def _softmax(x, axis=-1):
    return torch.softmax(x, dim=axis)
def _gelu(x):
    return torch.nn.functional.gelu(x, approximate="tanh")
def _silu(x):
    return torch.nn.functional.silu(x)
def _sigmoid(x):
    return torch.sigmoid(x)
def _layernorm(x, axis=-1, eps=1e-5):
    m = x.mean(dim=axis, keepdim=True)
    v = x.var(dim=axis, keepdim=True, unbiased=False)
    return (x - m) / torch.sqrt(v + eps)
def _scatter_add(tgt, idx, src, axis=0):
    return tgt.clone().index_add_(axis, idx, src)
def _segment_sum(src, seg, n):
    out = torch.zeros((n,) + src.shape[1:], dtype=src.dtype)
    return out.index_add_(0, seg, src)
def _pad(x, pw):
    flat = tuple(v for pair in reversed(pw) for v in pair)
    return torch.nn.functional.pad(x, flat)

kernel_name = 'parallel_sum__ssm_conv_s4d__dense_activation_mish__prompt'

def setup_inputs():

    _w = (lambda a: (lambda t: t.to(torch.bfloat16) if t.is_floating_point() else t)(torch.as_tensor(a)))
    def _A_setup():
        Bsz, L, D, N = 1, 2048, 640, 128
        return {
            "u": _w((lambda *s: torch.randn(*s, dtype=torch.bfloat16) * 0.05)(Bsz, L, D)),
            "log_A_real": _w((lambda *s: torch.randn(*s, dtype=torch.bfloat16) * 3.2)(D, N) * 0.5),
            "A_imag": _w((lambda *s: torch.randn(*s, dtype=torch.bfloat16) * 0.05)(D, N) * 3.14),
            "C_real": _w((lambda *s: torch.randn(*s, dtype=torch.bfloat16) * 0.2)(D, N) / N ** 0.5),
            "C_imag": _w((lambda *s: torch.randn(*s, dtype=torch.bfloat16) * 0.2)(D, N) / N ** 0.5),
            "log_dt": _w((lambda *s: torch.randn(*s, dtype=torch.bfloat16) * 0.05)(D) * 0.5 - 4.0),
            "D_skip": _w((lambda *s: torch.randn(*s, dtype=torch.bfloat16) * 0.05)(D)),
        }
    _A_in = _A_setup()

    def _B_setup():
        B, S, D, F = 1, 2048, 640, 1728
        return {
            "x": _w((lambda *s: torch.randn(*s, dtype=torch.bfloat16) * 0.05)(B, S, D)),
            "w1": _w((lambda *s: torch.randn(*s, dtype=torch.bfloat16) * 0.8)(D, F) * (D ** -0.5)),
            "b1": _w((lambda *s: torch.randn(*s, dtype=torch.bfloat16) * 0.2)(F)),
            "w2": _w((lambda *s: torch.randn(*s, dtype=torch.bfloat16) * 0.2)(F, D) * (F ** -0.5)),
        }
    _B_in = _B_setup()

    return {
        "A_u": _A_in["u"],
        "A_log_A_real": _A_in["log_A_real"],
        "A_A_imag": _A_in["A_imag"],
        "A_C_real": _A_in["C_real"],
        "A_C_imag": _A_in["C_imag"],
        "A_log_dt": _A_in["log_dt"],
        "A_D_skip": _A_in["D_skip"],
        "B_x": _B_in["x"],
        "B_w1": _B_in["w1"],
        "B_b1": _B_in["b1"],
        "B_w2": _B_in["w2"],
    }

def reference(A_u, A_log_A_real, A_A_imag, A_C_real, A_C_imag, A_log_dt, A_D_skip, B_x, B_w1, B_b1, B_w2):
    def _A_ref(u, log_A_real, A_imag, C_real, C_imag, log_dt, D_skip):
        L = u.shape[1]
        dt = torch.exp(log_dt)
        A = -torch.exp(log_A_real) + 1j * A_imag
        C = (C_real + 1j * C_imag) * dt[:, None]
        pos = (lambda x, dt: x.to(dt))(torch.arange(L), log_dt.dtype)
        P = torch.exp((A * dt[:, None])[:, :, None] * pos[None, None, :])
        K = 2.0 * torch.einsum("dn,dnl->dl", C, P).real
        L2 = 2 * L
        u_t = (lambda x, *d: x.permute(*d))(u, 0, 2, 1)
        Kf = (lambda x, *a, **k: torch.fft.rfft(x.float(), *a, **k))(K, L2)
        Uf = (lambda x, *a, **k: torch.fft.rfft(x.float(), *a, **k))(u_t, L2)
        y = (lambda x, *a, **k: torch.fft.irfft(x, *a, **k).to(torch.bfloat16))(Uf * Kf[None, :, :], L2)[:, :, :L]
        y = (lambda x, *d: x.permute(*d))(y, 0, 2, 1)
        return y + u * D_skip[None, None, :]

    def _B_ref(x, w1, b1, w2):
        h = torch.einsum("bsd,df->bsf", x, w1) + b1
        sp = torch.log1p(torch.exp(-torch.abs(h))) + torch.where(h > 0, h, torch.zeros_like(h))
        act = h * torch.tanh(sp)
        return torch.einsum("bsf,fd->bsd", act, w2)

    x = A_u
    xn = _layernorm(x, axis=-1)
    return x + _A_ref(xn, A_log_A_real, A_A_imag, A_C_real, A_C_imag, A_log_dt, A_D_skip) + _B_ref(xn, B_w1, B_b1, B_w2)
```

```cpp
#include <hip/hip_runtime.h>
#include <hip/hip_bf16.h>
#include <stdint.h>

namespace {

constexpr int L_ = 2048, D_ = 640, NS_ = 128, F_ = 1728;

typedef __attribute__((ext_vector_type(8))) short bf16x8;
typedef __attribute__((ext_vector_type(4))) float f32x4;
typedef __attribute__((ext_vector_type(16))) float f32x16;
typedef __attribute__((ext_vector_type(2))) float f32x2;
typedef __attribute__((ext_vector_type(2))) __bf16 bf16x2;
typedef unsigned short u16;

constexpr float LOG2E = 1.4426950408889634f;
constexpr float LN2 = 0.6931471805599453f;
constexpr float INV2PI = 0.15915494309189535f;

__device__ __forceinline__ float bf2f(u16 v) { return __uint_as_float(((uint32_t)v) << 16); }
__device__ __forceinline__ float bflo(uint32_t v) { return __uint_as_float(v << 16); }
__device__ __forceinline__ float bfhi(uint32_t v) { return __uint_as_float(v & 0xffff0000u); }
__device__ __forceinline__ u16 f2bf(float x) {
  __bf16 h = (__bf16)x;
  return __builtin_bit_cast(u16, h);
}
__device__ __forceinline__ float bfr(float x) { return bf2f(f2bf(x)); }
__device__ __forceinline__ uint32_t pack2(float a, float b) {
  f32x2 v = {a, b};
  bf16x2 r = __builtin_convertvector(v, bf16x2);
  return __builtin_bit_cast(uint32_t, r);
}

__device__ __forceinline__ float mish_bf(float h) {
  float e = bfr(__builtin_amdgcn_exp2f(-fabsf(h) * LOG2E));
  float s = e * (1.f - e * (0.5f - e * (1.f / 3.f)));
  float lg = __builtin_amdgcn_logf(1.f + e) * LN2;
  float l = bfr(e < 0.015625f ? s : lg);
  float sp = bfr(l + fmaxf(h, 0.f));
  float q = __builtin_amdgcn_exp2f(sp * (2.f * LOG2E));
  float t = 1.f - 2.f * __builtin_amdgcn_rcpf(q + 1.f);
  float ts = sp * (1.f - sp * sp * (1.f / 3.f));
  t = bfr(sp < 0.03125f ? ts : t);
  return h * t;
}

__device__ __forceinline__ float wave_sum(float v) {
#pragma unroll
  for (int o = 32; o > 0; o >>= 1) v += __shfl_xor(v, o, 64);
  return v;
}

constexpr int K1_KGEN = D_;
constexpr int K1_LN = L_ / 16;
constexpr int K1_T1 = (D_ / 64) * (F_ / 64);
constexpr int K1_BLOCKS = K1_KGEN + K1_LN + 2 * K1_T1;

__global__ __launch_bounds__(256) void k1_prep(
    const u16* __restrict__ x, const u16* __restrict__ logAr, const u16* __restrict__ Aim,
    const u16* __restrict__ Cre, const u16* __restrict__ Cim, const u16* __restrict__ logdt,
    const u16* __restrict__ w1, const u16* __restrict__ w2,
    u16* __restrict__ xn, u16* __restrict__ xnT, u16* __restrict__ Kbuf,
    u16* __restrict__ w1T, u16* __restrict__ w2T) {
  __shared__ __attribute__((aligned(16))) char smem[16 * 1296];
  const int tid = threadIdx.x, lane = tid & 63, wave = tid >> 6;
  int b = blockIdx.x;
  if (b < K1_KGEN) {
    const int d = b;
    float4* prm = (float4*)smem;
    float* part = (float*)(smem + 2048);
    if (tid < NS_) {
      float dt = bfr(expf(bf2f(logdt[d])));
      float ar = -bfr(expf(bf2f(logAr[d * NS_ + tid]))) * dt;
      float ai = bf2f(Aim[d * NS_ + tid]) * dt;
      float cr = bf2f(Cre[d * NS_ + tid]) * dt;
      float ci = bf2f(Cim[d * NS_ + tid]) * dt;
      prm[tid] = make_float4(ar * LOG2E, ai * INV2PI, 2.f * cr, 2.f * ci);
    }
    __syncthreads();
    const int r = lane & 15, kq = lane >> 4;
    float p0 = (float)r;
    float p1 = (r < 8) ? (float)(256 + 2 * r) : (float)(512 + 4 * ((r - 8) & 3) + 256 * ((r - 8) >> 2));
    float p2 = (r < 8) ? (float)(1024 + 8 * (r & 1) + 256 * (r >> 1)) : 2048.f;
    float pb = (float)(16 * r);
    f32x4 acc0 = {0.f, 0.f, 0.f, 0.f}, acc1 = acc0, acc2 = acc0;
#pragma unroll 2
    for (int g = 0; g < 8; ++g) {
      const float4 P = prm[wave * 32 + g * 4 + kq];
      float mb = __builtin_amdgcn_exp2f(P.x * pb);
      float rb = __builtin_amdgcn_fractf(P.y * pb);
      float br = mb * __builtin_amdgcn_cosf(rb);
      float bi = -mb * __builtin_amdgcn_sinf(rb);
      {
        float m = __builtin_amdgcn_exp2f(P.x * p0);
        float rv = __builtin_amdgcn_fractf(P.y * p0);
        float er = m * __builtin_amdgcn_cosf(rv), ei = m * __builtin_amdgcn_sinf(rv);
        float cr = P.z * er - P.w * ei, ci = P.z * ei + P.w * er;
        acc0 = __builtin_amdgcn_mfma_f32_16x16x4f32(cr, br, acc0, 0, 0, 0);
        acc0 = __builtin_amdgcn_mfma_f32_16x16x4f32(ci, bi, acc0, 0, 0, 0);
      }
      {
        float m = __builtin_amdgcn_exp2f(P.x * p1);
        float rv = __builtin_amdgcn_fractf(P.y * p1);
        float er = m * __builtin_amdgcn_cosf(rv), ei = m * __builtin_amdgcn_sinf(rv);
        float cr = P.z * er - P.w * ei, ci = P.z * ei + P.w * er;
        acc1 = __builtin_amdgcn_mfma_f32_16x16x4f32(cr, br, acc1, 0, 0, 0);
        acc1 = __builtin_amdgcn_mfma_f32_16x16x4f32(ci, bi, acc1, 0, 0, 0);
      }
      {
        float m = __builtin_amdgcn_exp2f(P.x * p2);
        float rv = __builtin_amdgcn_fractf(P.y * p2);
        float er = m * __builtin_amdgcn_cosf(rv), ei = m * __builtin_amdgcn_sinf(rv);
        float cr = P.z * er - P.w * ei, ci = P.z * ei + P.w * er;
        acc2 = __builtin_amdgcn_mfma_f32_16x16x4f32(cr, br, acc2, 0, 0, 0);
        acc2 = __builtin_amdgcn_mfma_f32_16x16x4f32(ci, bi, acc2, 0, 0, 0);
      }
    }
#pragma unroll
    for (int i = 0; i < 4; ++i) {
      part[((wave * 3 + 0) * 16 + kq * 4 + i) * 16 + r] = acc0[i];
      part[((wave * 3 + 1) * 16 + kq * 4 + i) * 16 + r] = acc1[i];
      part[((wave * 3 + 2) * 16 + kq * 4 + i) * 16 + r] = acc2[i];
    }
    __syncthreads();
    float kv[8];
#pragma unroll
    for (int jj = 0; jj < 8; ++jj) {
      int j = tid * 8 + jj;
      int p;
      if (j < 256) {
        p = j;
      } else {
        int s = (j < 512) ? 1 : (j < 1024) ? 2 : 3;
        int q = j >> s, rem = j & ((1 << s) - 1), half = 1 << (s - 1);
        q += (rem > half || (rem == half && (q & 1))) ? 1 : 0;
        p = q << s;
      }
      int t, row, col;
      if (p < 256) { t = 0; row = p & 15; col = p >> 4; }
      else if (p < 512) { int m = (p - 256) >> 1; t = 1; row = m & 7; col = m >> 3; }
      else if (p < 1024) { int m = (p - 512) >> 2; int j2 = m >> 2; t = 1; row = 8 + (j2 >> 4) * 4 + (m & 3); col = j2 & 15; }
      else if (p < 2048) { int m = (p - 1024) >> 3; int j2 = m >> 1; t = 2; row = (j2 >> 4) * 2 + (m & 1); col = j2 & 15; }
      else { t = 2; row = 8; col = 0; }
      int o = (t * 16 + row) * 16 + col;
      kv[jj] = (part[o] + part[o + 768]) + (part[o + 1536] + part[o + 2304]);
    }
    uint32_t hi[4], lo[4];
#pragma unroll
    for (int i = 0; i < 4; ++i) {
      float a0 = kv[7 - 2 * i], a1 = kv[6 - 2 * i];
      uint32_t h = pack2(a0, a1);
      hi[i] = h;
      lo[i] = pack2(a0 - bflo(h), a1 - bfhi(h));
    }
    u16* dst = Kbuf + (size_t)d * 4096 + (tid >> 2) * 64 + (tid & 3) * 8;
    *(uint4*)dst = make_uint4(hi[0], hi[1], hi[2], hi[3]);
    *(uint4*)(dst + 32) = make_uint4(lo[0], lo[1], lo[2], lo[3]);
    return;
  }
  b -= K1_KGEN;
  if (b < K1_LN) {
    const int l0 = b * 16;
    uint32_t* tile = (uint32_t*)smem;
#pragma unroll
    for (int rr = 0; rr < 4; ++rr) {
      const int row = wave * 4 + rr;
      const uint32_t* xr = (const uint32_t*)(x + (size_t)(l0 + row) * D_);
      uint32_t v[5];
#pragma unroll
      for (int i = 0; i < 5; ++i) v[i] = xr[lane + 64 * i];
      float s = 0.f;
#pragma unroll
      for (int i = 0; i < 5; ++i) s += bflo(v[i]) + bfhi(v[i]);
      s = wave_sum(s);
      const float mean = s * (1.f / D_);
      float q = 0.f;
#pragma unroll
      for (int i = 0; i < 5; ++i) {
        float a = bflo(v[i]) - mean, c = bfhi(v[i]) - mean;
        q += a * a + c * c;
      }
      q = wave_sum(q);
      const float mb = bfr(mean);
      const float vb = bfr(q * (1.f / D_));
      const float sd = bfr(sqrtf(bfr(vb + 1e-5f)));
      uint32_t* xo = (uint32_t*)(xn + (size_t)(l0 + row) * D_);
#pragma unroll
      for (int i = 0; i < 5; ++i) {
        float a = bfr(bflo(v[i]) - mb) / sd, c = bfr(bfhi(v[i]) - mb) / sd;
        uint32_t o = pack2(a, c);
        xo[lane + 64 * i] = o;
        tile[row * 324 + lane + 64 * i] = o;
      }
    }
    __syncthreads();
    const u16* t16 = (const u16*)smem;
    for (int d = tid; d < D_; d += 256) {
      uint32_t o[8];
#pragma unroll
      for (int i = 0; i < 8; ++i)
        o[i] = (uint32_t)t16[(2 * i) * 648 + d] | ((uint32_t)t16[(2 * i + 1) * 648 + d] << 16);
      uint4* dst = (uint4*)(xnT + (size_t)d * L_ + l0);
      dst[0] = make_uint4(o[0], o[1], o[2], o[3]);
      dst[1] = make_uint4(o[4], o[5], o[6], o[7]);
    }
    return;
  }
  b -= K1_LN;
  {
    const u16* src; u16* dst; int R, C;
    if (b < K1_T1) { src = w1; dst = w1T; R = D_; C = F_; }
    else { b -= K1_T1; src = w2; dst = w2T; R = F_; C = D_; }
    const int tc = C / 64;
    const int r0 = (b / tc) * 64, c0 = (b % tc) * 64;
    u16* t16 = (u16*)smem;
#pragma unroll
    for (int i = 0; i < 2; ++i) {
      int rr = (tid >> 3) + 32 * i, ch = tid & 7;
      uint4 v = *(const uint4*)(src + (size_t)(r0 + rr) * C + c0 + ch * 8);
      *(uint4*)(t16 + rr * 72 + ch * 8) = v;
    }
    __syncthreads();
#pragma unroll
    for (int i = 0; i < 2; ++i) {
      int cc = (tid >> 3) + 32 * i, ch = tid & 7;
      uint32_t o[4];
#pragma unroll
      for (int k = 0; k < 4; ++k)
        o[k] = (uint32_t)t16[(ch * 8 + 2 * k) * 72 + cc] | ((uint32_t)t16[(ch * 8 + 2 * k + 1) * 72 + cc] << 16);
      *(uint4*)(dst + (size_t)(c0 + cc) * R + r0 + ch * 8) = make_uint4(o[0], o[1], o[2], o[3]);
    }
  }
}

constexpr int K2_BLOCKS = 216 * 5;
constexpr int CONV_W_BYTES = 2080 * 16;
constexpr int CONV_K_OFF = CONV_W_BYTES;
constexpr int K2_LDS = 49152;

__device__ __forceinline__ void conv_seg(f32x16& acc, const char* kb, const char* wb, int d0, int d1) {
#pragma unroll 4
  for (int dl = d0; dl < d1; ++dl) {
#pragma unroll
    for (int q = 0; q < 2; ++q) {
      bf16x8 ah = *(const bf16x8*)(kb - dl * 144 + q * 32);
      bf16x8 al = *(const bf16x8*)(kb - dl * 144 + q * 32 + 64);
      bf16x8 bb = *(const bf16x8*)(wb + (2 * dl - q) * 256);
      acc = __builtin_amdgcn_mfma_f32_32x32x16_bf16(ah, bb, acc, 0, 0, 0);
      acc = __builtin_amdgcn_mfma_f32_32x32x16_bf16(al, bb, acc, 0, 0, 0);
    }
  }
}

__global__ __launch_bounds__(256, 3) void k2_conv_gemm1(
    const u16* __restrict__ xn, const u16* __restrict__ xnT, const u16* __restrict__ Kbuf,
    const u16* __restrict__ Dskip, const u16* __restrict__ w1T, const u16* __restrict__ b1,
    u16* __restrict__ act, u16* __restrict__ AoutT) {
  __shared__ __attribute__((aligned(16))) char smem[K2_LDS];
  const int tid = threadIdx.x, lane = tid & 63;
  const int wave = __builtin_amdgcn_readfirstlane(tid >> 6);
  const int grp = blockIdx.x / 5, pos = blockIdx.x % 5;
  if (pos < 3) {
    const int d = grp * 3 + pos;
    if (d >= D_) return;
    const uint32_t* urow = (const uint32_t*)(xnT + (size_t)d * L_);
    for (int idx = tid; idx < 2080; idx += 256) {
      const int e = idx - 32;
      uint4 w;
      if (e >= 0 && e <= 2040) {
        const int dw = e >> 1;
        uint32_t v0 = urow[dw], v1 = urow[dw + 1], v2 = urow[dw + 2], v3 = urow[dw + 3], v4 = urow[dw + 4];
        const uint32_t sh = (e & 1) * 16;
        w.x = __builtin_amdgcn_alignbit(v1, v0, sh);
        w.y = __builtin_amdgcn_alignbit(v2, v1, sh);
        w.z = __builtin_amdgcn_alignbit(v3, v2, sh);
        w.w = __builtin_amdgcn_alignbit(v4, v3, sh);
      } else {
        const u16* u16row = (const u16*)urow;
        uint32_t t[8];
#pragma unroll
        for (int j = 0; j < 8; ++j) {
          int ii = e + j;
          t[j] = (ii >= 0 && ii < L_) ? (uint32_t)u16row[ii] : 0u;
        }
        w = make_uint4(t[0] | (t[1] << 16), t[2] | (t[3] << 16), t[4] | (t[5] << 16), t[6] | (t[7] << 16));
      }
      *(uint4*)(smem + idx * 16) = w;
    }
    {
      char* kl = smem + CONV_K_OFF;
      const uint4* ks = (const uint4*)(Kbuf + (size_t)d * 4096);
#pragma unroll
      for (int i = 0; i < 2; ++i) {
        int c = tid + 256 * i;
        *(uint4*)(kl + (32 + (c >> 3)) * 144 + (c & 7) * 16) = ks[c];
      }
      *(uint4*)(kl + (tid >> 3) * 144 + (tid & 7) * 16) = make_uint4(0, 0, 0, 0);
    }
    __syncthreads();
    const int c = lane & 31, h = lane >> 5;
    const char* wb = smem + (c + 25 - 8 * h) * 16;
    const char* kb1 = smem + CONV_K_OFF + (32 + c + 32) * 144 + h * 16;
    const char* kb0 = smem + CONV_K_OFF + (c + 32) * 144 + h * 16;
    f32x16 accA = {}, accB = {};
    if (wave == 0) conv_seg(accA, kb1, wb, 0, 24);
    else if (wave == 1) conv_seg(accA, kb1, wb, 24, 48);
    else if (wave == 2) { conv_seg(accA, kb1, wb, 48, 64); conv_seg(accB, kb0, wb, 0, 8); }
    else conv_seg(accA, kb0, wb, 8, 32);
    __syncthreads();
    float* P = (float*)smem;
    {
      float* pa = P + (wave == 3 ? 4 : wave) * 1024;
#pragma unroll
      for (int i = 0; i < 16; ++i) pa[((i & 3) + 8 * (i >> 2) + 4 * h) * 32 + c] = accA[i];
      if (wave == 2) {
        float* pb = P + 3 * 1024;
#pragma unroll
        for (int i = 0; i < 16; ++i) pb[((i & 3) + 8 * (i >> 2) + 4 * h) * 32 + c] = accB[i];
      }
    }
    __syncthreads();
    {
      const int a = tid >> 2, b0 = (tid & 3) * 8;
      const int o = (a & 31) * 32 + b0;
      float y[8];
      if (a >= 32) {
        const f32x4* p0 = (const f32x4*)(P + o);
        const f32x4* p1 = (const f32x4*)(P + 1024 + o);
        const f32x4* p2 = (const f32x4*)(P + 2048 + o);
#pragma unroll
        for (int i = 0; i < 2; ++i) {
          f32x4 s = p0[i] + p1[i] + p2[i];
          y[4 * i] = s[0]; y[4 * i + 1] = s[1]; y[4 * i + 2] = s[2]; y[4 * i + 3] = s[3];
        }
      } else {
        const f32x4* p0 = (const f32x4*)(P + 3072 + o);
        const f32x4* p1 = (const f32x4*)(P + 4096 + o);
#pragma unroll
        for (int i = 0; i < 2; ++i) {
          f32x4 s = p0[i] + p1[i];
          y[4 * i] = s[0]; y[4 * i + 1] = s[1]; y[4 * i + 2] = s[2]; y[4 * i + 3] = s[3];
        }
      }
      const float dsk = bf2f(Dskip[d]);
      const uint4 uv = *(const uint4*)(xnT + (size_t)d * L_ + tid * 8);
      const uint32_t uu[4] = {uv.x, uv.y, uv.z, uv.w};
      uint32_t o4[4];
#pragma unroll
      for (int i = 0; i < 4; ++i) {
        float r0 = bfr(y[2 * i]) + bfr(bflo(uu[i]) * dsk);
        float r1 = bfr(y[2 * i + 1]) + bfr(bfhi(uu[i]) * dsk);
        o4[i] = pack2(r0, r1);
      }
      *(uint4*)(AoutT + (size_t)d * L_ + tid * 8) = make_uint4(o4[0], o4[1], o4[2], o4[3]);
    }
    return;
  }
  const int tile = grp * 2 + (pos - 3);
  const int tm = tile & 15, tn = tile >> 4;
  const int m0 = tm * 128, n0 = tn * 64;
  const u16* gsrc[6];
#pragma unroll
  for (int i = 0; i < 6; ++i) {
    const int ch = wave + 4 * i;
    const int row = ch * 8 + (lane >> 3);
    const int sc = (lane & 7) ^ (row & 7);
    gsrc[i] = (row < 128 ? xn + (size_t)(m0 + row) * D_ : w1T + (size_t)(n0 + row - 128) * D_) + sc * 8;
  }
  auto stage = [&](int kt, int buf) {
#pragma unroll
    for (int i = 0; i < 6; ++i)
      __builtin_amdgcn_global_load_lds((const uint32_t*)(gsrc[i] + kt * 64),
                                       (uint32_t*)(smem + buf * 24576 + (wave + 4 * i) * 1024), 16, 0, 0);
  };
  const int wm = wave & 1, wn = wave >> 1;
  const int fr = lane & 15, fq = lane >> 4;
  f32x4 acc[2][4];
#pragma unroll
  for (int i = 0; i < 2; ++i)
#pragma unroll
    for (int j = 0; j < 4; ++j) acc[i][j] = (f32x4){0.f, 0.f, 0.f, 0.f};
  stage(0, 0);
  asm volatile("s_waitcnt vmcnt(0)" ::: "memory");
  __syncthreads();
  for (int kt = 0; kt < 10; ++kt) {
    const int buf = kt & 1;
    if (kt + 1 < 10) stage(kt + 1, buf ^ 1);
    const char* base = smem + buf * 24576;
#pragma unroll
    for (int ks = 0; ks < 2; ++ks) {
      bf16x8 af[2], bfm[4];
#pragma unroll
      for (int i = 0; i < 2; ++i) {
        const int row = 128 + wn * 32 + i * 16 + fr;
        af[i] = *(const bf16x8*)(base + row * 128 + (((ks * 4 + fq) ^ (row & 7)) * 16));
      }
#pragma unroll
      for (int j = 0; j < 4; ++j) {
        const int row = wm * 64 + j * 16 + fr;
        bfm[j] = *(const bf16x8*)(base + row * 128 + (((ks * 4 + fq) ^ (row & 7)) * 16));
      }
#pragma unroll
      for (int i = 0; i < 2; ++i)
#pragma unroll
        for (int j = 0; j < 4; ++j)
          acc[i][j] = __builtin_amdgcn_mfma_f32_16x16x32_bf16(af[i], bfm[j], acc[i][j], 0, 0, 0);
    }
    asm volatile("s_waitcnt vmcnt(0)" ::: "memory");
    __syncthreads();
  }
#pragma unroll
  for (int i = 0; i < 2; ++i) {
    const int n = n0 + wn * 32 + i * 16 + fq * 4;
    const uint2 bb = *(const uint2*)(b1 + n);
    const float bv[4] = {bflo(bb.x), bfhi(bb.x), bflo(bb.y), bfhi(bb.y)};
#pragma unroll
    for (int j = 0; j < 4; ++j) {
      const int m = m0 + wm * 64 + j * 16 + fr;
      float r[4];
#pragma unroll
      for (int k = 0; k < 4; ++k) r[k] = mish_bf(bfr(bfr(acc[i][j][k]) + bv[k]));
      *(uint2*)(act + (size_t)m * F_ + n) = make_uint2(pack2(r[0], r[1]), pack2(r[2], r[3]));
    }
  }
}

constexpr int K3_STAGE = 144 * 128;
constexpr int K3_NBUF = 4;
constexpr int K3_AO_OFF = K3_STAGE * K3_NBUF;
constexpr int K3_LDS = K3_AO_OFF + 80 * 128;

__global__ __launch_bounds__(256) void k3_gemm2(
    const u16* __restrict__ act, const u16* __restrict__ w2T, const u16* __restrict__ x,
    const u16* __restrict__ AoutT, u16* __restrict__ out) {
  __shared__ __attribute__((aligned(16))) char smem[K3_LDS];
  const int tid = threadIdx.x, lane = tid & 63;
  const int wave = __builtin_amdgcn_readfirstlane(tid >> 6);
  const int bid = blockIdx.x;
  const int rb = (bid & 7) * 4 + ((bid >> 3) & 3), nt = bid >> 5;
  const int m0 = rb * 64, n0 = nt * 80;
  {
#pragma unroll
    for (int i = 0; i < 3; ++i) {
      const int ch = wave + 4 * i;
      if (ch < 10) {
        const int row = ch * 8 + (lane >> 3);
        __builtin_amdgcn_global_load_lds((const uint32_t*)(AoutT + (size_t)(n0 + row) * L_ + m0 + (lane & 7) * 8),
                                         (uint32_t*)(smem + K3_AO_OFF + ch * 1024), 16, 0, 0);
      }
    }
  }
  const u16* gsrc[5];
#pragma unroll
  for (int i = 0; i < 5; ++i) {
    int ch = wave + 4 * i;
    if (ch > 17) ch = 17;
    const int row = ch * 8 + (lane >> 3);
    const int sc = (lane & 7) ^ (row & 7);
    gsrc[i] = (row < 64 ? act + (size_t)(m0 + row) * F_ : w2T + (size_t)(n0 + row - 64) * F_) + sc * 8;
  }
  auto stage = [&](int kt, int buf) {
#pragma unroll
    for (int i = 0; i < 4; ++i)
      __builtin_amdgcn_global_load_lds((const uint32_t*)(gsrc[i] + kt * 64),
                                       (uint32_t*)(smem + buf * K3_STAGE + (wave + 4 * i) * 1024), 16, 0, 0);
    if (wave < 2)
      __builtin_amdgcn_global_load_lds((const uint32_t*)(gsrc[4] + kt * 64),
                                       (uint32_t*)(smem + buf * K3_STAGE + (wave + 16) * 1024), 16, 0, 0);
  };
  const int wm = wave & 1, ksp = wave >> 1;
  const int fr = lane & 15, fq = lane >> 4;
  f32x4 acc[5][2];
#pragma unroll
  for (int i = 0; i < 5; ++i)
#pragma unroll
    for (int j = 0; j < 2; ++j) acc[i][j] = (f32x4){0.f, 0.f, 0.f, 0.f};
  constexpr int NT = F_ / 64;
  stage(0, 0); stage(1, 1); stage(2, 2);
  for (int kt = 0; kt < NT; ++kt) {
    const int rem = NT - 1 - kt;
    if (rem >= 2) {
      if (wave < 2) asm volatile("s_waitcnt vmcnt(10)" ::: "memory");
      else asm volatile("s_waitcnt vmcnt(8)" ::: "memory");
    } else if (rem == 1) {
      if (wave < 2) asm volatile("s_waitcnt vmcnt(5)" ::: "memory");
      else asm volatile("s_waitcnt vmcnt(4)" ::: "memory");
    } else {
      asm volatile("s_waitcnt vmcnt(0)" ::: "memory");
    }
    asm volatile("s_waitcnt lgkmcnt(0)" ::: "memory");
    __builtin_amdgcn_s_barrier();
    if (kt + 3 < NT) stage(kt + 3, (kt + 3) & 3);
    const char* base = smem + (kt & 3) * K3_STAGE;
    bf16x8 af[5], bfm[2];
#pragma unroll
    for (int i = 0; i < 5; ++i) {
      const int row = 64 + i * 16 + fr;
      af[i] = *(const bf16x8*)(base + row * 128 + (((ksp * 4 + fq) ^ (row & 7)) * 16));
    }
#pragma unroll
    for (int j = 0; j < 2; ++j) {
      const int row = wm * 32 + j * 16 + fr;
      bfm[j] = *(const bf16x8*)(base + row * 128 + (((ksp * 4 + fq) ^ (row & 7)) * 16));
    }
#pragma unroll
    for (int i = 0; i < 5; ++i)
#pragma unroll
      for (int j = 0; j < 2; ++j)
        acc[i][j] = __builtin_amdgcn_mfma_f32_16x16x32_bf16(af[i], bfm[j], acc[i][j], 0, 0, 0);
  }
  __syncthreads();
  float* red = (float*)smem;
#pragma unroll
  for (int i = 0; i < 5; ++i)
#pragma unroll
    for (int j = 0; j < 2; ++j) {
      const int m = wm * 32 + j * 16 + fr, n = i * 16 + fq * 4;
      *(f32x4*)(red + (ksp * 64 + m) * 84 + n) = acc[i][j];
    }
  __syncthreads();
  const u16* ao = (const u16*)(smem + K3_AO_OFF);
  for (int it = tid; it < 640; it += 256) {
    const int m = it / 10, c8 = (it % 10) * 8;
    const f32x4* r0 = (const f32x4*)(red + m * 84 + c8);
    const f32x4* r1 = (const f32x4*)(red + (64 + m) * 84 + c8);
    const f32x4 s0 = r0[0] + r1[0], s1 = r0[1] + r1[1];
    const float s[8] = {s0[0], s0[1], s0[2], s0[3], s1[0], s1[1], s1[2], s1[3]};
    const uint4 xv = *(const uint4*)(x + (size_t)(m0 + m) * D_ + n0 + c8);
    const uint32_t xx[4] = {xv.x, xv.y, xv.z, xv.w};
    uint32_t o[4];
#pragma unroll
    for (int i = 0; i < 4; ++i) {
      float a0 = bf2f(ao[(c8 + 2 * i) * 64 + m]), a1 = bf2f(ao[(c8 + 2 * i + 1) * 64 + m]);
      float v0 = bfr(bflo(xx[i]) + a0) + bfr(s[2 * i]);
      float v1 = bfr(bfhi(xx[i]) + a1) + bfr(s[2 * i + 1]);
      o[i] = pack2(v0, v1);
    }
    *(uint4*)(out + (size_t)(m0 + m) * D_ + n0 + c8) = make_uint4(o[0], o[1], o[2], o[3]);
  }
}

}

extern "C" void kernel_launch(void* const* d_in, const int* in_sizes, int n_in,
                              void* d_out, int out_size, void* d_ws, size_t ws_size,
                              hipStream_t stream) {
  const u16* x = (const u16*)d_in[0];
  const u16* logAr = (const u16*)d_in[1];
  const u16* Aim = (const u16*)d_in[2];
  const u16* Cre = (const u16*)d_in[3];
  const u16* Cim = (const u16*)d_in[4];
  const u16* logdt = (const u16*)d_in[5];
  const u16* Dskip = (const u16*)d_in[6];
  const u16* w1 = (const u16*)d_in[8];
  const u16* b1 = (const u16*)d_in[9];
  const u16* w2 = (const u16*)d_in[10];
  u16* out = (u16*)d_out;
  u16* ws = (u16*)d_ws;
  u16* xn = ws;
  u16* xnT = xn + (size_t)L_ * D_;
  u16* Kbuf = xnT + (size_t)L_ * D_;
  u16* w1T = Kbuf + (size_t)D_ * 4096;
  u16* w2T = w1T + (size_t)D_ * F_;
  u16* act = w2T + (size_t)D_ * F_;
  u16* AoutT = act + (size_t)L_ * F_;
  k1_prep<<<K1_BLOCKS, 256, 0, stream>>>(x, logAr, Aim, Cre, Cim, logdt, w1, w2, xn, xnT, Kbuf, w1T, w2T);
  k2_conv_gemm1<<<K2_BLOCKS, 256, 0, stream>>>(xn, xnT, Kbuf, Dskip, w1T, b1, act, AoutT);
  k3_gemm2<<<256, 256, 0, stream>>>(act, w2T, x, AoutT, out);
}
```

```cpp
#include <hip/hip_runtime.h>
#include <hip/hip_bf16.h>
#include <stdint.h>

#define TS_BEGIN(k)
#define TS_END(k)

namespace {

constexpr int L_ = 2048, D_ = 640, NS_ = 128, F_ = 1728;

typedef __attribute__((ext_vector_type(8))) short bf16x8;
typedef __attribute__((ext_vector_type(4))) float f32x4;
typedef __attribute__((ext_vector_type(16))) float f32x16;
typedef __attribute__((ext_vector_type(2))) float f32x2;
typedef __attribute__((ext_vector_type(2))) __bf16 bf16x2;
typedef unsigned short u16;

constexpr float LOG2E = 1.4426950408889634f;
constexpr float LN2 = 0.6931471805599453f;
constexpr float INV2PI = 0.15915494309189535f;

__device__ __forceinline__ float bf2f(u16 v) { return __uint_as_float(((uint32_t)v) << 16); }
__device__ __forceinline__ float bflo(uint32_t v) { return __uint_as_float(v << 16); }
__device__ __forceinline__ float bfhi(uint32_t v) { return __uint_as_float(v & 0xffff0000u); }
__device__ __forceinline__ u16 f2bf(float x) {
  __bf16 h = (__bf16)x;
  return __builtin_bit_cast(u16, h);
}
__device__ __forceinline__ float bfr(float x) { return bf2f(f2bf(x)); }
__device__ __forceinline__ uint32_t pack2(float a, float b) {
  f32x2 v = {a, b};
  bf16x2 r = __builtin_convertvector(v, bf16x2);
  return __builtin_bit_cast(uint32_t, r);
}

__device__ __forceinline__ float tanh_sp_bf(float h) {
  float e = bfr(__builtin_amdgcn_exp2f(-fabsf(h) * LOG2E));
  float s = e * (1.f - e * (0.5f - e * (1.f / 3.f)));
  float lg = __builtin_amdgcn_logf(1.f + e) * LN2;
  float l = bfr(e < 0.015625f ? s : lg);
  float sp = bfr(l + fmaxf(h, 0.f));
  float q = __builtin_amdgcn_exp2f(sp * (2.f * LOG2E));
  float t = 1.f - 2.f * __builtin_amdgcn_rcpf(q + 1.f);
  float ts = sp * (1.f - sp * sp * (1.f / 3.f));
  return bfr(sp < 0.03125f ? ts : t);
}
constexpr int TB_LO = 117 << 7, TB_HI = 134 << 7;
constexpr int TB_N = TB_HI - TB_LO + 1;
constexpr int TB_STRIDE = 2304;
constexpr int TB_BYTES = 2 * TB_STRIDE * 2;

template <int CTRL>
__device__ __forceinline__ float dppf(float v) {
  return __builtin_bit_cast(float, __builtin_amdgcn_update_dpp(0, __builtin_bit_cast(int, v), CTRL, 0xF, 0xF, true));
}
__device__ __forceinline__ float rdl(float v, int l) {
  return __builtin_bit_cast(float, __builtin_amdgcn_readlane(__builtin_bit_cast(int, v), l));
}
__device__ __forceinline__ float wave_sum(float v) {
  v += dppf<0xB1>(v);
  v += dppf<0x4E>(v);
  v += dppf<0x141>(v);
  v += dppf<0x140>(v);
  return (rdl(v, 0) + rdl(v, 16)) + (rdl(v, 32) + rdl(v, 48));
}

constexpr int K1_KGEN = D_;
constexpr int K1_LN = L_ / 16;
constexpr int K1_T1 = (D_ / 64) * (F_ / 64);
constexpr int K1_BLOCKS = K1_KGEN + K1_LN + 2 * K1_T1 + 1;

__device__ __forceinline__ void k1_body(
    const u16* __restrict__ x, const u16* __restrict__ logAr, const u16* __restrict__ Aim,
    const u16* __restrict__ Cre, const u16* __restrict__ Cim, const u16* __restrict__ logdt,
    const u16* __restrict__ w1, const u16* __restrict__ w2,
    u16* __restrict__ xn, u16* __restrict__ xnT, u16* __restrict__ Kbuf,
    u16* __restrict__ w1T, u16* __restrict__ w2T, u16* __restrict__ tbl) {
  __shared__ __attribute__((aligned(16))) char smem[16 * 1296];
  const int tid = threadIdx.x, lane = tid & 63, wave = tid >> 6;
  int b = blockIdx.x;
  if (b < K1_KGEN) {
    const int d = b;
    float4* prm = (float4*)smem;
    float* part = (float*)(smem + 2048);
    float* kp = (float*)(smem + 2048 + 12288);
    if (tid < NS_) {
      float dt = bfr(expf(bf2f(logdt[d])));
      float ar = -bfr(expf(bf2f(logAr[d * NS_ + tid]))) * dt;
      float ai = bf2f(Aim[d * NS_ + tid]) * dt;
      float cr = bf2f(Cre[d * NS_ + tid]) * dt;
      float ci = bf2f(Cim[d * NS_ + tid]) * dt;
      prm[tid] = make_float4(ar * LOG2E, ai * INV2PI, 2.f * cr, 2.f * ci);
    }
    __syncthreads();
    const int r = lane & 15, kq = lane >> 4;
    float p0 = (float)r;
    float p1 = (r < 8) ? (float)(256 + 2 * r) : (float)(512 + 4 * ((r - 8) & 3) + 256 * ((r - 8) >> 2));
    float p2 = (r < 8) ? (float)(1024 + 8 * (r & 1) + 256 * (r >> 1)) : 2048.f;
    float pb = (float)(16 * r);
    f32x4 acc0 = {0.f, 0.f, 0.f, 0.f}, acc1 = acc0, acc2 = acc0;
#pragma unroll 2
    for (int g = 0; g < 8; ++g) {
      const float4 P = prm[wave * 32 + g * 4 + kq];
      float mb = __builtin_amdgcn_exp2f(P.x * pb);
      float rb = __builtin_amdgcn_fractf(P.y * pb);
      float fr_ = mb * __builtin_amdgcn_cosf(rb), fi_ = mb * __builtin_amdgcn_sinf(rb);
      float br = P.z * fr_ - P.w * fi_;
      float bi = -(P.z * fi_ + P.w * fr_);
      {
        float m = __builtin_amdgcn_exp2f(P.x * p0);
        float rv = __builtin_amdgcn_fractf(P.y * p0);
        float er = m * __builtin_amdgcn_cosf(rv), ei = m * __builtin_amdgcn_sinf(rv);
        acc0 = __builtin_amdgcn_mfma_f32_16x16x4f32(er, br, acc0, 0, 0, 0);
        acc0 = __builtin_amdgcn_mfma_f32_16x16x4f32(ei, bi, acc0, 0, 0, 0);
      }
      {
        float m = __builtin_amdgcn_exp2f(P.x * p1);
        float rv = __builtin_amdgcn_fractf(P.y * p1);
        float er = m * __builtin_amdgcn_cosf(rv), ei = m * __builtin_amdgcn_sinf(rv);
        acc1 = __builtin_amdgcn_mfma_f32_16x16x4f32(er, br, acc1, 0, 0, 0);
        acc1 = __builtin_amdgcn_mfma_f32_16x16x4f32(ei, bi, acc1, 0, 0, 0);
      }
      {
        float m = __builtin_amdgcn_exp2f(P.x * p2);
        float rv = __builtin_amdgcn_fractf(P.y * p2);
        float er = m * __builtin_amdgcn_cosf(rv), ei = m * __builtin_amdgcn_sinf(rv);
        acc2 = __builtin_amdgcn_mfma_f32_16x16x4f32(er, br, acc2, 0, 0, 0);
        acc2 = __builtin_amdgcn_mfma_f32_16x16x4f32(ei, bi, acc2, 0, 0, 0);
      }
    }
#pragma unroll
    for (int i = 0; i < 4; ++i) {
      part[((wave * 3 + 0) * 16 + kq * 4 + i) * 16 + r] = acc0[i];
      part[((wave * 3 + 1) * 16 + kq * 4 + i) * 16 + r] = acc1[i];
      part[((wave * 3 + 2) * 16 + kq * 4 + i) * 16 + r] = acc2[i];
    }
    __syncthreads();
    {
      int o;
      o = (tid & 15) * 16 + (tid >> 4);
      kp[tid] = (part[o] + part[o + 768]) + (part[o + 1536] + part[o + 2304]);
      if (tid < 128) { o = (16 + (tid & 7)) * 16 + (tid >> 3); }
      else { int m = tid - 128, j2 = m >> 2; o = (16 + 8 + (j2 >> 4) * 4 + (m & 3)) * 16 + (j2 & 15); }
      kp[256 + tid] = (part[o] + part[o + 768]) + (part[o + 1536] + part[o + 2304]);
      if (tid <= 128) {
        int j2 = tid >> 1;
        o = (tid == 128) ? (32 + 8) * 16 : (32 + (j2 >> 4) * 2 + (tid & 1)) * 16 + (j2 & 15);
        kp[512 + tid] = (part[o] + part[o + 768]) + (part[o + 1536] + part[o + 2304]);
      }
    }
    __syncthreads();
    float kv[8];
    {
      const int j0 = tid * 8;
      const int s = (j0 < 256) ? 0 : (j0 < 512) ? 1 : (j0 < 1024) ? 2 : 3;
      const int x0 = s ? j0 - (128 << s) : j0;
      const int bidx = s ? 128 + 128 * s : 0;
      const int hm1 = s ? (1 << (s - 1)) - 1 : 0;
#pragma unroll
      for (int jj = 0; jj < 8; ++jj) {
        const int xx = x0 + jj;
        const int q = s ? ((xx + hm1 + ((xx >> s) & 1)) >> s) : xx;
        kv[jj] = kp[bidx + q];
      }
    }
    uint32_t hi[4], lo[4];
#pragma unroll
    for (int i = 0; i < 4; ++i) {
      float a0 = kv[7 - 2 * i], a1 = kv[6 - 2 * i];
      uint32_t h = pack2(a0, a1);
      hi[i] = h;
      lo[i] = pack2(a0 - bflo(h), a1 - bfhi(h));
    }
    u16* dst = Kbuf + (size_t)d * 4096 + (tid >> 2) * 64 + (tid & 3) * 8;
    *(uint4*)dst = make_uint4(hi[0], hi[1], hi[2], hi[3]);
    *(uint4*)(dst + 32) = make_uint4(lo[0], lo[1], lo[2], lo[3]);
    return;
  }
  b -= K1_KGEN;
  if (b < K1_LN) {
    const int xc = b & 7, kk = b >> 3;
    const int l0 = ((xc * 4 + (kk >> 2)) * 4 + (kk & 3)) * 16;
    uint32_t* tile = (uint32_t*)smem;
    uint32_t v[4][5];
#pragma unroll
    for (int rr = 0; rr < 4; ++rr) {
      const uint32_t* xr = (const uint32_t*)(x + (size_t)(l0 + wave * 4 + rr) * D_);
#pragma unroll
      for (int i = 0; i < 5; ++i) v[rr][i] = xr[lane + 64 * i];
    }
    float mean[4], q[4];
#pragma unroll
    for (int rr = 0; rr < 4; ++rr) {
      float s = 0.f;
#pragma unroll
      for (int i = 0; i < 5; ++i) s += bflo(v[rr][i]) + bfhi(v[rr][i]);
      mean[rr] = wave_sum(s) * (1.f / D_);
    }
#pragma unroll
    for (int rr = 0; rr < 4; ++rr) {
      float s = 0.f;
#pragma unroll
      for (int i = 0; i < 5; ++i) {
        float a = bflo(v[rr][i]) - mean[rr], c = bfhi(v[rr][i]) - mean[rr];
        s += a * a + c * c;
      }
      q[rr] = wave_sum(s);
    }
#pragma unroll
    for (int rr = 0; rr < 4; ++rr) {
      const int row = wave * 4 + rr;
      const float mb = bfr(mean[rr]);
      const float vb = bfr(q[rr] * (1.f / D_));
      const float sd = bfr(sqrtf(bfr(vb + 1e-5f)));
      uint32_t* xo = (uint32_t*)(xn + (size_t)(l0 + row) * D_);
#pragma unroll
      for (int i = 0; i < 5; ++i) {
        float a = bfr(bflo(v[rr][i]) - mb) / sd, c = bfr(bfhi(v[rr][i]) - mb) / sd;
        uint32_t o = pack2(a, c);
        xo[lane + 64 * i] = o;
        tile[row * 324 + lane + 64 * i] = o;
      }
    }
    __syncthreads();
    const u16* t16 = (const u16*)smem;
    for (int d = tid; d < D_; d += 256) {
      uint32_t o[8];
#pragma unroll
      for (int i = 0; i < 8; ++i)
        o[i] = (uint32_t)t16[(2 * i) * 648 + d] | ((uint32_t)t16[(2 * i + 1) * 648 + d] << 16);
      uint4* dst = (uint4*)(xnT + (size_t)d * L_ + l0);
      dst[0] = make_uint4(o[0], o[1], o[2], o[3]);
      dst[1] = make_uint4(o[4], o[5], o[6], o[7]);
    }
    return;
  }
  b -= K1_LN;
  if (b == 2 * K1_T1) {
    for (int i = tid; i < 2 * TB_STRIDE; i += 256) {
      const int sg = i >= TB_STRIDE;
      int m = i - sg * TB_STRIDE;
      if (m > TB_N - 1) m = TB_N - 1;
      const u16 bits = (u16)((TB_LO + m) | (sg << 15));
      tbl[i] = f2bf(tanh_sp_bf(bf2f(bits)));
    }
    return;
  }
  {
    const u16* src; u16* dst; int R, C;
    if (b < K1_T1) { src = w1; dst = w1T; R = D_; C = F_; }
    else { b -= K1_T1; src = w2; dst = w2T; R = F_; C = D_; }
    const int tc = C / 64;
    const int r0 = (b / tc) * 64, c0 = (b % tc) * 64;
    u16* t16 = (u16*)smem;
#pragma unroll
    for (int i = 0; i < 2; ++i) {
      int rr = (tid >> 3) + 32 * i, ch = tid & 7;
      uint4 v = *(const uint4*)(src + (size_t)(r0 + rr) * C + c0 + ch * 8);
      *(uint4*)(t16 + rr * 72 + ch * 8) = v;
    }
    __syncthreads();
#pragma unroll
    for (int i = 0; i < 2; ++i) {
      int cc = (tid >> 3) + 32 * i, ch = tid & 7;
      uint32_t o[4];
#pragma unroll
      for (int k = 0; k < 4; ++k)
        o[k] = (uint32_t)t16[(ch * 8 + 2 * k) * 72 + cc] | ((uint32_t)t16[(ch * 8 + 2 * k + 1) * 72 + cc] << 16);
      *(uint4*)(dst + (size_t)(c0 + cc) * R + r0 + ch * 8) = make_uint4(o[0], o[1], o[2], o[3]);
    }
  }
}

__global__ __launch_bounds__(256) void k1_prep(
    const u16* __restrict__ x, const u16* __restrict__ logAr, const u16* __restrict__ Aim,
    const u16* __restrict__ Cre, const u16* __restrict__ Cim, const u16* __restrict__ logdt,
    const u16* __restrict__ w1, const u16* __restrict__ w2,
    u16* __restrict__ xn, u16* __restrict__ xnT, u16* __restrict__ Kbuf,
    u16* __restrict__ w1T, u16* __restrict__ w2T, u16* __restrict__ tbl) {
  TS_BEGIN(0)
  k1_body(x, logAr, Aim, Cre, Cim, logdt, w1, w2, xn, xnT, Kbuf, w1T, w2T, tbl);
  TS_END(0)
}

constexpr int G1_STAGE = 240 * 128;
constexpr int K2_RING = 0;
constexpr int K2_WIN = 3 * G1_STAGE;
constexpr int K2_KM = K2_WIN + 2080 * 16;
constexpr int K2_U = K2_KM + 96 * 144;
constexpr int K2_USZ = 4224;
constexpr int K2_CNT = K2_U + 2 * K2_USZ;
constexpr int K2_LDS = K2_CNT + 64;

__device__ __forceinline__ void gbar(uint32_t* cnt, uint32_t target, int lane) {
  asm volatile("s_waitcnt lgkmcnt(0)" ::: "memory");
  if (lane == 0) __hip_atomic_fetch_add(cnt, 1u, __ATOMIC_RELAXED, __HIP_MEMORY_SCOPE_WORKGROUP);
  while (true) {
    uint32_t c = __hip_atomic_load(cnt, __ATOMIC_RELAXED, __HIP_MEMORY_SCOPE_WORKGROUP);
    if ((uint32_t)__builtin_amdgcn_readfirstlane(c) >= target) break;
    __builtin_amdgcn_s_sleep(1);
  }
  asm volatile("" ::: "memory");
}

template <int MODE>
__device__ __forceinline__ void conv_mfma(f32x16& acc0, f32x16& acc1, const char* ka, const char* wb) {
#pragma unroll
  for (int i = 0; i < 8; ++i) {
#pragma unroll
    for (int q = 0; q < 2; ++q) {
      const int ko = (7 - i) * 144 + q * 32;
      const int wo = (2 * i - q + 1) * 256;
      bf16x8 a0h = *(const bf16x8*)(ka + ko);
      bf16x8 a0l = *(const bf16x8*)(ka + ko + 64);
      bf16x8 b0 = *(const bf16x8*)(wb + wo);
      if (MODE & 2) {
        bf16x8 a1h = *(const bf16x8*)(ka + ko + 32 * 144);
        bf16x8 a1l = *(const bf16x8*)(ka + ko + 32 * 144 + 64);
        bf16x8 b1 = *(const bf16x8*)(wb + wo + 16384);
        acc1 = __builtin_amdgcn_mfma_f32_32x32x16_bf16(a1h, b0, acc1, 0, 0, 0);
        acc1 = __builtin_amdgcn_mfma_f32_32x32x16_bf16(a1l, b0, acc1, 0, 0, 0);
        acc1 = __builtin_amdgcn_mfma_f32_32x32x16_bf16(a0h, b1, acc1, 0, 0, 0);
        acc1 = __builtin_amdgcn_mfma_f32_32x32x16_bf16(a0l, b1, acc1, 0, 0, 0);
      }
      if (MODE & 1) {
        acc0 = __builtin_amdgcn_mfma_f32_32x32x16_bf16(a0h, b0, acc0, 0, 0, 0);
        acc0 = __builtin_amdgcn_mfma_f32_32x32x16_bf16(a0l, b0, acc0, 0, 0, 0);
      }
    }
  }
}

__device__ __forceinline__ void k2_body(
    const u16* __restrict__ xn, const u16* __restrict__ xnT, const u16* __restrict__ Kbuf,
    const u16* __restrict__ Dskip, const u16* __restrict__ w1T, const u16* __restrict__ b1,
    const u16* __restrict__ tbl, u16* __restrict__ act, u16* __restrict__ AoutT) {
  __shared__ __attribute__((aligned(16))) char smem[K2_LDS];
  const int tid = threadIdx.x, lane = tid & 63;
  const int wave = __builtin_amdgcn_readfirstlane(tid >> 6);
  const int bid = blockIdx.x;
  uint32_t* cnt = (uint32_t*)(smem + K2_CNT);
  if (wave < 4) {
    const int xc = bid & 7, jj = bid >> 3;
    const int tm = (xc & 3) * 4 + (jj & 3), tn = (xc >> 2) * 8 + (jj >> 2);
    const int m0 = tm * 128, n0 = tn * 112;
    const u16* gsrc[8];
#pragma unroll
    for (int i = 0; i < 8; ++i) {
      int ch = wave + 4 * i;
      if (ch > 29) ch = 29;
      const int row = ch * 8 + (lane >> 3);
      if (row < 128) {
        gsrc[i] = xn + (size_t)(m0 + row) * D_ + (((lane & 7) ^ ((row >> 1) & 7)) * 8);
      } else {
        const int r2 = row - 128;
        int n = n0 + r2;
        if (n > F_ - 1) n = F_ - 1;
        const int sw = (r2 < 96) ? (((r2 >> 1) & 1) | (((r2 >> 3) & 3) << 1)) : ((r2 >> 1) & 7);
        gsrc[i] = w1T + (size_t)n * D_ + (((lane & 7) ^ sw) * 8);
      }
    }
    auto stage = [&](int kt, int buf) {
#pragma unroll
      for (int i = 0; i < 7; ++i)
        __builtin_amdgcn_global_load_lds((const uint32_t*)(gsrc[i] + kt * 64),
                                         (uint32_t*)(smem + buf * G1_STAGE + (wave + 4 * i) * 1024), 16, 0, 0);
      if (wave < 2)
        __builtin_amdgcn_global_load_lds((const uint32_t*)(gsrc[7] + kt * 64),
                                         (uint32_t*)(smem + buf * G1_STAGE + (wave + 28) * 1024), 16, 0, 0);
    };
    stage(0, 0);
    stage(1, 1);
    const int fr = lane & 15, fq = lane >> 4;
    uint4 bb[3]; uint2 bb3;
#pragma unroll
    for (int p = 0; p < 3; ++p) {
      int n = n0 + 32 * p + 8 * fq;
      if (n > F_ - 8) n = F_ - 8;
      bb[p] = *(const uint4*)(b1 + n);
    }
    {
      int n = n0 + 96 + 4 * fq;
      if (n > F_ - 4) n = F_ - 4;
      bb3 = *(const uint2*)(b1 + n);
    }
    if (tid < 16) cnt[tid] = 0;
    __syncthreads();
    int aoff[7], boff[2];
#pragma unroll
    for (int i = 0; i < 7; ++i) {
      const int r2 = (i < 6) ? (32 * (i >> 1) + 8 * (fr >> 2) + 4 * (i & 1) + (fr & 3)) : (96 + fr);
      const int sw = (i < 6) ? (((r2 >> 1) & 1) | (((r2 >> 3) & 3) << 1)) : ((r2 >> 1) & 7);
      aoff[i] = (128 + r2) * 128 + ((fq ^ sw) * 16);
    }
#pragma unroll
    for (int j = 0; j < 2; ++j) {
      const int row = wave * 32 + j * 16 + fr;
      boff[j] = row * 128 + ((fq ^ ((row >> 1) & 7)) * 16);
    }
    f32x4 acc[7][2];
#pragma unroll
    for (int i = 0; i < 7; ++i)
#pragma unroll
      for (int j = 0; j < 2; ++j) acc[i][j] = (f32x4){0.f, 0.f, 0.f, 0.f};
    int slot = 0;
    for (int kt = 0; kt < 10; ++kt) {
      if (kt < 9) {
        if (wave < 2) asm volatile("s_waitcnt vmcnt(8)" ::: "memory");
        else asm volatile("s_waitcnt vmcnt(7)" ::: "memory");
      } else {
        asm volatile("s_waitcnt vmcnt(0)" ::: "memory");
      }
      gbar(cnt, 4 * (kt + 1), lane);
      int ns = slot + 2; if (ns >= 3) ns -= 3;
      if (kt < 8) stage(kt + 2, ns);
      else if (kt == 8) {
#pragma unroll
        for (int i = 0; i < 3; ++i) {
          const int ch = wave + 4 * i;
          if (ch < 9)
            __builtin_amdgcn_global_load_lds((const uint32_t*)(tbl + ch * 512 + lane * 8),
                                             (uint32_t*)(smem + ns * G1_STAGE + ch * 1024), 16, 0, 0);
        }
      }
      const char* base = smem + slot * G1_STAGE;
#pragma unroll
      for (int ks = 0; ks < 2; ++ks) {
        bf16x8 af[7], bfm[2];
#pragma unroll
        for (int j = 0; j < 2; ++j) bfm[j] = *(const bf16x8*)(base + (boff[j] ^ (ks * 64)));
#pragma unroll
        for (int i = 0; i < 7; ++i) af[i] = *(const bf16x8*)(base + (aoff[i] ^ (ks * 64)));
#pragma unroll
        for (int i = 0; i < 7; ++i)
#pragma unroll
          for (int j = 0; j < 2; ++j)
            acc[i][j] = __builtin_amdgcn_mfma_f32_16x16x32_bf16(af[i], bfm[j], acc[i][j], 0, 0, 0);
      }
      slot = slot + 1; if (slot >= 3) slot = 0;
    }
    const u16* tb = (const u16*)(smem + 1 * G1_STAGE);
    auto act2 = [&](float a0, float a1, uint32_t bias) -> uint32_t {
      uint32_t r = pack2(a0, a1);
      uint32_t h = pack2(bflo(r) + bflo(bias), bfhi(r) + bfhi(bias));
      uint32_t m0_ = h & 0x7fffu, m1_ = (h >> 16) & 0x7fffu;
      m0_ = min(max(m0_, (uint32_t)TB_LO), (uint32_t)TB_HI) - TB_LO + ((h >> 15) & 1u) * TB_STRIDE;
      m1_ = min(max(m1_, (uint32_t)TB_LO), (uint32_t)TB_HI) - TB_LO + (h >> 31) * TB_STRIDE;
      float t0 = bf2f(tb[m0_]), t1 = bf2f(tb[m1_]);
      return pack2(bflo(h) * t0, bfhi(h) * t1);
    };
#pragma unroll
    for (int j = 0; j < 2; ++j) {
      const int m = m0 + wave * 32 + j * 16 + fr;
      u16* arow = act + (size_t)m * F_;
#pragma unroll
      for (int p = 0; p < 3; ++p) {
        const int n = n0 + 32 * p + 8 * fq;
        uint4 o;
        o.x = act2(acc[2 * p][j][0], acc[2 * p][j][1], bb[p].x);
        o.y = act2(acc[2 * p][j][2], acc[2 * p][j][3], bb[p].y);
        o.z = act2(acc[2 * p + 1][j][0], acc[2 * p + 1][j][1], bb[p].z);
        o.w = act2(acc[2 * p + 1][j][2], acc[2 * p + 1][j][3], bb[p].w);
        if (n < F_) *(uint4*)(arow + n) = o;
      }
      {
        const int n = n0 + 96 + 4 * fq;
        uint2 o;
        o.x = act2(acc[6][j][0], acc[6][j][1], bb3.x);
        o.y = act2(acc[6][j][2], acc[6][j][3], bb3.y);
        if (n < F_) *(uint2*)(arow + n) = o;
      }
    }
    return;
  }
  const int cw = wave - 4, ct = tid - 256;
  const int ch2 = (bid < 128) ? 512 + bid : 384 + bid;
  uint4 kr0, kr1;
  auto fetch = [&](int d, int ub) {
    __builtin_amdgcn_global_load_lds((const uint32_t*)(xnT + (size_t)d * L_ + cw * 512 + lane * 8),
                                     (uint32_t*)(smem + K2_U + ub * K2_USZ + 64 + cw * 1024), 16, 0, 0);
    const uint4* ks = (const uint4*)(Kbuf + (size_t)d * 4096);
    kr0 = ks[ct];
    kr1 = ks[ct + 256];
  };
  fetch(bid, 0);
  *(uint4*)(smem + K2_KM + (ct >> 3) * 144 + (ct & 7) * 16) = make_uint4(0, 0, 0, 0);
  if (ct < 16) {
    const int ub = ct >> 3, k = ct & 7;
    *(uint4*)(smem + K2_U + ub * K2_USZ + (k < 4 ? k * 16 : 4160 + (k - 4) * 16)) = make_uint4(0, 0, 0, 0);
  }
  __syncthreads();
  const int c = lane & 31, h = lane >> 5;
  const char* wb = smem + K2_WIN + (c + 25 - 8 * h) * 16 + (16 * cw - 1) * 256;
  const char* ka = smem + K2_KM + (c + 32 - 8 * cw - 7) * 144 + h * 16;
  uint32_t ph = 0;
#pragma unroll 1
  for (int task = 0; task < 3; ++task) {
    const int d = (task == 0) ? bid : (task == 1) ? bid + 256 : ch2;
    const int ub = task & 1;
    const char* ubuf = smem + K2_U + ub * K2_USZ;
    asm volatile("s_waitcnt vmcnt(0)" ::: "memory");
    ph += 4; gbar(cnt + 8, ph, lane);
    *(uint4*)(smem + K2_KM + (32 + (ct >> 3)) * 144 + (ct & 7) * 16) = kr0;
    *(uint4*)(smem + K2_KM + (64 + (ct >> 3)) * 144 + (ct & 7) * 16) = kr1;
#pragma unroll
    for (int it = 0; it < 5; ++it) {
      const int pr = ct + 256 * it;
      if (it < 4 || ct < 16) {
        const uint32_t* s = (const uint32_t*)(ubuf) + pr;
        const uint32_t v0 = s[0], v1 = s[1], v2 = s[2], v3 = s[3], v4 = s[4];
        uint4* w = (uint4*)(smem + K2_WIN + pr * 32);
        w[0] = make_uint4(v0, v1, v2, v3);
        w[1] = make_uint4(__builtin_amdgcn_alignbit(v1, v0, 16), __builtin_amdgcn_alignbit(v2, v1, 16),
                          __builtin_amdgcn_alignbit(v3, v2, 16), __builtin_amdgcn_alignbit(v4, v3, 16));
      }
    }
    ph += 4; gbar(cnt + 8, ph, lane);
    if (task < 2) fetch(task == 0 ? bid + 256 : ch2, ub ^ 1);
    f32x16 acc0 = {}, acc1 = {};
    const int mode = (task < 2) ? 3 : (bid < 128 ? 2 : 1);
    if (mode == 3) conv_mfma<3>(acc0, acc1, ka, wb);
    else if (mode == 2) conv_mfma<2>(acc0, acc1, ka, wb);
    else conv_mfma<1>(acc0, acc1, ka, wb);
    ph += 4; gbar(cnt + 8, ph, lane);
    float* P = (float*)(smem + K2_WIN);
    {
      float* pa = P + cw * 2048;
      if (mode & 1) {
#pragma unroll
        for (int i = 0; i < 16; ++i) pa[((i & 3) + 8 * (i >> 2) + 4 * h) * 32 + c] = acc0[i];
      }
      if (mode & 2) {
#pragma unroll
        for (int i = 0; i < 16; ++i) pa[1024 + ((i & 3) + 8 * (i >> 2) + 4 * h) * 32 + c] = acc1[i];
      }
    }
    ph += 4; gbar(cnt + 8, ph, lane);
    if (mode & (ct < 128 ? 1 : 2)) {
      const f32x4* p0 = (const f32x4*)(P + ct * 8);
      float y[8];
#pragma unroll
      for (int i = 0; i < 2; ++i) {
        f32x4 s = (p0[i] + p0[512 + i]) + (p0[1024 + i] + p0[1536 + i]);
        y[4 * i] = s[0]; y[4 * i + 1] = s[1]; y[4 * i + 2] = s[2]; y[4 * i + 3] = s[3];
      }
      const float dsk = bf2f(Dskip[d]);
      const uint4 uv = *(const uint4*)(ubuf + 64 + ct * 16);
      const uint32_t uu[4] = {uv.x, uv.y, uv.z, uv.w};
      uint32_t o4[4];
#pragma unroll
      for (int i = 0; i < 4; ++i) {
        float r0 = bfr(y[2 * i]) + bfr(bflo(uu[i]) * dsk);
        float r1 = bfr(y[2 * i + 1]) + bfr(bfhi(uu[i]) * dsk);
        o4[i] = pack2(r0, r1);
      }
      *(uint4*)(AoutT + (size_t)d * L_ + ct * 8) = make_uint4(o4[0], o4[1], o4[2], o4[3]);
    }
  }
}

__global__ __launch_bounds__(512, 1) void k2_conv_gemm1(
    const u16* __restrict__ xn, const u16* __restrict__ xnT, const u16* __restrict__ Kbuf,
    const u16* __restrict__ Dskip, const u16* __restrict__ w1T, const u16* __restrict__ b1,
    const u16* __restrict__ tbl, u16* __restrict__ act, u16* __restrict__ AoutT) {
  TS_BEGIN(1)
  k2_body(xn, xnT, Kbuf, Dskip, w1T, b1, tbl, act, AoutT);
  TS_END(1)
}

constexpr int K3_STAGE = 144 * 128;
constexpr int K3_NBUF = 4;
constexpr int K3_AO_OFF = K3_STAGE * K3_NBUF;
constexpr int K3_LDS = K3_AO_OFF + 80 * 128;

__device__ __forceinline__ void k3_body(
    const u16* __restrict__ act, const u16* __restrict__ w2T, const u16* __restrict__ x,
    const u16* __restrict__ AoutT, u16* __restrict__ out) {
  __shared__ __attribute__((aligned(16))) char smem[K3_LDS];
  const int tid = threadIdx.x, lane = tid & 63;
  const int wave = __builtin_amdgcn_readfirstlane(tid >> 6);
  const int bid = blockIdx.x;
  const int xc = bid & 7, jj = bid >> 3;
  const int rb = (xc & 3) * 8 + (jj & 7), nt = (xc >> 2) * 4 + (jj >> 3);
  const int m0 = rb * 64, n0 = nt * 80;
  constexpr int NT = F_ / 64;
  f32x4 acc[5][2];
#pragma unroll
  for (int i = 0; i < 5; ++i)
#pragma unroll
    for (int j = 0; j < 2; ++j) acc[i][j] = (f32x4){0.f, 0.f, 0.f, 0.f};
  const int fr = lane & 15, fq = lane >> 4;
  const int wm = wave & 1, ksp = (wave >> 1) & 1;
  const int em0 = tid / 10, ec0 = (tid % 10) * 8;
  const int em1 = (512 + tid) / 10, ec1 = ((512 + tid) % 10) * 8;
  uint4 xv0, xv1 = make_uint4(0, 0, 0, 0);
  if (wave >= 4) {
    const int lw = wave - 4;
    const u16* gsrc[5];
#pragma unroll
    for (int i = 0; i < 5; ++i) {
      int ch = lw + 4 * i;
      if (ch > 17) ch = 17;
      const int row = ch * 8 + (lane >> 3);
      const int sc = (lane & 7) ^ (row & 7);
      gsrc[i] = (row < 64 ? act + (size_t)(m0 + row) * F_ : w2T + (size_t)(n0 + row - 64) * F_) + sc * 8;
    }
    auto stage = [&](int kt, int buf) {
#pragma unroll
      for (int i = 0; i < 4; ++i)
        __builtin_amdgcn_global_load_lds((const uint32_t*)(gsrc[i] + kt * 64),
                                         (uint32_t*)(smem + buf * K3_STAGE + (lw + 4 * i) * 1024), 16, 0, 0);
      if (lw < 2)
        __builtin_amdgcn_global_load_lds((const uint32_t*)(gsrc[4] + kt * 64),
                                         (uint32_t*)(smem + buf * K3_STAGE + (lw + 16) * 1024), 16, 0, 0);
    };
    stage(0, 0); stage(1, 1); stage(2, 2);
    for (int kt = 0; kt < NT; ++kt) {
      const int rem = NT - 1 - kt;
      if (rem >= 2) {
        if (lw < 2) asm volatile("s_waitcnt vmcnt(10)" ::: "memory");
        else asm volatile("s_waitcnt vmcnt(8)" ::: "memory");
      } else if (rem == 1) {
        if (lw < 2) asm volatile("s_waitcnt vmcnt(5)" ::: "memory");
        else asm volatile("s_waitcnt vmcnt(4)" ::: "memory");
      } else {
        asm volatile("s_waitcnt vmcnt(0)" ::: "memory");
      }
      __builtin_amdgcn_s_barrier();
      if (kt + 3 < NT) stage(kt + 3, (kt + 3) & 3);
    }
    xv0 = *(const uint4*)(x + (size_t)(m0 + em0) * D_ + n0 + ec0);
  } else {
#pragma unroll
    for (int i = 0; i < 3; ++i) {
      const int ch = wave + 4 * i;
      if (ch < 10) {
        const int row = ch * 8 + (lane >> 3);
        __builtin_amdgcn_global_load_lds((const uint32_t*)(AoutT + (size_t)(n0 + row) * L_ + m0 + (lane & 7) * 8),
                                         (uint32_t*)(smem + K3_AO_OFF + ch * 1024), 16, 0, 0);
      }
    }
    xv0 = *(const uint4*)(x + (size_t)(m0 + em0) * D_ + n0 + ec0);
    if (tid < 128) xv1 = *(const uint4*)(x + (size_t)(m0 + em1) * D_ + n0 + ec1);
    int aoff[5], boff[2];
#pragma unroll
    for (int i = 0; i < 5; ++i) {
      const int row = 64 + i * 16 + fr;
      aoff[i] = row * 128 + (((ksp * 4 + fq) ^ (row & 7)) * 16);
    }
#pragma unroll
    for (int j = 0; j < 2; ++j) {
      const int row = wm * 32 + j * 16 + fr;
      boff[j] = row * 128 + (((ksp * 4 + fq) ^ (row & 7)) * 16);
    }
    for (int kt = 0; kt < NT; ++kt) {
      asm volatile("s_waitcnt lgkmcnt(0)" ::: "memory");
      __builtin_amdgcn_s_barrier();
      const char* base = smem + (kt & 3) * K3_STAGE;
      bf16x8 af[5], bfm[2];
#pragma unroll
      for (int j = 0; j < 2; ++j) bfm[j] = *(const bf16x8*)(base + boff[j]);
#pragma unroll
      for (int i = 0; i < 5; ++i) af[i] = *(const bf16x8*)(base + aoff[i]);
#pragma unroll
      for (int i = 0; i < 5; ++i)
#pragma unroll
        for (int j = 0; j < 2; ++j)
          acc[i][j] = __builtin_amdgcn_mfma_f32_16x16x32_bf16(af[i], bfm[j], acc[i][j], 0, 0, 0);
    }
  }
  __syncthreads();
  float* red = (float*)smem;
  if (wave < 4) {
#pragma unroll
    for (int i = 0; i < 5; ++i)
#pragma unroll
      for (int j = 0; j < 2; ++j) {
        const int m = wm * 32 + j * 16 + fr, n = i * 16 + fq * 4;
        *(f32x4*)(red + (ksp * 64 + m) * 84 + n) = acc[i][j];
      }
  }
  __syncthreads();
  const u16* ao = (const u16*)(smem + K3_AO_OFF);
#pragma unroll
  for (int it = 0; it < 2; ++it) {
    if (it == 1 && tid >= 128) break;
    const int m = it ? em1 : em0, c8 = it ? ec1 : ec0;
    const uint4 xv = it ? xv1 : xv0;
    const f32x4* r0 = (const f32x4*)(red + m * 84 + c8);
    const f32x4* r1 = (const f32x4*)(red + (64 + m) * 84 + c8);
    const f32x4 s0 = r0[0] + r1[0], s1 = r0[1] + r1[1];
    const float s[8] = {s0[0], s0[1], s0[2], s0[3], s1[0], s1[1], s1[2], s1[3]};
    const uint32_t xx[4] = {xv.x, xv.y, xv.z, xv.w};
    uint32_t o[4];
#pragma unroll
    for (int i = 0; i < 4; ++i) {
      float a0 = bf2f(ao[(c8 + 2 * i) * 64 + m]), a1 = bf2f(ao[(c8 + 2 * i + 1) * 64 + m]);
      float v0 = bfr(bflo(xx[i]) + a0) + bfr(s[2 * i]);
      float v1 = bfr(bfhi(xx[i]) + a1) + bfr(s[2 * i + 1]);
      o[i] = pack2(v0, v1);
    }
    *(uint4*)(out + (size_t)(m0 + m) * D_ + n0 + c8) = make_uint4(o[0], o[1], o[2], o[3]);
  }
}

__global__ __launch_bounds__(512, 1) void k3_gemm2(
    const u16* __restrict__ act, const u16* __restrict__ w2T, const u16* __restrict__ x,
    const u16* __restrict__ AoutT, u16* __restrict__ out) {
  TS_BEGIN(2)
  k3_body(act, w2T, x, AoutT, out);
  TS_END(2)
}

}

extern "C" void kernel_launch(void* const* d_in, const int* in_sizes, int n_in,
                              void* d_out, int out_size, void* d_ws, size_t ws_size,
                              hipStream_t stream) {
  const u16* x = (const u16*)d_in[0];
  const u16* logAr = (const u16*)d_in[1];
  const u16* Aim = (const u16*)d_in[2];
  const u16* Cre = (const u16*)d_in[3];
  const u16* Cim = (const u16*)d_in[4];
  const u16* logdt = (const u16*)d_in[5];
  const u16* Dskip = (const u16*)d_in[6];
  const u16* w1 = (const u16*)d_in[8];
  const u16* b1 = (const u16*)d_in[9];
  const u16* w2 = (const u16*)d_in[10];
  u16* out = (u16*)d_out;
  u16* ws = (u16*)d_ws;
  u16* xn = ws;
  u16* xnT = xn + (size_t)L_ * D_;
  u16* Kbuf = xnT + (size_t)L_ * D_;
  u16* w1T = Kbuf + (size_t)D_ * 4096;
  u16* w2T = w1T + (size_t)D_ * F_;
  u16* act = w2T + (size_t)D_ * F_;
  u16* AoutT = act + (size_t)L_ * F_;
  u16* tbl = AoutT + (size_t)L_ * D_;
  k1_prep<<<K1_BLOCKS, 256, 0, stream>>>(x, logAr, Aim, Cre, Cim, logdt, w1, w2, xn, xnT, Kbuf, w1T, w2T, tbl);
  k2_conv_gemm1<<<256, 512, 0, stream>>>(xn, xnT, Kbuf, Dskip, w1T, b1, tbl, act, AoutT);
  k3_gemm2<<<256, 512, 0, stream>>>(act, w2T, x, AoutT, out);
}
```

```cpp
#include <hip/hip_runtime.h>
#include <hip/hip_bf16.h>
#include <stdint.h>

#define TS_BEGIN(k)
#define TS_END(k)
#define STAMP(i)

namespace {

constexpr int L_ = 2048, D_ = 640, NS_ = 128, F_ = 1728;

typedef __attribute__((ext_vector_type(8))) short bf16x8;
typedef __attribute__((ext_vector_type(4))) float f32x4;
typedef __attribute__((ext_vector_type(16))) float f32x16;
typedef __attribute__((ext_vector_type(2))) float f32x2;
typedef __attribute__((ext_vector_type(2))) __bf16 bf16x2;
typedef unsigned short u16;

constexpr float LOG2E = 1.4426950408889634f;
constexpr float LN2 = 0.6931471805599453f;
constexpr float INV2PI = 0.15915494309189535f;

__device__ __forceinline__ float bf2f(u16 v) { return __uint_as_float(((uint32_t)v) << 16); }
__device__ __forceinline__ float bflo(uint32_t v) { return __uint_as_float(v << 16); }
__device__ __forceinline__ float bfhi(uint32_t v) { return __uint_as_float(v & 0xffff0000u); }
__device__ __forceinline__ u16 f2bf(float x) {
  __bf16 h = (__bf16)x;
  return __builtin_bit_cast(u16, h);
}
__device__ __forceinline__ float bfr(float x) { return bf2f(f2bf(x)); }
__device__ __forceinline__ uint32_t pack2(float a, float b) {
  f32x2 v = {a, b};
  bf16x2 r = __builtin_convertvector(v, bf16x2);
  return __builtin_bit_cast(uint32_t, r);
}

__device__ __forceinline__ float tanh_sp_bf(float h) {
  float e = bfr(__builtin_amdgcn_exp2f(-fabsf(h) * LOG2E));
  float s = e * (1.f - e * (0.5f - e * (1.f / 3.f)));
  float lg = __builtin_amdgcn_logf(1.f + e) * LN2;
  float l = bfr(e < 0.015625f ? s : lg);
  float sp = bfr(l + fmaxf(h, 0.f));
  float q = __builtin_amdgcn_exp2f(sp * (2.f * LOG2E));
  float t = 1.f - 2.f * __builtin_amdgcn_rcpf(q + 1.f);
  float ts = sp * (1.f - sp * sp * (1.f / 3.f));
  return bfr(sp < 0.03125f ? ts : t);
}
constexpr int TB_LO = 117 << 7, TB_HI = 134 << 7;
constexpr int TB_N = TB_HI - TB_LO + 1;
constexpr int TB_STRIDE = 2304;
constexpr int TB_BYTES = 2 * TB_STRIDE * 2;

template <int CTRL>
__device__ __forceinline__ float dppf(float v) {
  return __builtin_bit_cast(float, __builtin_amdgcn_update_dpp(0, __builtin_bit_cast(int, v), CTRL, 0xF, 0xF, true));
}
__device__ __forceinline__ float rdl(float v, int l) {
  return __builtin_bit_cast(float, __builtin_amdgcn_readlane(__builtin_bit_cast(int, v), l));
}
__device__ __forceinline__ float wave_sum(float v) {
  v += dppf<0xB1>(v);
  v += dppf<0x4E>(v);
  v += dppf<0x141>(v);
  v += dppf<0x140>(v);
  return (rdl(v, 0) + rdl(v, 16)) + (rdl(v, 32) + rdl(v, 48));
}

constexpr int K1_KGEN = D_;
constexpr int K1_LN = L_ / 16;
constexpr int K1_T1 = (D_ / 64) * (F_ / 64);
constexpr int K1_BLOCKS = K1_KGEN + K1_LN + 2 * K1_T1 + 1;

__device__ __forceinline__ void k1_body(
    const u16* __restrict__ x, const u16* __restrict__ logAr, const u16* __restrict__ Aim,
    const u16* __restrict__ Cre, const u16* __restrict__ Cim, const u16* __restrict__ logdt,
    const u16* __restrict__ w1, const u16* __restrict__ w2,
    u16* __restrict__ xn, u16* __restrict__ xnT, u16* __restrict__ Kbuf,
    u16* __restrict__ w1T, u16* __restrict__ w2T, u16* __restrict__ tbl) {
  __shared__ __attribute__((aligned(16))) char smem[16 * 1296];
  const int tid = threadIdx.x, lane = tid & 63, wave = tid >> 6;
  int b = blockIdx.x;
  if (b < K1_KGEN) {
    const int d = b;
    float4* prm = (float4*)smem;
    float* part = (float*)(smem + 2048);
    float* kp = (float*)(smem + 2048 + 12288);
    if (tid < NS_) {
      float dt = bfr(expf(bf2f(logdt[d])));
      float ar = -bfr(expf(bf2f(logAr[d * NS_ + tid]))) * dt;
      float ai = bf2f(Aim[d * NS_ + tid]) * dt;
      float cr = bf2f(Cre[d * NS_ + tid]) * dt;
      float ci = bf2f(Cim[d * NS_ + tid]) * dt;
      prm[tid] = make_float4(ar * LOG2E, ai * INV2PI, 2.f * cr, 2.f * ci);
    }
    __syncthreads();
    const int r = lane & 15, kq = lane >> 4;
    float p0 = (float)r;
    float p1 = (r < 8) ? (float)(256 + 2 * r) : (float)(512 + 4 * ((r - 8) & 3) + 256 * ((r - 8) >> 2));
    float p2 = (r < 8) ? (float)(1024 + 8 * (r & 1) + 256 * (r >> 1)) : 2048.f;
    float pb = (float)(16 * r);
    f32x4 acc0 = {0.f, 0.f, 0.f, 0.f}, acc1 = acc0, acc2 = acc0;
#pragma unroll 2
    for (int g = 0; g < 8; ++g) {
      const float4 P = prm[wave * 32 + g * 4 + kq];
      float mb = __builtin_amdgcn_exp2f(P.x * pb);
      float rb = __builtin_amdgcn_fractf(P.y * pb);
      float fr_ = mb * __builtin_amdgcn_cosf(rb), fi_ = mb * __builtin_amdgcn_sinf(rb);
      float br = P.z * fr_ - P.w * fi_;
      float bi = -(P.z * fi_ + P.w * fr_);
      {
        float m = __builtin_amdgcn_exp2f(P.x * p0);
        float rv = __builtin_amdgcn_fractf(P.y * p0);
        float er = m * __builtin_amdgcn_cosf(rv), ei = m * __builtin_amdgcn_sinf(rv);
        acc0 = __builtin_amdgcn_mfma_f32_16x16x4f32(er, br, acc0, 0, 0, 0);
        acc0 = __builtin_amdgcn_mfma_f32_16x16x4f32(ei, bi, acc0, 0, 0, 0);
      }
      {
        float m = __builtin_amdgcn_exp2f(P.x * p1);
        float rv = __builtin_amdgcn_fractf(P.y * p1);
        float er = m * __builtin_amdgcn_cosf(rv), ei = m * __builtin_amdgcn_sinf(rv);
        acc1 = __builtin_amdgcn_mfma_f32_16x16x4f32(er, br, acc1, 0, 0, 0);
        acc1 = __builtin_amdgcn_mfma_f32_16x16x4f32(ei, bi, acc1, 0, 0, 0);
      }
      {
        float m = __builtin_amdgcn_exp2f(P.x * p2);
        float rv = __builtin_amdgcn_fractf(P.y * p2);
        float er = m * __builtin_amdgcn_cosf(rv), ei = m * __builtin_amdgcn_sinf(rv);
        acc2 = __builtin_amdgcn_mfma_f32_16x16x4f32(er, br, acc2, 0, 0, 0);
        acc2 = __builtin_amdgcn_mfma_f32_16x16x4f32(ei, bi, acc2, 0, 0, 0);
      }
    }
#pragma unroll
    for (int i = 0; i < 4; ++i) {
      part[((wave * 3 + 0) * 16 + kq * 4 + i) * 16 + r] = acc0[i];
      part[((wave * 3 + 1) * 16 + kq * 4 + i) * 16 + r] = acc1[i];
      part[((wave * 3 + 2) * 16 + kq * 4 + i) * 16 + r] = acc2[i];
    }
    __syncthreads();
    {
      int o;
      o = (tid & 15) * 16 + (tid >> 4);
      kp[tid] = (part[o] + part[o + 768]) + (part[o + 1536] + part[o + 2304]);
      if (tid < 128) { o = (16 + (tid & 7)) * 16 + (tid >> 3); }
      else { int m = tid - 128, j2 = m >> 2; o = (16 + 8 + (j2 >> 4) * 4 + (m & 3)) * 16 + (j2 & 15); }
      kp[256 + tid] = (part[o] + part[o + 768]) + (part[o + 1536] + part[o + 2304]);
      if (tid <= 128) {
        int j2 = tid >> 1;
        o = (tid == 128) ? (32 + 8) * 16 : (32 + (j2 >> 4) * 2 + (tid & 1)) * 16 + (j2 & 15);
        kp[512 + tid] = (part[o] + part[o + 768]) + (part[o + 1536] + part[o + 2304]);
      }
    }
    __syncthreads();
    float kv[8];
    {
      const int j0 = tid * 8;
      const int s = (j0 < 256) ? 0 : (j0 < 512) ? 1 : (j0 < 1024) ? 2 : 3;
      const int x0 = s ? j0 - (128 << s) : j0;
      const int bidx = s ? 128 + 128 * s : 0;
      const int hm1 = s ? (1 << (s - 1)) - 1 : 0;
#pragma unroll
      for (int jj = 0; jj < 8; ++jj) {
        const int xx = x0 + jj;
        const int q = s ? ((xx + hm1 + ((xx >> s) & 1)) >> s) : xx;
        kv[jj] = kp[bidx + q];
      }
    }
    uint32_t hi[4], lo[4];
#pragma unroll
    for (int i = 0; i < 4; ++i) {
      float a0 = kv[2 * i], a1 = kv[2 * i + 1];
      uint32_t h = pack2(a0, a1);
      hi[i] = h;
      lo[i] = pack2(a0 - bflo(h), a1 - bfhi(h));
    }
    u16* dst = Kbuf + (size_t)d * 4096 + (tid >> 2) * 64 + (tid & 3) * 8;
    *(uint4*)dst = make_uint4(hi[0], hi[1], hi[2], hi[3]);
    *(uint4*)(dst + 32) = make_uint4(lo[0], lo[1], lo[2], lo[3]);
    return;
  }
  b -= K1_KGEN;
  if (b < K1_LN) {
    const int xc = b & 7, kk = b >> 3;
    const int l0 = ((xc * 4 + (kk >> 2)) * 4 + (kk & 3)) * 16;
    uint32_t* tile = (uint32_t*)smem;
    uint32_t v[4][5];
#pragma unroll
    for (int rr = 0; rr < 4; ++rr) {
      const uint32_t* xr = (const uint32_t*)(x + (size_t)(l0 + wave * 4 + rr) * D_);
#pragma unroll
      for (int i = 0; i < 5; ++i) v[rr][i] = xr[lane + 64 * i];
    }
    float mean[4], q[4];
#pragma unroll
    for (int rr = 0; rr < 4; ++rr) {
      float s = 0.f;
#pragma unroll
      for (int i = 0; i < 5; ++i) s += bflo(v[rr][i]) + bfhi(v[rr][i]);
      mean[rr] = wave_sum(s) * (1.f / D_);
    }
#pragma unroll
    for (int rr = 0; rr < 4; ++rr) {
      float s = 0.f;
#pragma unroll
      for (int i = 0; i < 5; ++i) {
        float a = bflo(v[rr][i]) - mean[rr], c = bfhi(v[rr][i]) - mean[rr];
        s += a * a + c * c;
      }
      q[rr] = wave_sum(s);
    }
#pragma unroll
    for (int rr = 0; rr < 4; ++rr) {
      const int row = wave * 4 + rr;
      const float mb = bfr(mean[rr]);
      const float vb = bfr(q[rr] * (1.f / D_));
      const float sd = bfr(sqrtf(bfr(vb + 1e-5f)));
      uint32_t* xo = (uint32_t*)(xn + (size_t)(l0 + row) * D_);
#pragma unroll
      for (int i = 0; i < 5; ++i) {
        float a = bfr(bflo(v[rr][i]) - mb) / sd, c = bfr(bfhi(v[rr][i]) - mb) / sd;
        uint32_t o = pack2(a, c);
        xo[lane + 64 * i] = o;
        tile[row * 324 + lane + 64 * i] = o;
      }
    }
    __syncthreads();
    const u16* t16 = (const u16*)smem;
    for (int d = tid; d < D_; d += 256) {
      uint32_t o[8];
#pragma unroll
      for (int i = 0; i < 8; ++i)
        o[i] = (uint32_t)t16[(2 * i) * 648 + d] | ((uint32_t)t16[(2 * i + 1) * 648 + d] << 16);
      uint4* dst = (uint4*)(xnT + (size_t)d * L_ + l0);
      dst[0] = make_uint4(o[0], o[1], o[2], o[3]);
      dst[1] = make_uint4(o[4], o[5], o[6], o[7]);
    }
    return;
  }
  b -= K1_LN;
  if (b == 2 * K1_T1) {
    for (int i = tid; i < 2 * TB_STRIDE; i += 256) {
      const int sg = i >= TB_STRIDE;
      int m = i - sg * TB_STRIDE;
      if (m > TB_N - 1) m = TB_N - 1;
      const u16 bits = (u16)((TB_LO + m) | (sg << 15));
      tbl[i] = f2bf(tanh_sp_bf(bf2f(bits)));
    }
    return;
  }
  {
    const u16* src; u16* dst; int R, C;
    if (b < K1_T1) { src = w1; dst = w1T; R = D_; C = F_; }
    else { b -= K1_T1; src = w2; dst = w2T; R = F_; C = D_; }
    const int tc = C / 64;
    const int r0 = (b / tc) * 64, c0 = (b % tc) * 64;
    u16* t16 = (u16*)smem;
#pragma unroll
    for (int i = 0; i < 2; ++i) {
      int rr = (tid >> 3) + 32 * i, ch = tid & 7;
      uint4 v = *(const uint4*)(src + (size_t)(r0 + rr) * C + c0 + ch * 8);
      *(uint4*)(t16 + rr * 72 + ch * 8) = v;
    }
    __syncthreads();
#pragma unroll
    for (int i = 0; i < 2; ++i) {
      int cc = (tid >> 3) + 32 * i, ch = tid & 7;
      uint32_t o[4];
#pragma unroll
      for (int k = 0; k < 4; ++k)
        o[k] = (uint32_t)t16[(ch * 8 + 2 * k) * 72 + cc] | ((uint32_t)t16[(ch * 8 + 2 * k + 1) * 72 + cc] << 16);
      *(uint4*)(dst + (size_t)(c0 + cc) * R + r0 + ch * 8) = make_uint4(o[0], o[1], o[2], o[3]);
    }
  }
}

__global__ __launch_bounds__(256) void k1_prep(
    const u16* __restrict__ x, const u16* __restrict__ logAr, const u16* __restrict__ Aim,
    const u16* __restrict__ Cre, const u16* __restrict__ Cim, const u16* __restrict__ logdt,
    const u16* __restrict__ w1, const u16* __restrict__ w2,
    u16* __restrict__ xn, u16* __restrict__ xnT, u16* __restrict__ Kbuf,
    u16* __restrict__ w1T, u16* __restrict__ w2T, u16* __restrict__ tbl) {
  TS_BEGIN(0)
  k1_body(x, logAr, Aim, Cre, Cim, logdt, w1, w2, xn, xnT, Kbuf, w1T, w2T, tbl);
  TS_END(0)
}

constexpr int G1_STAGE = 240 * 128;
constexpr int K2_U = 3 * G1_STAGE;
constexpr int K2_KM = K2_U + 4 * 4160;
constexpr int K2_P = K2_KM + 96 * 144;
constexpr int K2_CNT = K2_P + 32768;
constexpr int K2_LDS = K2_CNT + 64;

typedef __attribute__((address_space(3))) const char* lds_cptr;
typedef short v4i16_t __attribute__((ext_vector_type(4)));
__device__ __forceinline__ v4i16_t trd(lds_cptr p) {
  return __builtin_amdgcn_ds_read_tr16_b64_v4i16((__attribute__((address_space(3))) v4i16_t*)p);
}
__device__ __forceinline__ bf16x8 rd16(lds_cptr p) { return *(const __attribute__((address_space(3))) bf16x8*)p; }
#define SBAR() __builtin_amdgcn_sched_barrier(0)

__device__ __forceinline__ void gbar(uint32_t* cnt, uint32_t target, int lane) {
  asm volatile("s_waitcnt lgkmcnt(0)" ::: "memory");
  if (lane == 0) __hip_atomic_fetch_add(cnt, 1u, __ATOMIC_RELAXED, __HIP_MEMORY_SCOPE_WORKGROUP);
  while (true) {
    uint32_t c = __hip_atomic_load(cnt, __ATOMIC_RELAXED, __HIP_MEMORY_SCOPE_WORKGROUP);
    if ((uint32_t)__builtin_amdgcn_readfirstlane(c) >= target) break;
    __builtin_amdgcn_s_sleep(1);
  }
  asm volatile("" ::: "memory");
}

template <int MODE>
__device__ __forceinline__ void conv_mfma(f32x16& acc0, f32x16& acc1, lds_cptr ka, lds_cptr ub) {
  bf16x8 a0h[2], a0l[2], a1h[2], a1l[2];
  v4i16_t b0x[2], b0y[2], b1x[2], b1y[2];
#define KO(s) ((7 - ((s) >> 1)) * 144 + ((s) & 1) * 32)
#define UO(s) (64 * ((s) >> 1) + 32 * (1 - ((s) & 1)))
#define CAT(x, y) __builtin_shufflevector(x, y, 0, 1, 2, 3, 4, 5, 6, 7)
  b0x[0] = trd(ub + UO(0) + 8); b0y[0] = trd(ub + UO(0));
  if (MODE & 2) a1h[0] = rd16(ka + KO(0) + 32 * 144);
  a0h[0] = rd16(ka + KO(0));
  if (MODE & 2) a1l[0] = rd16(ka + KO(0) + 32 * 144 + 64);
  a0l[0] = rd16(ka + KO(0) + 64);
  if (MODE & 2) { b1x[0] = trd(ub + UO(0) + 2048 + 8); b1y[0] = trd(ub + UO(0) + 2048); }
  SBAR();
#pragma unroll
  for (int s = 0; s < 16; ++s) {
    const int c = s & 1, n = c ^ 1;
    const bool pf = s < 15;
    const bf16x8 b0 = CAT(b0x[c], b0y[c]);
    if (MODE == 3) {
      acc1 = __builtin_amdgcn_mfma_f32_32x32x16_bf16(a1h[c], b0, acc1, 0, 0, 0);
      if (pf) { b0x[n] = trd(ub + UO(s + 1) + 8); b0y[n] = trd(ub + UO(s + 1)); a1h[n] = rd16(ka + KO(s + 1) + 32 * 144); }
      SBAR();
      acc0 = __builtin_amdgcn_mfma_f32_32x32x16_bf16(a0h[c], b0, acc0, 0, 0, 0);
      if (pf) { a0h[n] = rd16(ka + KO(s + 1)); }
      SBAR();
      acc1 = __builtin_amdgcn_mfma_f32_32x32x16_bf16(a1l[c], b0, acc1, 0, 0, 0);
      if (pf) { a1l[n] = rd16(ka + KO(s + 1) + 32 * 144 + 64); }
      SBAR();
      acc0 = __builtin_amdgcn_mfma_f32_32x32x16_bf16(a0l[c], b0, acc0, 0, 0, 0);
      if (pf) { a0l[n] = rd16(ka + KO(s + 1) + 64); }
      SBAR();
      const bf16x8 b1 = CAT(b1x[c], b1y[c]);
      acc1 = __builtin_amdgcn_mfma_f32_32x32x16_bf16(a0h[c], b1, acc1, 0, 0, 0);
      if (pf) { b1x[n] = trd(ub + UO(s + 1) + 2048 + 8); b1y[n] = trd(ub + UO(s + 1) + 2048); }
      SBAR();
      acc1 = __builtin_amdgcn_mfma_f32_32x32x16_bf16(a0l[c], b1, acc1, 0, 0, 0);
      SBAR();
    } else if (MODE == 2) {
      acc1 = __builtin_amdgcn_mfma_f32_32x32x16_bf16(a1h[c], b0, acc1, 0, 0, 0);
      if (pf) { b0x[n] = trd(ub + UO(s + 1) + 8); b0y[n] = trd(ub + UO(s + 1)); a1h[n] = rd16(ka + KO(s + 1) + 32 * 144); }
      SBAR();
      acc1 = __builtin_amdgcn_mfma_f32_32x32x16_bf16(a1l[c], b0, acc1, 0, 0, 0);
      if (pf) { a1l[n] = rd16(ka + KO(s + 1) + 32 * 144 + 64); a0h[n] = rd16(ka + KO(s + 1)); }
      SBAR();
      const bf16x8 b1 = CAT(b1x[c], b1y[c]);
      acc0 = __builtin_amdgcn_mfma_f32_32x32x16_bf16(a0h[c], b1, acc0, 0, 0, 0);
      if (pf) { b1x[n] = trd(ub + UO(s + 1) + 2048 + 8); b1y[n] = trd(ub + UO(s + 1) + 2048); }
      SBAR();
      acc0 = __builtin_amdgcn_mfma_f32_32x32x16_bf16(a0l[c], b1, acc0, 0, 0, 0);
      if (pf) { a0l[n] = rd16(ka + KO(s + 1) + 64); }
      SBAR();
    } else {
      acc0 = __builtin_amdgcn_mfma_f32_32x32x16_bf16(a0h[c], b0, acc0, 0, 0, 0);
      if (pf) { b0x[n] = trd(ub + UO(s + 1) + 8); b0y[n] = trd(ub + UO(s + 1)); a0h[n] = rd16(ka + KO(s + 1)); }
      SBAR();
      acc1 = __builtin_amdgcn_mfma_f32_32x32x16_bf16(a0l[c], b0, acc1, 0, 0, 0);
      if (pf) { a0l[n] = rd16(ka + KO(s + 1) + 64); }
      SBAR();
    }
  }
  if (MODE == 2) acc1 += acc0;
  if (MODE == 1) acc0 += acc1;
#undef KO
#undef UO
#undef CAT
}

__device__ __forceinline__ void k2_body(
    const u16* __restrict__ xn, const u16* __restrict__ xnT, const u16* __restrict__ Kbuf,
    const u16* __restrict__ Dskip, const u16* __restrict__ w1T, const u16* __restrict__ b1,
    const u16* __restrict__ tbl, u16* __restrict__ act, u16* __restrict__ AoutT) {
  __shared__ __attribute__((aligned(16))) char smem[K2_LDS];
  const int tid = threadIdx.x, lane = tid & 63;
  const int wave = __builtin_amdgcn_readfirstlane(tid >> 6);
  const int bid = blockIdx.x;
  uint32_t* cnt = (uint32_t*)(smem + K2_CNT);
  if (wave < 4) {
    const int xc = bid & 7, jj = bid >> 3;
    const int tm = (xc & 3) * 4 + (jj & 3), tn = (xc >> 2) * 8 + (jj >> 2);
    const int m0 = tm * 128, n0 = tn * 112;
    const u16* gsrc[8];
#pragma unroll
    for (int i = 0; i < 8; ++i) {
      int ch = wave + 4 * i;
      if (ch > 29) ch = 29;
      const int row = ch * 8 + (lane >> 3);
      if (row < 128) {
        gsrc[i] = xn + (size_t)(m0 + row) * D_ + (((lane & 7) ^ ((row >> 1) & 7)) * 8);
      } else {
        const int r2 = row - 128;
        int n = n0 + r2;
        if (n > F_ - 1) n = F_ - 1;
        const int sw = (r2 < 96) ? (((r2 >> 1) & 1) | (((r2 >> 3) & 3) << 1)) : ((r2 >> 1) & 7);
        gsrc[i] = w1T + (size_t)n * D_ + (((lane & 7) ^ sw) * 8);
      }
    }
    auto stage = [&](int kt, int buf) {
#pragma unroll
      for (int i = 0; i < 7; ++i)
        __builtin_amdgcn_global_load_lds((const uint32_t*)(gsrc[i] + kt * 64),
                                         (uint32_t*)(smem + buf * G1_STAGE + (wave + 4 * i) * 1024), 16, 0, 0);
      if (wave < 2)
        __builtin_amdgcn_global_load_lds((const uint32_t*)(gsrc[7] + kt * 64),
                                         (uint32_t*)(smem + buf * G1_STAGE + (wave + 28) * 1024), 16, 0, 0);
    };
    stage(0, 0);
    stage(1, 1);
    const int fr = lane & 15, fq = lane >> 4;
    uint4 bb[3]; uint2 bb3;
#pragma unroll
    for (int p = 0; p < 3; ++p) {
      int n = n0 + 32 * p + 8 * fq;
      if (n > F_ - 8) n = F_ - 8;
      bb[p] = *(const uint4*)(b1 + n);
    }
    {
      int n = n0 + 96 + 4 * fq;
      if (n > F_ - 4) n = F_ - 4;
      bb3 = *(const uint2*)(b1 + n);
    }
    STAMP(0)
    if (tid < 16) cnt[tid] = 0;
    __syncthreads();
    STAMP(1)
    int aoff[7], boff[2];
#pragma unroll
    for (int i = 0; i < 7; ++i) {
      const int r2 = (i < 6) ? (32 * (i >> 1) + 8 * (fr >> 2) + 4 * (i & 1) + (fr & 3)) : (96 + fr);
      const int sw = (i < 6) ? (((r2 >> 1) & 1) | (((r2 >> 3) & 3) << 1)) : ((r2 >> 1) & 7);
      aoff[i] = (128 + r2) * 128 + ((fq ^ sw) * 16);
    }
#pragma unroll
    for (int j = 0; j < 2; ++j) {
      const int row = wave * 32 + j * 16 + fr;
      boff[j] = row * 128 + ((fq ^ ((row >> 1) & 7)) * 16);
    }
    f32x4 acc[7][2];
#pragma unroll
    for (int i = 0; i < 7; ++i)
#pragma unroll
      for (int j = 0; j < 2; ++j) acc[i][j] = (f32x4){0.f, 0.f, 0.f, 0.f};
    int slot = 0;
    for (int kt = 0; kt < 10; ++kt) {
      if (kt < 9) {
        if (wave < 2) asm volatile("s_waitcnt vmcnt(8)" ::: "memory");
        else asm volatile("s_waitcnt vmcnt(7)" ::: "memory");
      } else {
        asm volatile("s_waitcnt vmcnt(0)" ::: "memory");
      }
      gbar(cnt, 4 * (kt + 1), lane);
      STAMP(2 + kt)
      int ns = slot + 2; if (ns >= 3) ns -= 3;
      if (kt < 8) stage(kt + 2, ns);
      else if (kt == 8) {
#pragma unroll
        for (int i = 0; i < 3; ++i) {
          const int ch = wave + 4 * i;
          if (ch < 9)
            __builtin_amdgcn_global_load_lds((const uint32_t*)(tbl + ch * 512 + lane * 8),
                                             (uint32_t*)(smem + ns * G1_STAGE + ch * 1024), 16, 0, 0);
        }
      }
      const char* base = smem + slot * G1_STAGE;
#pragma unroll
      for (int ks = 0; ks < 2; ++ks) {
        bf16x8 af[7], bfm[2];
#pragma unroll
        for (int j = 0; j < 2; ++j) bfm[j] = *(const bf16x8*)(base + (boff[j] ^ (ks * 64)));
#pragma unroll
        for (int i = 0; i < 7; ++i) af[i] = *(const bf16x8*)(base + (aoff[i] ^ (ks * 64)));
#pragma unroll
        for (int i = 0; i < 7; ++i)
#pragma unroll
          for (int j = 0; j < 2; ++j)
            acc[i][j] = __builtin_amdgcn_mfma_f32_16x16x32_bf16(af[i], bfm[j], acc[i][j], 0, 0, 0);
      }
      slot = slot + 1; if (slot >= 3) slot = 0;
    }
    STAMP(12)
    const u16* tb = (const u16*)(smem + 1 * G1_STAGE);
    auto act2 = [&](float a0, float a1, uint32_t bias) -> uint32_t {
      uint32_t r = pack2(a0, a1);
      uint32_t h = pack2(bflo(r) + bflo(bias), bfhi(r) + bfhi(bias));
      uint32_t m0_ = h & 0x7fffu, m1_ = (h >> 16) & 0x7fffu;
      m0_ = min(max(m0_, (uint32_t)TB_LO), (uint32_t)TB_HI) - TB_LO + ((h >> 15) & 1u) * TB_STRIDE;
      m1_ = min(max(m1_, (uint32_t)TB_LO), (uint32_t)TB_HI) - TB_LO + (h >> 31) * TB_STRIDE;
      float t0 = bf2f(tb[m0_]), t1 = bf2f(tb[m1_]);
      return pack2(bflo(h) * t0, bfhi(h) * t1);
    };
#pragma unroll
    for (int j = 0; j < 2; ++j) {
      const int m = m0 + wave * 32 + j * 16 + fr;
      u16* arow = act + (size_t)m * F_;
#pragma unroll
      for (int p = 0; p < 3; ++p) {
        const int n = n0 + 32 * p + 8 * fq;
        uint4 o;
        o.x = act2(acc[2 * p][j][0], acc[2 * p][j][1], bb[p].x);
        o.y = act2(acc[2 * p][j][2], acc[2 * p][j][3], bb[p].y);
        o.z = act2(acc[2 * p + 1][j][0], acc[2 * p + 1][j][1], bb[p].z);
        o.w = act2(acc[2 * p + 1][j][2], acc[2 * p + 1][j][3], bb[p].w);
        if (n < F_) *(uint4*)(arow + n) = o;
      }
      {
        const int n = n0 + 96 + 4 * fq;
        uint2 o;
        o.x = act2(acc[6][j][0], acc[6][j][1], bb3.x);
        o.y = act2(acc[6][j][2], acc[6][j][3], bb3.y);
        if (n < F_) *(uint2*)(arow + n) = o;
      }
    }
    STAMP(13)
    return;
  }
  const int cw = wave - 4, ct = tid - 256;
  const int ch2 = (bid < 128) ? 512 + bid : 384 + bid;
  uint4 kr0, kr1, uv; uint2 up; u16 dk;
  auto fetch = [&](int d) {
    const u16* us = xnT + (size_t)d * L_ + ct * 8;
    uv = *(const uint4*)us;
    up = *(const uint2*)(us - (ct ? 4 : 0));
    const uint4* ks = (const uint4*)(Kbuf + (size_t)d * 4096);
    kr0 = ks[ct];
    kr1 = ks[ct + 256];
    dk = Dskip[d];
  };
  auto put = [&]() {
    const uint32_t pm2 = ct ? up.x : 0u, pm1 = ct ? up.y : 0u;
    const uint32_t am = __builtin_amdgcn_alignbit(pm1, pm2, 16), a0 = __builtin_amdgcn_alignbit(uv.x, pm1, 16);
    const uint32_t a1 = __builtin_amdgcn_alignbit(uv.y, uv.x, 16), a2 = __builtin_amdgcn_alignbit(uv.z, uv.y, 16);
    const uint32_t a3 = __builtin_amdgcn_alignbit(uv.w, uv.z, 16);
    char* ud = smem + K2_U + 64 + ct * 16;
    *(uint4*)(ud) = uv;
    *(uint4*)(ud + 4160) = make_uint4(a0, a1, a2, a3);
    *(uint4*)(ud + 2 * 4160) = make_uint4(pm1, uv.x, uv.y, uv.z);
    *(uint4*)(ud + 3 * 4160) = make_uint4(am, a0, a1, a2);
    *(uint4*)(smem + K2_KM + (32 + (ct >> 3)) * 144 + (ct & 7) * 16) = kr0;
    *(uint4*)(smem + K2_KM + (64 + (ct >> 3)) * 144 + (ct & 7) * 16) = kr1;
  };
  fetch(bid);
  *(uint4*)(smem + K2_KM + (ct >> 3) * 144 + (ct & 7) * 16) = make_uint4(0, 0, 0, 0);
  if (ct < 16) *(uint4*)(smem + K2_U + (ct >> 2) * 4160 + (ct & 3) * 16) = make_uint4(0, 0, 0, 0);
  __syncthreads();
  const int c = lane & 31, h = lane >> 5;
  lds_cptr ub = (lds_cptr)(smem + K2_U + ((lane >> 2) & 3) * 4160 + (lane & 16) * 2 + (lane & 3) * 8 - 16 * h + 24 + 512 * cw);
  lds_cptr ka = (lds_cptr)(smem + K2_KM + (c + 32 - 8 * cw - 7) * 144 + h * 16);
  float* P = (float*)(smem + K2_P);
  uint32_t ph = 0;
  put();
  uint4 ucur = uv; u16 dcur = dk;
#pragma unroll 1
  for (int task = 0; task < 3; ++task) {
    const int d = (task == 0) ? bid : (task == 1) ? bid + 256 : ch2;
    STAMP(16 + task * 5)
    ph += 4; gbar(cnt + 8, ph, lane);
    STAMP(17 + task * 5)
    if (task < 2) fetch(task == 0 ? bid + 256 : ch2);
    f32x16 acc0 = {}, acc1 = {};
    const int mode = (task < 2) ? 3 : (bid < 128 ? 2 : 1);
    if (mode == 3) conv_mfma<3>(acc0, acc1, ka, ub);
    else if (mode == 2) conv_mfma<2>(acc0, acc1, ka, ub);
    else conv_mfma<1>(acc0, acc1, ka, ub);
    STAMP(18 + task * 5)
    {
      float* pa = P + cw * 2048;
      if (mode & 1) {
#pragma unroll
        for (int i = 0; i < 16; ++i) pa[((i & 3) + 8 * (i >> 2) + 4 * h) * 32 + c] = acc0[i];
      }
      if (mode & 2) {
#pragma unroll
        for (int i = 0; i < 16; ++i) pa[1024 + ((i & 3) + 8 * (i >> 2) + 4 * h) * 32 + c] = acc1[i];
      }
    }
    ph += 4; gbar(cnt + 8, ph, lane);
    STAMP(19 + task * 5)
    if (task < 2) put();
    if (mode & (ct < 128 ? 1 : 2)) {
      const f32x4* p0 = (const f32x4*)(P + ct * 8);
      float y[8];
#pragma unroll
      for (int i = 0; i < 2; ++i) {
        f32x4 s = (p0[i] + p0[512 + i]) + (p0[1024 + i] + p0[1536 + i]);
        y[4 * i] = s[0]; y[4 * i + 1] = s[1]; y[4 * i + 2] = s[2]; y[4 * i + 3] = s[3];
      }
      const float dsk = bf2f(dcur);
      const uint32_t uu[4] = {ucur.x, ucur.y, ucur.z, ucur.w};
      uint32_t o4[4];
#pragma unroll
      for (int i = 0; i < 4; ++i) {
        float r0 = bfr(y[2 * i]) + bfr(bflo(uu[i]) * dsk);
        float r1 = bfr(y[2 * i + 1]) + bfr(bfhi(uu[i]) * dsk);
        o4[i] = pack2(r0, r1);
      }
      *(uint4*)(AoutT + (size_t)d * L_ + ct * 8) = make_uint4(o4[0], o4[1], o4[2], o4[3]);
    }
    ucur = uv; dcur = dk;
    STAMP(20 + task * 5)
  }
}

__global__ __launch_bounds__(512, 1) void k2_conv_gemm1(
    const u16* __restrict__ xn, const u16* __restrict__ xnT, const u16* __restrict__ Kbuf,
    const u16* __restrict__ Dskip, const u16* __restrict__ w1T, const u16* __restrict__ b1,
    const u16* __restrict__ tbl, u16* __restrict__ act, u16* __restrict__ AoutT) {
  TS_BEGIN(1)
  k2_body(xn, xnT, Kbuf, Dskip, w1T, b1, tbl, act, AoutT);
  TS_END(1)
}

constexpr int K3_STAGE = 144 * 128;
constexpr int K3_NBUF = 4;
constexpr int K3_AO_OFF = K3_STAGE * K3_NBUF;
constexpr int K3_LDS = K3_AO_OFF + 80 * 128;

__device__ __forceinline__ void k3_body(
    const u16* __restrict__ act, const u16* __restrict__ w2T, const u16* __restrict__ x,
    const u16* __restrict__ AoutT, u16* __restrict__ out) {
  __shared__ __attribute__((aligned(16))) char smem[K3_LDS];
  const int tid = threadIdx.x, lane = tid & 63;
  const int wave = __builtin_amdgcn_readfirstlane(tid >> 6);
  const int bid = blockIdx.x;
  const int xc = bid & 7, jj = bid >> 3;
  const int rb = (xc & 3) * 8 + (jj & 7), nt = (xc >> 2) * 4 + (jj >> 3);
  const int m0 = rb * 64, n0 = nt * 80;
  constexpr int NT = F_ / 64;
  f32x4 acc[5][2];
#pragma unroll
  for (int i = 0; i < 5; ++i)
#pragma unroll
    for (int j = 0; j < 2; ++j) acc[i][j] = (f32x4){0.f, 0.f, 0.f, 0.f};
  const int fr = lane & 15, fq = lane >> 4;
  const int wm = wave & 1, ksp = (wave >> 1) & 1;
  const int em0 = tid / 10, ec0 = (tid % 10) * 8;
  const int em1 = (512 + tid) / 10, ec1 = ((512 + tid) % 10) * 8;
  uint4 xv0, xv1 = make_uint4(0, 0, 0, 0);
  if (wave >= 4) {
    const int lw = wave - 4;
    const u16* gsrc[5];
#pragma unroll
    for (int i = 0; i < 5; ++i) {
      int ch = lw + 4 * i;
      if (ch > 17) ch = 17;
      const int row = ch * 8 + (lane >> 3);
      const int sc = (lane & 7) ^ (row & 7);
      gsrc[i] = (row < 64 ? act + (size_t)(m0 + row) * F_ : w2T + (size_t)(n0 + row - 64) * F_) + sc * 8;
    }
    auto stage = [&](int kt, int buf) {
#pragma unroll
      for (int i = 0; i < 4; ++i)
        __builtin_amdgcn_global_load_lds((const uint32_t*)(gsrc[i] + kt * 64),
                                         (uint32_t*)(smem + buf * K3_STAGE + (lw + 4 * i) * 1024), 16, 0, 0);
      if (lw < 2)
        __builtin_amdgcn_global_load_lds((const uint32_t*)(gsrc[4] + kt * 64),
                                         (uint32_t*)(smem + buf * K3_STAGE + (lw + 16) * 1024), 16, 0, 0);
    };
    stage(0, 0); stage(1, 1); stage(2, 2);
    for (int kt = 0; kt < NT; ++kt) {
      const int rem = NT - 1 - kt;
      if (rem >= 2) {
        if (lw < 2) asm volatile("s_waitcnt vmcnt(10)" ::: "memory");
        else asm volatile("s_waitcnt vmcnt(8)" ::: "memory");
      } else if (rem == 1) {
        if (lw < 2) asm volatile("s_waitcnt vmcnt(5)" ::: "memory");
        else asm volatile("s_waitcnt vmcnt(4)" ::: "memory");
      } else {
        asm volatile("s_waitcnt vmcnt(0)" ::: "memory");
      }
      __builtin_amdgcn_s_barrier();
      if (kt + 3 < NT) stage(kt + 3, (kt + 3) & 3);
    }
    xv0 = *(const uint4*)(x + (size_t)(m0 + em0) * D_ + n0 + ec0);
  } else {
#pragma unroll
    for (int i = 0; i < 3; ++i) {
      const int ch = wave + 4 * i;
      if (ch < 10) {
        const int row = ch * 8 + (lane >> 3);
        __builtin_amdgcn_global_load_lds((const uint32_t*)(AoutT + (size_t)(n0 + row) * L_ + m0 + (lane & 7) * 8),
                                         (uint32_t*)(smem + K3_AO_OFF + ch * 1024), 16, 0, 0);
      }
    }
    xv0 = *(const uint4*)(x + (size_t)(m0 + em0) * D_ + n0 + ec0);
    if (tid < 128) xv1 = *(const uint4*)(x + (size_t)(m0 + em1) * D_ + n0 + ec1);
    int aoff[5], boff[2];
#pragma unroll
    for (int i = 0; i < 5; ++i) {
      const int row = 64 + i * 16 + fr;
      aoff[i] = row * 128 + (((ksp * 4 + fq) ^ (row & 7)) * 16);
    }
#pragma unroll
    for (int j = 0; j < 2; ++j) {
      const int row = wm * 32 + j * 16 + fr;
      boff[j] = row * 128 + (((ksp * 4 + fq) ^ (row & 7)) * 16);
    }
    for (int kt = 0; kt < NT; ++kt) {
      asm volatile("s_waitcnt lgkmcnt(0)" ::: "memory");
      __builtin_amdgcn_s_barrier();
      const char* base = smem + (kt & 3) * K3_STAGE;
      bf16x8 af[5], bfm[2];
#pragma unroll
      for (int j = 0; j < 2; ++j) bfm[j] = *(const bf16x8*)(base + boff[j]);
#pragma unroll
      for (int i = 0; i < 5; ++i) af[i] = *(const bf16x8*)(base + aoff[i]);
#pragma unroll
      for (int i = 0; i < 5; ++i)
#pragma unroll
        for (int j = 0; j < 2; ++j)
          acc[i][j] = __builtin_amdgcn_mfma_f32_16x16x32_bf16(af[i], bfm[j], acc[i][j], 0, 0, 0);
    }
  }
  __syncthreads();
  float* red = (float*)smem;
  if (wave < 4) {
#pragma unroll
    for (int i = 0; i < 5; ++i)
#pragma unroll
      for (int j = 0; j < 2; ++j) {
        const int m = wm * 32 + j * 16 + fr, n = i * 16 + fq * 4;
        *(f32x4*)(red + (ksp * 64 + m) * 84 + n) = acc[i][j];
      }
  }
  __syncthreads();
  const u16* ao = (const u16*)(smem + K3_AO_OFF);
#pragma unroll
  for (int it = 0; it < 2; ++it) {
    if (it == 1 && tid >= 128) break;
    const int m = it ? em1 : em0, c8 = it ? ec1 : ec0;
    const uint4 xv = it ? xv1 : xv0;
    const f32x4* r0 = (const f32x4*)(red + m * 84 + c8);
    const f32x4* r1 = (const f32x4*)(red + (64 + m) * 84 + c8);
    const f32x4 s0 = r0[0] + r1[0], s1 = r0[1] + r1[1];
    const float s[8] = {s0[0], s0[1], s0[2], s0[3], s1[0], s1[1], s1[2], s1[3]};
    const uint32_t xx[4] = {xv.x, xv.y, xv.z, xv.w};
    uint32_t o[4];
#pragma unroll
    for (int i = 0; i < 4; ++i) {
      float a0 = bf2f(ao[(c8 + 2 * i) * 64 + m]), a1 = bf2f(ao[(c8 + 2 * i + 1) * 64 + m]);
      float v0 = bfr(bflo(xx[i]) + a0) + bfr(s[2 * i]);
      float v1 = bfr(bfhi(xx[i]) + a1) + bfr(s[2 * i + 1]);
      o[i] = pack2(v0, v1);
    }
    *(uint4*)(out + (size_t)(m0 + m) * D_ + n0 + c8) = make_uint4(o[0], o[1], o[2], o[3]);
  }
}

__global__ __launch_bounds__(512, 1) void k3_gemm2(
    const u16* __restrict__ act, const u16* __restrict__ w2T, const u16* __restrict__ x,
    const u16* __restrict__ AoutT, u16* __restrict__ out) {
  TS_BEGIN(2)
  k3_body(act, w2T, x, AoutT, out);
  TS_END(2)
}

}

extern "C" void kernel_launch(void* const* d_in, const int* in_sizes, int n_in,
                              void* d_out, int out_size, void* d_ws, size_t ws_size,
                              hipStream_t stream) {
  const u16* x = (const u16*)d_in[0];
  const u16* logAr = (const u16*)d_in[1];
  const u16* Aim = (const u16*)d_in[2];
  const u16* Cre = (const u16*)d_in[3];
  const u16* Cim = (const u16*)d_in[4];
  const u16* logdt = (const u16*)d_in[5];
  const u16* Dskip = (const u16*)d_in[6];
  const u16* w1 = (const u16*)d_in[8];
  const u16* b1 = (const u16*)d_in[9];
  const u16* w2 = (const u16*)d_in[10];
  u16* out = (u16*)d_out;
  u16* ws = (u16*)d_ws;
  u16* xn = ws;
  u16* xnT = xn + (size_t)L_ * D_;
  u16* Kbuf = xnT + (size_t)L_ * D_;
  u16* w1T = Kbuf + (size_t)D_ * 4096;
  u16* w2T = w1T + (size_t)D_ * F_;
  u16* act = w2T + (size_t)D_ * F_;
  u16* AoutT = act + (size_t)L_ * F_;
  u16* tbl = AoutT + (size_t)L_ * D_;
  k1_prep<<<K1_BLOCKS, 256, 0, stream>>>(x, logAr, Aim, Cre, Cim, logdt, w1, w2, xn, xnT, Kbuf, w1T, w2T, tbl);
  k2_conv_gemm1<<<256, 512, 0, stream>>>(xn, xnT, Kbuf, Dskip, w1T, b1, tbl, act, AoutT);
  k3_gemm2<<<256, 512, 0, stream>>>(act, w2T, x, AoutT, out);
}
```

```cpp
#include <hip/hip_runtime.h>
#include <hip/hip_bf16.h>
#include <stdint.h>

#define TS_BEGIN(k)
#define TS_END(k)
#define STAMP(i)

namespace {

constexpr int L_ = 2048, D_ = 640, NS_ = 128, F_ = 1728;

typedef __attribute__((ext_vector_type(8))) short bf16x8;
typedef __attribute__((ext_vector_type(4))) float f32x4;
typedef __attribute__((ext_vector_type(16))) float f32x16;
typedef __attribute__((ext_vector_type(2))) float f32x2;
typedef __attribute__((ext_vector_type(2))) __bf16 bf16x2;
typedef unsigned short u16;

constexpr float LOG2E = 1.4426950408889634f;
constexpr float LN2 = 0.6931471805599453f;
constexpr float INV2PI = 0.15915494309189535f;

__device__ __forceinline__ float bf2f(u16 v) { return __uint_as_float(((uint32_t)v) << 16); }
__device__ __forceinline__ float bflo(uint32_t v) { return __uint_as_float(v << 16); }
__device__ __forceinline__ float bfhi(uint32_t v) { return __uint_as_float(v & 0xffff0000u); }
__device__ __forceinline__ u16 f2bf(float x) {
  __bf16 h = (__bf16)x;
  return __builtin_bit_cast(u16, h);
}
__device__ __forceinline__ float bfr(float x) { return bf2f(f2bf(x)); }
__device__ __forceinline__ uint32_t pack2(float a, float b) {
  f32x2 v = {a, b};
  bf16x2 r = __builtin_convertvector(v, bf16x2);
  return __builtin_bit_cast(uint32_t, r);
}

__device__ __forceinline__ float tanh_sp_bf(float h) {
  float e = bfr(__builtin_amdgcn_exp2f(-fabsf(h) * LOG2E));
  float s = e * (1.f - e * (0.5f - e * (1.f / 3.f)));
  float lg = __builtin_amdgcn_logf(1.f + e) * LN2;
  float l = bfr(e < 0.015625f ? s : lg);
  float sp = bfr(l + fmaxf(h, 0.f));
  float q = __builtin_amdgcn_exp2f(sp * (2.f * LOG2E));
  float t = 1.f - 2.f * __builtin_amdgcn_rcpf(q + 1.f);
  float ts = sp * (1.f - sp * sp * (1.f / 3.f));
  return bfr(sp < 0.03125f ? ts : t);
}
constexpr int TB_LO = 117 << 7, TB_HI = 134 << 7;
constexpr int TB_N = TB_HI - TB_LO + 1;
constexpr int TB_STRIDE = 2304;
constexpr int TB_BYTES = 2 * TB_STRIDE * 2;

template <int CTRL>
__device__ __forceinline__ float dppf(float v) {
  return __builtin_bit_cast(float, __builtin_amdgcn_update_dpp(0, __builtin_bit_cast(int, v), CTRL, 0xF, 0xF, true));
}
__device__ __forceinline__ float rdl(float v, int l) {
  return __builtin_bit_cast(float, __builtin_amdgcn_readlane(__builtin_bit_cast(int, v), l));
}
__device__ __forceinline__ float wave_sum(float v) {
  v += dppf<0xB1>(v);
  v += dppf<0x4E>(v);
  v += dppf<0x141>(v);
  v += dppf<0x140>(v);
  return (rdl(v, 0) + rdl(v, 16)) + (rdl(v, 32) + rdl(v, 48));
}

constexpr int K1_KGEN = D_;
constexpr int K1_LN = L_ / 16;
constexpr int K1_T1 = (D_ / 64) * (F_ / 64);
constexpr int K1_BLOCKS = K1_KGEN + K1_LN + 2 * K1_T1 + 1;

__device__ __forceinline__ void k1_body(
    const u16* __restrict__ x, const u16* __restrict__ logAr, const u16* __restrict__ Aim,
    const u16* __restrict__ Cre, const u16* __restrict__ Cim, const u16* __restrict__ logdt,
    const u16* __restrict__ w1, const u16* __restrict__ w2,
    u16* __restrict__ xn, u16* __restrict__ xnT, u16* __restrict__ Kbuf,
    u16* __restrict__ w1T, u16* __restrict__ w2T, u16* __restrict__ tbl) {
  __shared__ __attribute__((aligned(16))) char smem[16 * 1296];
  const int tid = threadIdx.x, lane = tid & 63, wave = tid >> 6;
  int b = blockIdx.x;
  if (b < K1_KGEN) {
    const int d = b;
    float4* prm = (float4*)smem;
    float* part = (float*)(smem + 2048);
    float* kp = (float*)(smem + 2048 + 12288);
    if (tid < NS_) {
      float dt = bfr(expf(bf2f(logdt[d])));
      float ar = -bfr(expf(bf2f(logAr[d * NS_ + tid]))) * dt;
      float ai = bf2f(Aim[d * NS_ + tid]) * dt;
      float cr = bf2f(Cre[d * NS_ + tid]) * dt;
      float ci = bf2f(Cim[d * NS_ + tid]) * dt;
      prm[tid] = make_float4(ar * LOG2E, ai * INV2PI, 2.f * cr, 2.f * ci);
    }
    __syncthreads();
    const int r = lane & 15, kq = lane >> 4;
    float p0 = (float)r;
    float p1 = (r < 8) ? (float)(256 + 2 * r) : (float)(512 + 4 * ((r - 8) & 3) + 256 * ((r - 8) >> 2));
    float p2 = (r < 8) ? (float)(1024 + 8 * (r & 1) + 256 * (r >> 1)) : 2048.f;
    float pb = (float)(16 * r);
    f32x4 acc0 = {0.f, 0.f, 0.f, 0.f}, acc1 = acc0, acc2 = acc0;
#pragma unroll 2
    for (int g = 0; g < 8; ++g) {
      const float4 P = prm[wave * 32 + g * 4 + kq];
      float mb = __builtin_amdgcn_exp2f(P.x * pb);
      float rb = __builtin_amdgcn_fractf(P.y * pb);
      float fr_ = mb * __builtin_amdgcn_cosf(rb), fi_ = mb * __builtin_amdgcn_sinf(rb);
      float br = P.z * fr_ - P.w * fi_;
      float bi = -(P.z * fi_ + P.w * fr_);
      {
        float m = __builtin_amdgcn_exp2f(P.x * p0);
        float rv = __builtin_amdgcn_fractf(P.y * p0);
        float er = m * __builtin_amdgcn_cosf(rv), ei = m * __builtin_amdgcn_sinf(rv);
        acc0 = __builtin_amdgcn_mfma_f32_16x16x4f32(er, br, acc0, 0, 0, 0);
        acc0 = __builtin_amdgcn_mfma_f32_16x16x4f32(ei, bi, acc0, 0, 0, 0);
      }
      {
        float m = __builtin_amdgcn_exp2f(P.x * p1);
        float rv = __builtin_amdgcn_fractf(P.y * p1);
        float er = m * __builtin_amdgcn_cosf(rv), ei = m * __builtin_amdgcn_sinf(rv);
        acc1 = __builtin_amdgcn_mfma_f32_16x16x4f32(er, br, acc1, 0, 0, 0);
        acc1 = __builtin_amdgcn_mfma_f32_16x16x4f32(ei, bi, acc1, 0, 0, 0);
      }
      {
        float m = __builtin_amdgcn_exp2f(P.x * p2);
        float rv = __builtin_amdgcn_fractf(P.y * p2);
        float er = m * __builtin_amdgcn_cosf(rv), ei = m * __builtin_amdgcn_sinf(rv);
        acc2 = __builtin_amdgcn_mfma_f32_16x16x4f32(er, br, acc2, 0, 0, 0);
        acc2 = __builtin_amdgcn_mfma_f32_16x16x4f32(ei, bi, acc2, 0, 0, 0);
      }
    }
#pragma unroll
    for (int i = 0; i < 4; ++i) {
      part[((wave * 3 + 0) * 16 + kq * 4 + i) * 16 + r] = acc0[i];
      part[((wave * 3 + 1) * 16 + kq * 4 + i) * 16 + r] = acc1[i];
      part[((wave * 3 + 2) * 16 + kq * 4 + i) * 16 + r] = acc2[i];
    }
    __syncthreads();
    {
      int o;
      o = (tid & 15) * 16 + (tid >> 4);
      kp[tid] = (part[o] + part[o + 768]) + (part[o + 1536] + part[o + 2304]);
      if (tid < 128) { o = (16 + (tid & 7)) * 16 + (tid >> 3); }
      else { int m = tid - 128, j2 = m >> 2; o = (16 + 8 + (j2 >> 4) * 4 + (m & 3)) * 16 + (j2 & 15); }
      kp[256 + tid] = (part[o] + part[o + 768]) + (part[o + 1536] + part[o + 2304]);
      if (tid <= 128) {
        int j2 = tid >> 1;
        o = (tid == 128) ? (32 + 8) * 16 : (32 + (j2 >> 4) * 2 + (tid & 1)) * 16 + (j2 & 15);
        kp[512 + tid] = (part[o] + part[o + 768]) + (part[o + 1536] + part[o + 2304]);
      }
    }
    __syncthreads();
    float kv[8];
    {
      const int j0 = tid * 8;
      const int s = (j0 < 256) ? 0 : (j0 < 512) ? 1 : (j0 < 1024) ? 2 : 3;
      const int x0 = s ? j0 - (128 << s) : j0;
      const int bidx = s ? 128 + 128 * s : 0;
      const int hm1 = s ? (1 << (s - 1)) - 1 : 0;
#pragma unroll
      for (int jj = 0; jj < 8; ++jj) {
        const int xx = x0 + jj;
        const int q = s ? ((xx + hm1 + ((xx >> s) & 1)) >> s) : xx;
        kv[jj] = kp[bidx + q];
      }
    }
    uint32_t hi[4], lo[4];
#pragma unroll
    for (int i = 0; i < 4; ++i) {
      float a0 = kv[2 * i], a1 = kv[2 * i + 1];
      uint32_t h = pack2(a0, a1);
      hi[i] = h;
      lo[i] = pack2(a0 - bflo(h), a1 - bfhi(h));
    }
    u16* dst = Kbuf + (size_t)d * 4096 + tid * 8;
    *(uint4*)dst = make_uint4(hi[0], hi[1], hi[2], hi[3]);
    *(uint4*)(dst + 2048) = make_uint4(lo[0], lo[1], lo[2], lo[3]);
    return;
  }
  b -= K1_KGEN;
  if (b < K1_LN) {
    const int xc = b & 7, kk = b >> 3;
    const int l0 = ((xc * 4 + (kk >> 2)) * 4 + (kk & 3)) * 16;
    uint32_t* tile = (uint32_t*)smem;
    uint32_t v[4][5];
#pragma unroll
    for (int rr = 0; rr < 4; ++rr) {
      const uint32_t* xr = (const uint32_t*)(x + (size_t)(l0 + wave * 4 + rr) * D_);
#pragma unroll
      for (int i = 0; i < 5; ++i) v[rr][i] = xr[lane + 64 * i];
    }
    float mean[4], q[4];
#pragma unroll
    for (int rr = 0; rr < 4; ++rr) {
      float s = 0.f;
#pragma unroll
      for (int i = 0; i < 5; ++i) s += bflo(v[rr][i]) + bfhi(v[rr][i]);
      mean[rr] = wave_sum(s) * (1.f / D_);
    }
#pragma unroll
    for (int rr = 0; rr < 4; ++rr) {
      float s = 0.f;
#pragma unroll
      for (int i = 0; i < 5; ++i) {
        float a = bflo(v[rr][i]) - mean[rr], c = bfhi(v[rr][i]) - mean[rr];
        s += a * a + c * c;
      }
      q[rr] = wave_sum(s);
    }
#pragma unroll
    for (int rr = 0; rr < 4; ++rr) {
      const int row = wave * 4 + rr;
      const float mb = bfr(mean[rr]);
      const float vb = bfr(q[rr] * (1.f / D_));
      const float sd = bfr(sqrtf(bfr(vb + 1e-5f)));
      uint32_t* xo = (uint32_t*)(xn + (size_t)(l0 + row) * D_);
#pragma unroll
      for (int i = 0; i < 5; ++i) {
        float a = bfr(bflo(v[rr][i]) - mb) / sd, c = bfr(bfhi(v[rr][i]) - mb) / sd;
        uint32_t o = pack2(a, c);
        xo[lane + 64 * i] = o;
        tile[row * 324 + lane + 64 * i] = o;
      }
    }
    __syncthreads();
    const u16* t16 = (const u16*)smem;
    for (int d = tid; d < D_; d += 256) {
      uint32_t o[8];
#pragma unroll
      for (int i = 0; i < 8; ++i)
        o[i] = (uint32_t)t16[(2 * i) * 648 + d] | ((uint32_t)t16[(2 * i + 1) * 648 + d] << 16);
      uint4* dst = (uint4*)(xnT + (size_t)d * L_ + l0);
      dst[0] = make_uint4(o[0], o[1], o[2], o[3]);
      dst[1] = make_uint4(o[4], o[5], o[6], o[7]);
    }
    return;
  }
  b -= K1_LN;
  if (b == 2 * K1_T1) {
    for (int i = tid; i < 2 * TB_STRIDE; i += 256) {
      const int sg = i >= TB_STRIDE;
      int m = i - sg * TB_STRIDE;
      if (m > TB_N - 1) m = TB_N - 1;
      const u16 bits = (u16)((TB_LO + m) | (sg << 15));
      tbl[i] = f2bf(tanh_sp_bf(bf2f(bits)));
    }
    return;
  }
  {
    const u16* src; u16* dst; int R, C;
    if (b < K1_T1) { src = w1; dst = w1T; R = D_; C = F_; }
    else { b -= K1_T1; src = w2; dst = w2T; R = F_; C = D_; }
    const int tc = C / 64;
    const int r0 = (b / tc) * 64, c0 = (b % tc) * 64;
    u16* t16 = (u16*)smem;
#pragma unroll
    for (int i = 0; i < 2; ++i) {
      int rr = (tid >> 3) + 32 * i, ch = tid & 7;
      uint4 v = *(const uint4*)(src + (size_t)(r0 + rr) * C + c0 + ch * 8);
      *(uint4*)(t16 + rr * 72 + ch * 8) = v;
    }
    __syncthreads();
#pragma unroll
    for (int i = 0; i < 2; ++i) {
      int cc = (tid >> 3) + 32 * i, ch = tid & 7;
      uint32_t o[4];
#pragma unroll
      for (int k = 0; k < 4; ++k)
        o[k] = (uint32_t)t16[(ch * 8 + 2 * k) * 72 + cc] | ((uint32_t)t16[(ch * 8 + 2 * k + 1) * 72 + cc] << 16);
      *(uint4*)(dst + (size_t)(c0 + cc) * R + r0 + ch * 8) = make_uint4(o[0], o[1], o[2], o[3]);
    }
  }
}

__global__ __launch_bounds__(256) void k1_prep(
    const u16* __restrict__ x, const u16* __restrict__ logAr, const u16* __restrict__ Aim,
    const u16* __restrict__ Cre, const u16* __restrict__ Cim, const u16* __restrict__ logdt,
    const u16* __restrict__ w1, const u16* __restrict__ w2,
    u16* __restrict__ xn, u16* __restrict__ xnT, u16* __restrict__ Kbuf,
    u16* __restrict__ w1T, u16* __restrict__ w2T, u16* __restrict__ tbl) {
  TS_BEGIN(0)
  k1_body(x, logAr, Aim, Cre, Cim, logdt, w1, w2, xn, xnT, Kbuf, w1T, w2T, tbl);
  TS_END(0)
}

constexpr int G1_STAGE = 240 * 128;
constexpr int K2_U = 3 * G1_STAGE;
constexpr int K2_KM = K2_U + 4 * 4160;
constexpr int K2_P = K2_KM + 2 * 4608;
constexpr int K2_CNT = K2_P + 32768;
constexpr int K2_LDS = K2_CNT + 64;

typedef __attribute__((address_space(3))) const char* lds_cptr;
typedef short v4i16_t __attribute__((ext_vector_type(4)));
__device__ __forceinline__ v4i16_t trd(lds_cptr p) {
  return __builtin_amdgcn_ds_read_tr16_b64_v4i16((__attribute__((address_space(3))) v4i16_t*)p);
}
__device__ __forceinline__ bf16x8 rd16(lds_cptr p) { return *(const __attribute__((address_space(3))) bf16x8*)p; }
#define SBAR() __builtin_amdgcn_sched_barrier(0)

__device__ __forceinline__ void gbar(uint32_t* cnt, uint32_t target, int lane) {
  asm volatile("s_waitcnt lgkmcnt(0)" ::: "memory");
  if (lane == 0) __hip_atomic_fetch_add(cnt, 1u, __ATOMIC_RELAXED, __HIP_MEMORY_SCOPE_WORKGROUP);
  while (true) {
    uint32_t c = __hip_atomic_load(cnt, __ATOMIC_RELAXED, __HIP_MEMORY_SCOPE_WORKGROUP);
    if ((uint32_t)__builtin_amdgcn_readfirstlane(c) >= target) break;
    __builtin_amdgcn_s_sleep(1);
  }
  asm volatile("" ::: "memory");
}

template <int MASK>
__device__ __forceinline__ void conv_mfma(f32x4 (&acc)[8], lds_cptr ka, lds_cptr ub) {
  constexpr int T = (MASK & 0x80) ? 7 : 5;
  bf16x8 ah[2][8], al[2][8];
  v4i16_t bx[2], by[2];
#define CAT(x, y) __builtin_shufflevector(x, y, 0, 1, 2, 3, 4, 5, 6, 7)
  bx[0] = trd(ub + 8); by[0] = trd(ub);
#pragma unroll
  for (int b = 0; b <= T; ++b) { ah[0][b] = rd16(ka + 256 + 512 * b); al[0][b] = rd16(ka + 256 + 512 * b + 4608); }
  SBAR();
#pragma unroll
  for (int rr = 0; rr < 2; ++rr) {
#pragma unroll
    for (int a = 0; a <= T; ++a) {
      const int cur = (rr * (T + 1) + a) & 1, nxt = cur ^ 1;
      const bf16x8 bf = CAT(bx[cur], by[cur]);
      int cnt = 0, pend = -1;
#pragma unroll
      for (int bb = 0; bb <= T - a; ++bb) {
        const int b = (rr == 1 && a == 0) ? T - bb : bb;
        if (!((MASK >> (a + b)) & 1)) continue;
        acc[a + b] = __builtin_amdgcn_mfma_f32_16x16x32_bf16(ah[rr][b], bf, acc[a + b], 0, 0, 0);
        if (cnt == 0) {
          if (a < T) { bx[nxt] = trd(ub + rr * 256 + 512 * (a + 1) + 8); by[nxt] = trd(ub + rr * 256 + 512 * (a + 1)); }
          else if (rr == 0) { bx[nxt] = trd(ub + 256 + 8); by[nxt] = trd(ub + 256); }
        }
        SBAR();
        if (pend >= 0) {
          acc[a + pend] = __builtin_amdgcn_mfma_f32_16x16x32_bf16(al[rr][pend], bf, acc[a + pend], 0, 0, 0);
          SBAR();
        }
        pend = b; ++cnt;
      }
      acc[a + pend] = __builtin_amdgcn_mfma_f32_16x16x32_bf16(al[rr][pend], bf, acc[a + pend], 0, 0, 0);
      if (rr == 0) { ah[1][T - a] = rd16(ka + 512 * (T - a)); al[1][T - a] = rd16(ka + 512 * (T - a) + 4608); }
      SBAR();
    }
  }
#undef CAT
}

__device__ __forceinline__ void k2_body(
    const u16* __restrict__ xn, const u16* __restrict__ xnT, const u16* __restrict__ Kbuf,
    const u16* __restrict__ Dskip, const u16* __restrict__ w1T, const u16* __restrict__ b1,
    const u16* __restrict__ tbl, u16* __restrict__ act, u16* __restrict__ AoutT) {
  __shared__ __attribute__((aligned(16))) char smem[K2_LDS];
  const int tid = threadIdx.x, lane = tid & 63;
  const int wave = __builtin_amdgcn_readfirstlane(tid >> 6);
  const int bid = blockIdx.x;
  uint32_t* cnt = (uint32_t*)(smem + K2_CNT);
  if (wave < 4) {
    const int xc = bid & 7, jj = bid >> 3;
    const int tm = (xc & 3) * 4 + (jj & 3), tn = (xc >> 2) * 8 + (jj >> 2);
    const int m0 = tm * 128, n0 = tn * 112;
    const u16* gsrc[8];
#pragma unroll
    for (int i = 0; i < 8; ++i) {
      int ch = wave + 4 * i;
      if (ch > 29) ch = 29;
      const int row = ch * 8 + (lane >> 3);
      if (row < 128) {
        gsrc[i] = xn + (size_t)(m0 + row) * D_ + (((lane & 7) ^ ((row >> 1) & 7)) * 8);
      } else {
        const int r2 = row - 128;
        int n = n0 + r2;
        if (n > F_ - 1) n = F_ - 1;
        const int sw = (r2 < 96) ? (((r2 >> 1) & 1) | (((r2 >> 3) & 3) << 1)) : ((r2 >> 1) & 7);
        gsrc[i] = w1T + (size_t)n * D_ + (((lane & 7) ^ sw) * 8);
      }
    }
    auto stage = [&](int kt, int buf) {
#pragma unroll
      for (int i = 0; i < 7; ++i)
        __builtin_amdgcn_global_load_lds((const uint32_t*)(gsrc[i] + kt * 64),
                                         (uint32_t*)(smem + buf * G1_STAGE + (wave + 4 * i) * 1024), 16, 0, 0);
      if (wave < 2)
        __builtin_amdgcn_global_load_lds((const uint32_t*)(gsrc[7] + kt * 64),
                                         (uint32_t*)(smem + buf * G1_STAGE + (wave + 28) * 1024), 16, 0, 0);
    };
    stage(0, 0);
    stage(1, 1);
    const int fr = lane & 15, fq = lane >> 4;
    uint4 bb[3]; uint2 bb3;
#pragma unroll
    for (int p = 0; p < 3; ++p) {
      int n = n0 + 32 * p + 8 * fq;
      if (n > F_ - 8) n = F_ - 8;
      bb[p] = *(const uint4*)(b1 + n);
    }
    {
      int n = n0 + 96 + 4 * fq;
      if (n > F_ - 4) n = F_ - 4;
      bb3 = *(const uint2*)(b1 + n);
    }
    STAMP(0)
    if (tid < 16) cnt[tid] = 0;
    __syncthreads();
    STAMP(1)
    int aoff[7], boff[2];
#pragma unroll
    for (int i = 0; i < 7; ++i) {
      const int r2 = (i < 6) ? (32 * (i >> 1) + 8 * (fr >> 2) + 4 * (i & 1) + (fr & 3)) : (96 + fr);
      const int sw = (i < 6) ? (((r2 >> 1) & 1) | (((r2 >> 3) & 3) << 1)) : ((r2 >> 1) & 7);
      aoff[i] = (128 + r2) * 128 + ((fq ^ sw) * 16);
    }
#pragma unroll
    for (int j = 0; j < 2; ++j) {
      const int row = wave * 32 + j * 16 + fr;
      boff[j] = row * 128 + ((fq ^ ((row >> 1) & 7)) * 16);
    }
    f32x4 acc[7][2];
#pragma unroll
    for (int i = 0; i < 7; ++i)
#pragma unroll
      for (int j = 0; j < 2; ++j) acc[i][j] = (f32x4){0.f, 0.f, 0.f, 0.f};
    int slot = 0;
    for (int kt = 0; kt < 10; ++kt) {
      if (kt < 9) {
        if (wave < 2) asm volatile("s_waitcnt vmcnt(8)" ::: "memory");
        else asm volatile("s_waitcnt vmcnt(7)" ::: "memory");
      } else {
        asm volatile("s_waitcnt vmcnt(0)" ::: "memory");
      }
      gbar(cnt, 4 * (kt + 1), lane);
      STAMP(2 + kt)
      int ns = slot + 2; if (ns >= 3) ns -= 3;
      if (kt < 8) stage(kt + 2, ns);
      else if (kt == 8) {
#pragma unroll
        for (int i = 0; i < 3; ++i) {
          const int ch = wave + 4 * i;
          if (ch < 9)
            __builtin_amdgcn_global_load_lds((const uint32_t*)(tbl + ch * 512 + lane * 8),
                                             (uint32_t*)(smem + ns * G1_STAGE + ch * 1024), 16, 0, 0);
        }
      }
      const char* base = smem + slot * G1_STAGE;
#pragma unroll
      for (int ks = 0; ks < 2; ++ks) {
        bf16x8 af[7], bfm[2];
#pragma unroll
        for (int j = 0; j < 2; ++j) bfm[j] = *(const bf16x8*)(base + (boff[j] ^ (ks * 64)));
#pragma unroll
        for (int i = 0; i < 7; ++i) af[i] = *(const bf16x8*)(base + (aoff[i] ^ (ks * 64)));
#pragma unroll
        for (int i = 0; i < 7; ++i)
#pragma unroll
          for (int j = 0; j < 2; ++j)
            acc[i][j] = __builtin_amdgcn_mfma_f32_16x16x32_bf16(af[i], bfm[j], acc[i][j], 0, 0, 0);
      }
      slot = slot + 1; if (slot >= 3) slot = 0;
    }
    STAMP(12)
    const u16* tb = (const u16*)(smem + 1 * G1_STAGE);
    auto act2 = [&](float a0, float a1, uint32_t bias) -> uint32_t {
      uint32_t r = pack2(a0, a1);
      uint32_t h = pack2(bflo(r) + bflo(bias), bfhi(r) + bfhi(bias));
      uint32_t m0_ = h & 0x7fffu, m1_ = (h >> 16) & 0x7fffu;
      m0_ = min(max(m0_, (uint32_t)TB_LO), (uint32_t)TB_HI) - TB_LO + ((h >> 15) & 1u) * TB_STRIDE;
      m1_ = min(max(m1_, (uint32_t)TB_LO), (uint32_t)TB_HI) - TB_LO + (h >> 31) * TB_STRIDE;
      float t0 = bf2f(tb[m0_]), t1 = bf2f(tb[m1_]);
      return pack2(bflo(h) * t0, bfhi(h) * t1);
    };
#pragma unroll
    for (int j = 0; j < 2; ++j) {
      const int m = m0 + wave * 32 + j * 16 + fr;
      u16* arow = act + (size_t)m * F_;
#pragma unroll
      for (int p = 0; p < 3; ++p) {
        const int n = n0 + 32 * p + 8 * fq;
        uint4 o;
        o.x = act2(acc[2 * p][j][0], acc[2 * p][j][1], bb[p].x);
        o.y = act2(acc[2 * p][j][2], acc[2 * p][j][3], bb[p].y);
        o.z = act2(acc[2 * p + 1][j][0], acc[2 * p + 1][j][1], bb[p].z);
        o.w = act2(acc[2 * p + 1][j][2], acc[2 * p + 1][j][3], bb[p].w);
        if (n < F_) *(uint4*)(arow + n) = o;
      }
      {
        const int n = n0 + 96 + 4 * fq;
        uint2 o;
        o.x = act2(acc[6][j][0], acc[6][j][1], bb3.x);
        o.y = act2(acc[6][j][2], acc[6][j][3], bb3.y);
        if (n < F_) *(uint2*)(arow + n) = o;
      }
    }
    STAMP(13)
    return;
  }
  const int cw = wave - 4, ct = tid - 256;
  const int ch2 = (bid < 128) ? 512 + bid : 384 + bid;
  uint4 kr0, kr1, uv; uint2 up; u16 dk;
  auto fetch = [&](int d) {
    const u16* us = xnT + (size_t)d * L_ + ct * 8;
    uv = *(const uint4*)us;
    up = *(const uint2*)(us - (ct ? 4 : 0));
    const uint4* ks = (const uint4*)(Kbuf + (size_t)d * 4096);
    kr0 = ks[ct];
    kr1 = ks[ct + 256];
    dk = Dskip[d];
  };
  auto put = [&]() {
    const uint32_t pm2 = ct ? up.x : 0u, pm1 = ct ? up.y : 0u;
    const uint32_t am = __builtin_amdgcn_alignbit(pm1, pm2, 16), a0 = __builtin_amdgcn_alignbit(uv.x, pm1, 16);
    const uint32_t a1 = __builtin_amdgcn_alignbit(uv.y, uv.x, 16), a2 = __builtin_amdgcn_alignbit(uv.z, uv.y, 16);
    const uint32_t a3 = __builtin_amdgcn_alignbit(uv.w, uv.z, 16);
    char* ud = smem + K2_U + 64 + ct * 16;
    *(uint4*)(ud) = uv;
    *(uint4*)(ud + 4160) = make_uint4(a0, a1, a2, a3);
    *(uint4*)(ud + 2 * 4160) = make_uint4(pm1, uv.x, uv.y, uv.z);
    *(uint4*)(ud + 3 * 4160) = make_uint4(am, a0, a1, a2);
    *(uint4*)(smem + K2_KM + 512 + ct * 16) = kr0;
    *(uint4*)(smem + K2_KM + 4608 + 512 + ct * 16) = kr1;
  };
  fetch(bid);
  if (ct < 64) *(uint4*)(smem + K2_KM + (ct >> 5) * 4608 + (ct & 31) * 16) = make_uint4(0, 0, 0, 0);
  else if (ct < 80) *(uint4*)(smem + K2_U + ((ct >> 2) & 3) * 4160 + (ct & 3) * 16) = make_uint4(0, 0, 0, 0);
  __syncthreads();
  const int fr = lane & 15, kg = lane >> 4;
  lds_cptr ub = (lds_cptr)(smem + K2_U + ((lane >> 2) & 3) * 4160 + 32 * (kg >> 1) - 16 * (kg & 1) + (lane & 3) * 8 + 56 + 64 * cw);
  lds_cptr ka = (lds_cptr)(smem + K2_KM + 512 + 32 * fr - 32 * (kg >> 1) + 16 * (kg & 1) - 64 * cw - 256);
  float* P = (float*)(smem + K2_P);
  uint32_t ph = 0;
  put();
  uint4 ucur = uv; u16 dcur = dk;
#pragma unroll 1
  for (int task = 0; task < 3; ++task) {
    const int d = (task == 0) ? bid : (task == 1) ? bid + 256 : ch2;
    STAMP(16 + task * 5)
    ph += 4; gbar(cnt + 8, ph, lane);
    STAMP(17 + task * 5)
    if (task < 2) fetch(task == 0 ? bid + 256 : ch2);
    f32x4 acc[8];
#pragma unroll
    for (int i = 0; i < 8; ++i) acc[i] = (f32x4){0.f, 0.f, 0.f, 0.f};
    const int mask = (task < 2) ? 0xFF : (bid < 128 ? 0xC3 : 0x3C);
    if (mask == 0xFF) conv_mfma<0xFF>(acc, ka, ub);
    else if (mask == 0xC3) conv_mfma<0xC3>(acc, ka, ub);
    else conv_mfma<0x3C>(acc, ka, ub);
    STAMP(18 + task * 5)
    {
      float* pa = P + cw * 2048 + kg * 64 + fr;
#pragma unroll
      for (int i = 0; i < 8; ++i)
        if ((mask >> i) & 1) {
#pragma unroll
          for (int r = 0; r < 4; ++r) pa[i * 256 + r * 16] = acc[i][r];
        }
    }
    ph += 4; gbar(cnt + 8, ph, lane);
    STAMP(19 + task * 5)
    if (task < 2) put();
    if ((mask >> (ct >> 5)) & 1) {
      const f32x4* p0 = (const f32x4*)(P + ct * 8);
      float y[8];
#pragma unroll
      for (int i = 0; i < 2; ++i) {
        f32x4 s = (p0[i] + p0[512 + i]) + (p0[1024 + i] + p0[1536 + i]);
        y[4 * i] = s[0]; y[4 * i + 1] = s[1]; y[4 * i + 2] = s[2]; y[4 * i + 3] = s[3];
      }
      const float dsk = bf2f(dcur);
      const uint32_t uu[4] = {ucur.x, ucur.y, ucur.z, ucur.w};
      uint32_t o4[4];
#pragma unroll
      for (int i = 0; i < 4; ++i) {
        float r0 = bfr(y[2 * i]) + bfr(bflo(uu[i]) * dsk);
        float r1 = bfr(y[2 * i + 1]) + bfr(bfhi(uu[i]) * dsk);
        o4[i] = pack2(r0, r1);
      }
      *(uint4*)(AoutT + (size_t)d * L_ + ct * 8) = make_uint4(o4[0], o4[1], o4[2], o4[3]);
    }
    ucur = uv; dcur = dk;
    STAMP(20 + task * 5)
  }
}

__global__ __launch_bounds__(512, 1) void k2_conv_gemm1(
    const u16* __restrict__ xn, const u16* __restrict__ xnT, const u16* __restrict__ Kbuf,
    const u16* __restrict__ Dskip, const u16* __restrict__ w1T, const u16* __restrict__ b1,
    const u16* __restrict__ tbl, u16* __restrict__ act, u16* __restrict__ AoutT) {
  TS_BEGIN(1)
  k2_body(xn, xnT, Kbuf, Dskip, w1T, b1, tbl, act, AoutT);
  TS_END(1)
}

constexpr int K3_STAGE = 144 * 128;
constexpr int K3_NBUF = 4;
constexpr int K3_AO_OFF = K3_STAGE * K3_NBUF;
constexpr int K3_LDS = K3_AO_OFF + 80 * 128;

__device__ __forceinline__ void k3_body(
    const u16* __restrict__ act, const u16* __restrict__ w2T, const u16* __restrict__ x,
    const u16* __restrict__ AoutT, u16* __restrict__ out) {
  __shared__ __attribute__((aligned(16))) char smem[K3_LDS];
  const int tid = threadIdx.x, lane = tid & 63;
  const int wave = __builtin_amdgcn_readfirstlane(tid >> 6);
  const int bid = blockIdx.x;
  const int xc = bid & 7, jj = bid >> 3;
  const int rb = (xc & 3) * 8 + (jj & 7), nt = (xc >> 2) * 4 + (jj >> 3);
  const int m0 = rb * 64, n0 = nt * 80;
  constexpr int NT = F_ / 64;
  f32x4 acc[5][2];
#pragma unroll
  for (int i = 0; i < 5; ++i)
#pragma unroll
    for (int j = 0; j < 2; ++j) acc[i][j] = (f32x4){0.f, 0.f, 0.f, 0.f};
  const int fr = lane & 15, fq = lane >> 4;
  const int wm = wave & 1, ksp = (wave >> 1) & 1;
  const int em0 = tid / 10, ec0 = (tid % 10) * 8;
  const int em1 = (512 + tid) / 10, ec1 = ((512 + tid) % 10) * 8;
  uint4 xv0, xv1 = make_uint4(0, 0, 0, 0);
  if (wave >= 4) {
    const int lw = wave - 4;
    const u16* gsrc[5];
#pragma unroll
    for (int i = 0; i < 5; ++i) {
      int ch = lw + 4 * i;
      if (ch > 17) ch = 17;
      const int row = ch * 8 + (lane >> 3);
      const int sc = (lane & 7) ^ (row & 7);
      gsrc[i] = (row < 64 ? act + (size_t)(m0 + row) * F_ : w2T + (size_t)(n0 + row - 64) * F_) + sc * 8;
    }
    auto stage = [&](int kt, int buf) {
#pragma unroll
      for (int i = 0; i < 4; ++i)
        __builtin_amdgcn_global_load_lds((const uint32_t*)(gsrc[i] + kt * 64),
                                         (uint32_t*)(smem + buf * K3_STAGE + (lw + 4 * i) * 1024), 16, 0, 0);
      if (lw < 2)
        __builtin_amdgcn_global_load_lds((const uint32_t*)(gsrc[4] + kt * 64),
                                         (uint32_t*)(smem + buf * K3_STAGE + (lw + 16) * 1024), 16, 0, 0);
    };
    stage(0, 0); stage(1, 1); stage(2, 2);
    for (int kt = 0; kt < NT; ++kt) {
      const int rem = NT - 1 - kt;
      if (rem >= 2) {
        if (lw < 2) asm volatile("s_waitcnt vmcnt(10)" ::: "memory");
        else asm volatile("s_waitcnt vmcnt(8)" ::: "memory");
      } else if (rem == 1) {
        if (lw < 2) asm volatile("s_waitcnt vmcnt(5)" ::: "memory");
        else asm volatile("s_waitcnt vmcnt(4)" ::: "memory");
      } else {
        asm volatile("s_waitcnt vmcnt(0)" ::: "memory");
      }
      __builtin_amdgcn_s_barrier();
      if (kt + 3 < NT) stage(kt + 3, (kt + 3) & 3);
    }
    xv0 = *(const uint4*)(x + (size_t)(m0 + em0) * D_ + n0 + ec0);
  } else {
#pragma unroll
    for (int i = 0; i < 3; ++i) {
      const int ch = wave + 4 * i;
      if (ch < 10) {
        const int row = ch * 8 + (lane >> 3);
        __builtin_amdgcn_global_load_lds((const uint32_t*)(AoutT + (size_t)(n0 + row) * L_ + m0 + (lane & 7) * 8),
                                         (uint32_t*)(smem + K3_AO_OFF + ch * 1024), 16, 0, 0);
      }
    }
    xv0 = *(const uint4*)(x + (size_t)(m0 + em0) * D_ + n0 + ec0);
    if (tid < 128) xv1 = *(const uint4*)(x + (size_t)(m0 + em1) * D_ + n0 + ec1);
    int aoff[5], boff[2];
#pragma unroll
    for (int i = 0; i < 5; ++i) {
      const int row = 64 + i * 16 + fr;
      aoff[i] = row * 128 + (((ksp * 4 + fq) ^ (row & 7)) * 16);
    }
#pragma unroll
    for (int j = 0; j < 2; ++j) {
      const int row = wm * 32 + j * 16 + fr;
      boff[j] = row * 128 + (((ksp * 4 + fq) ^ (row & 7)) * 16);
    }
    for (int kt = 0; kt < NT; ++kt) {
      asm volatile("s_waitcnt lgkmcnt(0)" ::: "memory");
      __builtin_amdgcn_s_barrier();
      const char* base = smem + (kt & 3) * K3_STAGE;
      bf16x8 af[5], bfm[2];
#pragma unroll
      for (int j = 0; j < 2; ++j) bfm[j] = *(const bf16x8*)(base + boff[j]);
#pragma unroll
      for (int i = 0; i < 5; ++i) af[i] = *(const bf16x8*)(base + aoff[i]);
#pragma unroll
      for (int i = 0; i < 5; ++i)
#pragma unroll
        for (int j = 0; j < 2; ++j)
          acc[i][j] = __builtin_amdgcn_mfma_f32_16x16x32_bf16(af[i], bfm[j], acc[i][j], 0, 0, 0);
    }
  }
  __syncthreads();
  float* red = (float*)smem;
  if (wave < 4) {
#pragma unroll
    for (int i = 0; i < 5; ++i)
#pragma unroll
      for (int j = 0; j < 2; ++j) {
        const int m = wm * 32 + j * 16 + fr, n = i * 16 + fq * 4;
        *(f32x4*)(red + (ksp * 64 + m) * 84 + n) = acc[i][j];
      }
  }
  __syncthreads();
  const u16* ao = (const u16*)(smem + K3_AO_OFF);
#pragma unroll
  for (int it = 0; it < 2; ++it) {
    if (it == 1 && tid >= 128) break;
    const int m = it ? em1 : em0, c8 = it ? ec1 : ec0;
    const uint4 xv = it ? xv1 : xv0;
    const f32x4* r0 = (const f32x4*)(red + m * 84 + c8);
    const f32x4* r1 = (const f32x4*)(red + (64 + m) * 84 + c8);
    const f32x4 s0 = r0[0] + r1[0], s1 = r0[1] + r1[1];
    const float s[8] = {s0[0], s0[1], s0[2], s0[3], s1[0], s1[1], s1[2], s1[3]};
    const uint32_t xx[4] = {xv.x, xv.y, xv.z, xv.w};
    uint32_t o[4];
#pragma unroll
    for (int i = 0; i < 4; ++i) {
      float a0 = bf2f(ao[(c8 + 2 * i) * 64 + m]), a1 = bf2f(ao[(c8 + 2 * i + 1) * 64 + m]);
      float v0 = bfr(bflo(xx[i]) + a0) + bfr(s[2 * i]);
      float v1 = bfr(bfhi(xx[i]) + a1) + bfr(s[2 * i + 1]);
      o[i] = pack2(v0, v1);
    }
    *(uint4*)(out + (size_t)(m0 + m) * D_ + n0 + c8) = make_uint4(o[0], o[1], o[2], o[3]);
  }
}

__global__ __launch_bounds__(512, 1) void k3_gemm2(
    const u16* __restrict__ act, const u16* __restrict__ w2T, const u16* __restrict__ x,
    const u16* __restrict__ AoutT, u16* __restrict__ out) {
  TS_BEGIN(2)
  k3_body(act, w2T, x, AoutT, out);
  TS_END(2)
}

}

extern "C" void kernel_launch(void* const* d_in, const int* in_sizes, int n_in,
                              void* d_out, int out_size, void* d_ws, size_t ws_size,
                              hipStream_t stream) {
  const u16* x = (const u16*)d_in[0];
  const u16* logAr = (const u16*)d_in[1];
  const u16* Aim = (const u16*)d_in[2];
  const u16* Cre = (const u16*)d_in[3];
  const u16* Cim = (const u16*)d_in[4];
  const u16* logdt = (const u16*)d_in[5];
  const u16* Dskip = (const u16*)d_in[6];
  const u16* w1 = (const u16*)d_in[8];
  const u16* b1 = (const u16*)d_in[9];
  const u16* w2 = (const u16*)d_in[10];
  u16* out = (u16*)d_out;
  u16* ws = (u16*)d_ws;
  u16* xn = ws;
  u16* xnT = xn + (size_t)L_ * D_;
  u16* Kbuf = xnT + (size_t)L_ * D_;
  u16* w1T = Kbuf + (size_t)D_ * 4096;
  u16* w2T = w1T + (size_t)D_ * F_;
  u16* act = w2T + (size_t)D_ * F_;
  u16* AoutT = act + (size_t)L_ * F_;
  u16* tbl = AoutT + (size_t)L_ * D_;
  k1_prep<<<K1_BLOCKS, 256, 0, stream>>>(x, logAr, Aim, Cre, Cim, logdt, w1, w2, xn, xnT, Kbuf, w1T, w2T, tbl);
  k2_conv_gemm1<<<256, 512, 0, stream>>>(xn, xnT, Kbuf, Dskip, w1T, b1, tbl, act, AoutT);
  k3_gemm2<<<256, 512, 0, stream>>>(act, w2T, x, AoutT, out);
}
```

```cpp
#include <hip/hip_runtime.h>
#include <hip/hip_bf16.h>
#include <stdint.h>
#include <type_traits>

#define TS_BEGIN(k)
#define TS_END(k)
#define STAMP(i)
#define STAMPK(i)
#define STAMPK_DUMP

#define DMAV 0

namespace {

constexpr int L_ = 2048, D_ = 640, NS_ = 128, F_ = 1728;

typedef __attribute__((ext_vector_type(8))) short bf16x8;
typedef __attribute__((ext_vector_type(4))) float f32x4;
typedef __attribute__((ext_vector_type(16))) float f32x16;
typedef __attribute__((ext_vector_type(2))) float f32x2;
typedef __attribute__((ext_vector_type(2))) __bf16 bf16x2;
typedef unsigned short u16;

constexpr float LOG2E = 1.4426950408889634f;
constexpr float LN2 = 0.6931471805599453f;
constexpr float INV2PI = 0.15915494309189535f;

__device__ __forceinline__ float bf2f(u16 v) { return __uint_as_float(((uint32_t)v) << 16); }
__device__ __forceinline__ float bflo(uint32_t v) { return __uint_as_float(v << 16); }
__device__ __forceinline__ float bfhi(uint32_t v) { return __uint_as_float(v & 0xffff0000u); }
__device__ __forceinline__ u16 f2bf(float x) {
  __bf16 h = (__bf16)x;
  return __builtin_bit_cast(u16, h);
}
__device__ __forceinline__ float bfr(float x) { return bf2f(f2bf(x)); }
__device__ __forceinline__ uint32_t pack2(float a, float b) {
  f32x2 v = {a, b};
  bf16x2 r = __builtin_convertvector(v, bf16x2);
  return __builtin_bit_cast(uint32_t, r);
}

__device__ __forceinline__ float tanh_sp_bf(float h) {
  float e = bfr(__builtin_amdgcn_exp2f(-fabsf(h) * LOG2E));
  float s = e * (1.f - e * (0.5f - e * (1.f / 3.f)));
  float lg = __builtin_amdgcn_logf(1.f + e) * LN2;
  float l = bfr(e < 0.015625f ? s : lg);
  float sp = bfr(l + fmaxf(h, 0.f));
  float q = __builtin_amdgcn_exp2f(sp * (2.f * LOG2E));
  float t = 1.f - 2.f * __builtin_amdgcn_rcpf(q + 1.f);
  float ts = sp * (1.f - sp * sp * (1.f / 3.f));
  return bfr(sp < 0.03125f ? ts : t);
}
constexpr int TB_LO = 117 << 7, TB_HI = 134 << 7;
constexpr int TB_N = TB_HI - TB_LO + 1;
constexpr int TB_STRIDE = 2304;
constexpr int TB_BYTES = 2 * TB_STRIDE * 2;

template <int CTRL>
__device__ __forceinline__ float dppf(float v) {
  return __builtin_bit_cast(float, __builtin_amdgcn_update_dpp(0, __builtin_bit_cast(int, v), CTRL, 0xF, 0xF, true));
}
__device__ __forceinline__ float rdl(float v, int l) {
  return __builtin_bit_cast(float, __builtin_amdgcn_readlane(__builtin_bit_cast(int, v), l));
}
__device__ __forceinline__ float wave_sum(float v) {
  v += dppf<0xB1>(v);
  v += dppf<0x4E>(v);
  v += dppf<0x141>(v);
  v += dppf<0x140>(v);
  return (rdl(v, 0) + rdl(v, 16)) + (rdl(v, 32) + rdl(v, 48));
}

constexpr int K1_KGEN = D_;
constexpr int K1_LN = L_ / 16;
constexpr int K1_T1 = (D_ / 64) * (F_ / 64);
constexpr int K1_BLOCKS = K1_KGEN + K1_LN + 2 * K1_T1 + 1;

__device__ __forceinline__ void k1_body(
    const u16* __restrict__ x, const u16* __restrict__ logAr, const u16* __restrict__ Aim,
    const u16* __restrict__ Cre, const u16* __restrict__ Cim, const u16* __restrict__ logdt,
    const u16* __restrict__ w1, const u16* __restrict__ w2,
    u16* __restrict__ xn, u16* __restrict__ xnT, u16* __restrict__ Kbuf,
    u16* __restrict__ w1T, u16* __restrict__ w2T, u16* __restrict__ tbl) {
  __shared__ __attribute__((aligned(16))) char smem[16 * 1296];
  const int tid = threadIdx.x, lane = tid & 63, wave = tid >> 6;
  int b = blockIdx.x;
  if (b < K1_KGEN) {
    const int d = b;
    float4* prm = (float4*)smem;
    float* part = (float*)(smem + 2048);
    float* kp = (float*)(smem + 2048 + 12288);
    if (tid < NS_) {
      float dt = bfr(expf(bf2f(logdt[d])));
      float ar = -bfr(expf(bf2f(logAr[d * NS_ + tid]))) * dt;
      float ai = bf2f(Aim[d * NS_ + tid]) * dt;
      float cr = bf2f(Cre[d * NS_ + tid]) * dt;
      float ci = bf2f(Cim[d * NS_ + tid]) * dt;
      prm[tid] = make_float4(ar * LOG2E, ai * INV2PI, 2.f * cr, 2.f * ci);
    }
    __syncthreads();
    const int r = lane & 15, kq = lane >> 4;
    float p0 = (float)r;
    float p1 = (r < 8) ? (float)(256 + 2 * r) : (float)(512 + 4 * ((r - 8) & 3) + 256 * ((r - 8) >> 2));
    float p2 = (r < 8) ? (float)(1024 + 8 * (r & 1) + 256 * (r >> 1)) : 2048.f;
    float pb = (float)(16 * r);
    f32x4 acc0 = {0.f, 0.f, 0.f, 0.f}, acc1 = acc0, acc2 = acc0;
#pragma unroll 2
    for (int g = 0; g < 8; ++g) {
      const float4 P = prm[wave * 32 + g * 4 + kq];
      float mb = __builtin_amdgcn_exp2f(P.x * pb);
      float rb = __builtin_amdgcn_fractf(P.y * pb);
      float fr_ = mb * __builtin_amdgcn_cosf(rb), fi_ = mb * __builtin_amdgcn_sinf(rb);
      float br = P.z * fr_ - P.w * fi_;
      float bi = -(P.z * fi_ + P.w * fr_);
      {
        float m = __builtin_amdgcn_exp2f(P.x * p0);
        float rv = __builtin_amdgcn_fractf(P.y * p0);
        float er = m * __builtin_amdgcn_cosf(rv), ei = m * __builtin_amdgcn_sinf(rv);
        acc0 = __builtin_amdgcn_mfma_f32_16x16x4f32(er, br, acc0, 0, 0, 0);
        acc0 = __builtin_amdgcn_mfma_f32_16x16x4f32(ei, bi, acc0, 0, 0, 0);
      }
      {
        float m = __builtin_amdgcn_exp2f(P.x * p1);
        float rv = __builtin_amdgcn_fractf(P.y * p1);
        float er = m * __builtin_amdgcn_cosf(rv), ei = m * __builtin_amdgcn_sinf(rv);
        acc1 = __builtin_amdgcn_mfma_f32_16x16x4f32(er, br, acc1, 0, 0, 0);
        acc1 = __builtin_amdgcn_mfma_f32_16x16x4f32(ei, bi, acc1, 0, 0, 0);
      }
      {
        float m = __builtin_amdgcn_exp2f(P.x * p2);
        float rv = __builtin_amdgcn_fractf(P.y * p2);
        float er = m * __builtin_amdgcn_cosf(rv), ei = m * __builtin_amdgcn_sinf(rv);
        acc2 = __builtin_amdgcn_mfma_f32_16x16x4f32(er, br, acc2, 0, 0, 0);
        acc2 = __builtin_amdgcn_mfma_f32_16x16x4f32(ei, bi, acc2, 0, 0, 0);
      }
    }
#pragma unroll
    for (int i = 0; i < 4; ++i) {
      part[((wave * 3 + 0) * 16 + kq * 4 + i) * 16 + r] = acc0[i];
      part[((wave * 3 + 1) * 16 + kq * 4 + i) * 16 + r] = acc1[i];
      part[((wave * 3 + 2) * 16 + kq * 4 + i) * 16 + r] = acc2[i];
    }
    __syncthreads();
    {
      int o;
      o = (tid & 15) * 16 + (tid >> 4);
      kp[tid] = (part[o] + part[o + 768]) + (part[o + 1536] + part[o + 2304]);
      if (tid < 128) { o = (16 + (tid & 7)) * 16 + (tid >> 3); }
      else { int m = tid - 128, j2 = m >> 2; o = (16 + 8 + (j2 >> 4) * 4 + (m & 3)) * 16 + (j2 & 15); }
      kp[256 + tid] = (part[o] + part[o + 768]) + (part[o + 1536] + part[o + 2304]);
      if (tid <= 128) {
        int j2 = tid >> 1;
        o = (tid == 128) ? (32 + 8) * 16 : (32 + (j2 >> 4) * 2 + (tid & 1)) * 16 + (j2 & 15);
        kp[512 + tid] = (part[o] + part[o + 768]) + (part[o + 1536] + part[o + 2304]);
      }
    }
    __syncthreads();
    float kv[8];
    {
      const int j0 = tid * 8;
      const int s = (j0 < 256) ? 0 : (j0 < 512) ? 1 : (j0 < 1024) ? 2 : 3;
      const int x0 = s ? j0 - (128 << s) : j0;
      const int bidx = s ? 128 + 128 * s : 0;
      const int hm1 = s ? (1 << (s - 1)) - 1 : 0;
#pragma unroll
      for (int jj = 0; jj < 8; ++jj) {
        const int xx = x0 + jj;
        const int q = s ? ((xx + hm1 + ((xx >> s) & 1)) >> s) : xx;
        kv[jj] = kp[bidx + q];
      }
    }
    uint32_t hi[4], lo[4];
#pragma unroll
    for (int i = 0; i < 4; ++i) {
      float a0 = kv[2 * i], a1 = kv[2 * i + 1];
      uint32_t h = pack2(a0, a1);
      hi[i] = h;
      lo[i] = pack2(a0 - bflo(h), a1 - bfhi(h));
    }
    u16* dst = Kbuf + (size_t)d * 4096 + tid * 8;
    *(uint4*)dst = make_uint4(hi[0], hi[1], hi[2], hi[3]);
    *(uint4*)(dst + 2048) = make_uint4(lo[0], lo[1], lo[2], lo[3]);
    return;
  }
  b -= K1_KGEN;
  if (b < K1_LN) {
    const int xc = b & 7, kk = b >> 3;
    const int l0 = ((xc * 4 + (kk >> 2)) * 4 + (kk & 3)) * 16;
    uint32_t* tile = (uint32_t*)smem;
    uint32_t v[4][5];
#pragma unroll
    for (int rr = 0; rr < 4; ++rr) {
      const uint32_t* xr = (const uint32_t*)(x + (size_t)(l0 + wave * 4 + rr) * D_);
#pragma unroll
      for (int i = 0; i < 5; ++i) v[rr][i] = xr[lane + 64 * i];
    }
    float mean[4], q[4];
#pragma unroll
    for (int rr = 0; rr < 4; ++rr) {
      float s = 0.f;
#pragma unroll
      for (int i = 0; i < 5; ++i) s += bflo(v[rr][i]) + bfhi(v[rr][i]);
      mean[rr] = wave_sum(s) * (1.f / D_);
    }
#pragma unroll
    for (int rr = 0; rr < 4; ++rr) {
      float s = 0.f;
#pragma unroll
      for (int i = 0; i < 5; ++i) {
        float a = bflo(v[rr][i]) - mean[rr], c = bfhi(v[rr][i]) - mean[rr];
        s += a * a + c * c;
      }
      q[rr] = wave_sum(s);
    }
#pragma unroll
    for (int rr = 0; rr < 4; ++rr) {
      const int row = wave * 4 + rr;
      const float mb = bfr(mean[rr]);
      const float vb = bfr(q[rr] * (1.f / D_));
      const float sd = bfr(sqrtf(bfr(vb + 1e-5f)));
      const int l = l0 + row, r = l & 127;
      uint32_t* xo = (uint32_t*)xn + ((size_t)((l >> 7) * 10 + (lane >> 5)) * 128 + r) * 32 +
                     ((((lane >> 2) & 7) ^ ((r >> 1) & 7)) * 4) + (lane & 3);
#pragma unroll
      for (int i = 0; i < 5; ++i) {
        float a = bfr(bflo(v[rr][i]) - mb) / sd, c = bfr(bfhi(v[rr][i]) - mb) / sd;
        uint32_t o = pack2(a, c);
        xo[i * 2 * 128 * 32] = o;
        tile[row * 324 + lane + 64 * i] = o;
      }
    }
    __syncthreads();
    const u16* t16 = (const u16*)smem;
    for (int d = tid; d < D_; d += 256) {
      uint32_t o[8];
#pragma unroll
      for (int i = 0; i < 8; ++i)
        o[i] = (uint32_t)t16[(2 * i) * 648 + d] | ((uint32_t)t16[(2 * i + 1) * 648 + d] << 16);
      uint4* dst = (uint4*)(xnT + (size_t)d * L_ + l0);
      dst[0] = make_uint4(o[0], o[1], o[2], o[3]);
      dst[1] = make_uint4(o[4], o[5], o[6], o[7]);
    }
    return;
  }
  b -= K1_LN;
  if (b == 2 * K1_T1) {
    for (int i = tid; i < 2 * TB_STRIDE; i += 256) {
      const int sg = i >= TB_STRIDE;
      int m = i - sg * TB_STRIDE;
      if (m > TB_N - 1) m = TB_N - 1;
      const u16 bits = (u16)((TB_LO + m) | (sg << 15));
      tbl[i] = f2bf(tanh_sp_bf(bf2f(bits)));
    }
    return;
  }
  {
    const u16* src; u16* dst; int R, C;
    const bool first = b < K1_T1;
    if (first) { src = w1; dst = w1T; R = D_; C = F_; }
    else { b -= K1_T1; src = w2; dst = w2T; R = F_; C = D_; }
    const int tc = C / 64;
    const int r0 = (b / tc) * 64, c0 = (b % tc) * 64;
    u16* t16 = (u16*)smem;
#pragma unroll
    for (int i = 0; i < 2; ++i) {
      int rr = (tid >> 3) + 32 * i, ch = tid & 7;
      uint4 v = *(const uint4*)(src + (size_t)(r0 + rr) * C + c0 + ch * 8);
      *(uint4*)(t16 + rr * 72 + ch * 8) = v;
    }
    __syncthreads();
#pragma unroll
    for (int i = 0; i < 2; ++i) {
      int cc = (tid >> 3) + 32 * i, ch = tid & 7;
      uint32_t o[4];
#pragma unroll
      for (int k = 0; k < 4; ++k)
        o[k] = (uint32_t)t16[(ch * 8 + 2 * k) * 72 + cc] | ((uint32_t)t16[(ch * 8 + 2 * k + 1) * 72 + cc] << 16);
      const int n = c0 + cc, kt = r0 >> 6;
      size_t off;
      if (first) {
        const int tn = n / 112, r2 = n - tn * 112;
        const int sw = (r2 < 96) ? (((r2 >> 1) & 1) | (((r2 >> 3) & 3) << 1)) : ((r2 >> 1) & 7);
        off = ((size_t)(tn * 10 + kt) * 112 + r2) * 64 + ((ch ^ sw) * 8);
      } else {
        const int nt = n / 80, r = n - nt * 80;
        off = ((size_t)(nt * 27 + kt) * 80 + r) * 64 + ((ch ^ (r & 7)) * 8);
      }
      *(uint4*)(dst + off) = make_uint4(o[0], o[1], o[2], o[3]);
    }
  }
}

__global__ __launch_bounds__(256) void k1_prep(
    const u16* __restrict__ x, const u16* __restrict__ logAr, const u16* __restrict__ Aim,
    const u16* __restrict__ Cre, const u16* __restrict__ Cim, const u16* __restrict__ logdt,
    const u16* __restrict__ w1, const u16* __restrict__ w2,
    u16* __restrict__ xn, u16* __restrict__ xnT, u16* __restrict__ Kbuf,
    u16* __restrict__ w1T, u16* __restrict__ w2T, u16* __restrict__ tbl) {
  TS_BEGIN(0)
  k1_body(x, logAr, Aim, Cre, Cim, logdt, w1, w2, xn, xnT, Kbuf, w1T, w2T, tbl);
  TS_END(0)
}

constexpr int G1_STAGE = 240 * 128;
constexpr int K2_U = 3 * G1_STAGE;
constexpr int K2_KM = K2_U + 4 * 4160;
constexpr int K2_P = K2_KM + 2 * 4608;
constexpr int K2_CNT = K2_P + 32768;
constexpr int K2_LDS = K2_CNT + 64 + 256;

typedef __attribute__((address_space(3))) const char* lds_cptr;
typedef short v4i16_t __attribute__((ext_vector_type(4)));
__device__ __forceinline__ v4i16_t trd(lds_cptr p) {
  return __builtin_amdgcn_ds_read_tr16_b64_v4i16((__attribute__((address_space(3))) v4i16_t*)p);
}
__device__ __forceinline__ bf16x8 rd16(lds_cptr p) { return *(const __attribute__((address_space(3))) bf16x8*)p; }
#define SBAR() __builtin_amdgcn_sched_barrier(0)

__device__ __forceinline__ void gbar(uint32_t* cnt, uint32_t target, int lane) {
  const uint32_t addr = (uint32_t)(size_t)(lds_cptr)(const char*)cnt;
  asm volatile("s_waitcnt lgkmcnt(0)" ::: "memory");
  if (lane == 0) asm volatile("ds_add_u32 %0, %1" ::"v"(addr), "v"(1u) : "memory");
  while (true) {
    uint32_t c;
    asm volatile("ds_read_b32 %0, %1\n\ts_waitcnt lgkmcnt(0)" : "=v"(c) : "v"(addr) : "memory");
    if ((uint32_t)__builtin_amdgcn_readfirstlane(c) >= target) break;
    __builtin_amdgcn_s_sleep(1);
  }
}

template <int MASK>
__device__ __forceinline__ void conv_mfma(f32x4 (&acc)[8], lds_cptr ka, lds_cptr ub) {
  constexpr int T = (MASK & 0x80) ? 7 : 5;
  bf16x8 ah[2][8], al[2][8];
  v4i16_t bx[2], by[2];
#define CAT(x, y) __builtin_shufflevector(x, y, 0, 1, 2, 3, 4, 5, 6, 7)
  bx[0] = trd(ub + 8); by[0] = trd(ub);
#pragma unroll
  for (int b = 0; b <= T; ++b) { ah[0][b] = rd16(ka + 256 + 512 * b); al[0][b] = rd16(ka + 256 + 512 * b + 4608); }
  SBAR();
#pragma unroll
  for (int rr = 0; rr < 2; ++rr) {
#pragma unroll
    for (int a = 0; a <= T; ++a) {
      const int cur = (rr * (T + 1) + a) & 1, nxt = cur ^ 1;
      const bf16x8 bf = CAT(bx[cur], by[cur]);
      int cnt = 0, pend = -1;
#pragma unroll
      for (int bb = 0; bb <= T - a; ++bb) {
        const int b = (rr == 1 && a == 0) ? T - bb : bb;
        if (!((MASK >> (a + b)) & 1)) continue;
        acc[a + b] = __builtin_amdgcn_mfma_f32_16x16x32_bf16(ah[rr][b], bf, acc[a + b], 0, 0, 0);
        if (cnt == 0) {
          if (a < T) { bx[nxt] = trd(ub + rr * 256 + 512 * (a + 1) + 8); by[nxt] = trd(ub + rr * 256 + 512 * (a + 1)); }
          else if (rr == 0) { bx[nxt] = trd(ub + 256 + 8); by[nxt] = trd(ub + 256); }
        }
        SBAR();
        if (pend >= 0) {
          acc[a + pend] = __builtin_amdgcn_mfma_f32_16x16x32_bf16(al[rr][pend], bf, acc[a + pend], 0, 0, 0);
          SBAR();
        }
        pend = b; ++cnt;
      }
      acc[a + pend] = __builtin_amdgcn_mfma_f32_16x16x32_bf16(al[rr][pend], bf, acc[a + pend], 0, 0, 0);
      if (rr == 0) { ah[1][T - a] = rd16(ka + 512 * (T - a)); al[1][T - a] = rd16(ka + 512 * (T - a) + 4608); }
      SBAR();
    }
  }
#undef CAT
}

__device__ __forceinline__ void k2_body(
    const u16* __restrict__ xn, const u16* __restrict__ xnT, const u16* __restrict__ Kbuf,
    const u16* __restrict__ Dskip, const u16* __restrict__ w1T, const u16* __restrict__ b1,
    const u16* __restrict__ tbl, u16* __restrict__ act, u16* __restrict__ AoutT) {
  __shared__ __attribute__((aligned(16))) char smem[K2_LDS];
  const int tid = threadIdx.x, lane = tid & 63;
  const int wave = __builtin_amdgcn_readfirstlane(tid >> 6);
  const int bid = blockIdx.x;
  uint32_t* cnt = (uint32_t*)(smem + K2_CNT);
  if (wave < 4) {
    const int xc = bid & 7, jj = bid >> 3;
    const int tm = (xc & 3) * 4 + (jj & 3), tn = (xc >> 2) * 8 + (jj >> 2);
    const int m0 = tm * 128, n0 = tn * 112;
    const u16* gsrc[8];
#pragma unroll
    for (int i = 0; i < 8; ++i) {
      int ch = wave + 4 * i;
      if (ch > 29) ch = 29;
      if (ch < 16) {
        gsrc[i] = xn + (size_t)tm * 10 * 8192 + ch * 512 + lane * 8;
      } else {
        int r2 = (ch - 16) * 8 + (lane >> 3);
        if (n0 + r2 > F_ - 1) r2 = 0;
        gsrc[i] = w1T + (size_t)tn * 10 * 7168 + r2 * 64 + (lane & 7) * 8;
      }
    }
#define KSTR(i) ((wave + 4 * (i) < 16) ? 8192 : 7168)
    auto stage = [&](int kt, int buf) {
#pragma unroll
      for (int i = 0; i < 7; ++i)
        __builtin_amdgcn_global_load_lds((const uint32_t*)(gsrc[i] + kt * KSTR(i)),
                                         (uint32_t*)(smem + buf * G1_STAGE + (wave + 4 * i) * 1024), 16, 0, 0);
      if (wave < 2)
        __builtin_amdgcn_global_load_lds((const uint32_t*)(gsrc[7] + kt * KSTR(7)),
                                         (uint32_t*)(smem + buf * G1_STAGE + (wave + 28) * 1024), 16, 0, 0);
    };
    stage(0, 0);
    stage(1, 1);
    const int fr = lane & 15, fq = lane >> 4;
    uint4 bb[3]; uint2 bb3;
#pragma unroll
    for (int p = 0; p < 3; ++p) {
      int n = n0 + 32 * p + 8 * fq;
      if (n > F_ - 8) n = F_ - 8;
      bb[p] = *(const uint4*)(b1 + n);
    }
    {
      int n = n0 + 96 + 4 * fq;
      if (n > F_ - 4) n = F_ - 4;
      bb3 = *(const uint2*)(b1 + n);
    }
    STAMP(0)
    if (tid < 16) cnt[tid] = 0;
    __syncthreads();
    STAMP(1)
    int aoff[7], boff[2];
#pragma unroll
    for (int i = 0; i < 7; ++i) {
      const int r2 = (i < 6) ? (32 * (i >> 1) + 8 * (fr >> 2) + 4 * (i & 1) + (fr & 3)) : (96 + fr);
      const int sw = (i < 6) ? (((r2 >> 1) & 1) | (((r2 >> 3) & 3) << 1)) : ((r2 >> 1) & 7);
      aoff[i] = (128 + r2) * 128 + ((fq ^ sw) * 16);
    }
#pragma unroll
    for (int j = 0; j < 2; ++j) {
      const int row = wave * 32 + j * 16 + fr;
      boff[j] = row * 128 + ((fq ^ ((row >> 1) & 7)) * 16);
    }
    f32x4 acc[7][2];
#pragma unroll
    for (int i = 0; i < 7; ++i)
#pragma unroll
      for (int j = 0; j < 2; ++j) acc[i][j] = (f32x4){0.f, 0.f, 0.f, 0.f};
    int slot = 0;
    auto kstep = [&](auto dma_tag, int kt, int ns) {
      constexpr bool DMA = decltype(dma_tag)::value;
      const char* base = smem + slot * G1_STAGE;
      char* dst = smem + ns * G1_STAGE + wave * 1024;
      const int ko = kt + 2;
      bf16x8 af[2][7], bfm[2][2];
#pragma unroll
      for (int j = 0; j < 2; ++j) bfm[0][j] = *(const bf16x8*)(base + boff[j]);
#pragma unroll
      for (int i = 0; i < 7; ++i) af[0][i] = *(const bf16x8*)(base + aoff[i]);
      SBAR();
#pragma unroll
      for (int ks = 0; ks < 2; ++ks) {
#pragma unroll
        for (int i = 0; i < 7; ++i) {
          acc[i][0] = __builtin_amdgcn_mfma_f32_16x16x32_bf16(af[ks][i], bfm[ks][0], acc[i][0], 0, 0, 0);
          if (ks == 0) {
            if (i < 2) bfm[1][i] = *(const bf16x8*)(base + (boff[i] ^ 64));
            af[1][i] = *(const bf16x8*)(base + (aoff[i] ^ 64));
          }
          SBAR();
          acc[i][1] = __builtin_amdgcn_mfma_f32_16x16x32_bf16(af[ks][i], bfm[ks][1], acc[i][1], 0, 0, 0);
          const int g = ks * 7 + i;
          if (DMA && (g & 1) == 0)
            __builtin_amdgcn_global_load_lds((const uint32_t*)(gsrc[g >> 1] + ko * KSTR(g >> 1)), (uint32_t*)(dst + (g >> 1) * 4096), 16, 0, 0);
          if (DMA && g == 13 && wave < 2)
            __builtin_amdgcn_global_load_lds((const uint32_t*)(gsrc[7] + ko * KSTR(7)), (uint32_t*)(dst + 7 * 4096), 16, 0, 0);
          SBAR();
        }
      }
    };
    for (int kt = 0; kt < 10; ++kt) {
      if (kt < 9) {
        if (wave < 2) asm volatile("s_waitcnt vmcnt(8)" ::: "memory");
        else asm volatile("s_waitcnt vmcnt(7)" ::: "memory");
      } else {
        asm volatile("s_waitcnt vmcnt(0)" ::: "memory");
      }
      gbar(cnt, 4 * (kt + 1), lane);
      STAMPK(2 + kt)
      int ns = slot + 2; if (ns >= 3) ns -= 3;
      if (kt < 8) kstep(std::true_type{}, kt, ns);
      else {
        if (kt == 8) {
#pragma unroll
          for (int i = 0; i < 3; ++i) {
            const int ch = wave + 4 * i;
            if (ch < 9)
              __builtin_amdgcn_global_load_lds((const uint32_t*)(tbl + ch * 512 + lane * 8),
                                               (uint32_t*)(smem + ns * G1_STAGE + ch * 1024), 16, 0, 0);
          }
        }
        kstep(std::false_type{}, kt, ns);
      }
      slot = slot + 1; if (slot >= 3) slot = 0;
    }
    STAMP(12)
    const u16* tb = (const u16*)(smem + 1 * G1_STAGE);
    auto act2 = [&](float a0, float a1, uint32_t bias) -> uint32_t {
      uint32_t r = pack2(a0, a1);
      uint32_t h = pack2(bflo(r) + bflo(bias), bfhi(r) + bfhi(bias));
      uint32_t m0_ = h & 0x7fffu, m1_ = (h >> 16) & 0x7fffu;
      m0_ = min(max(m0_, (uint32_t)TB_LO), (uint32_t)TB_HI) - TB_LO + ((h >> 15) & 1u) * TB_STRIDE;
      m1_ = min(max(m1_, (uint32_t)TB_LO), (uint32_t)TB_HI) - TB_LO + (h >> 31) * TB_STRIDE;
      float t0 = bf2f(tb[m0_]), t1 = bf2f(tb[m1_]);
      return pack2(bflo(h) * t0, bfhi(h) * t1);
    };
#pragma unroll
    for (int j = 0; j < 2; ++j) {
      const int m = m0 + wave * 32 + j * 16 + fr;
      u16* arow = act + ((size_t)(m >> 6) * 27 * 64 + (m & 63)) * 64;
#pragma unroll
      for (int p = 0; p < 3; ++p) {
        const int n = n0 + 32 * p + 8 * fq;
        uint4 o;
        o.x = act2(acc[2 * p][j][0], acc[2 * p][j][1], bb[p].x);
        o.y = act2(acc[2 * p][j][2], acc[2 * p][j][3], bb[p].y);
        o.z = act2(acc[2 * p + 1][j][0], acc[2 * p + 1][j][1], bb[p].z);
        o.w = act2(acc[2 * p + 1][j][2], acc[2 * p + 1][j][3], bb[p].w);
        if (n < F_) *(uint4*)(arow + (n >> 6) * 4096 + ((((n >> 3) & 7) ^ (m & 7)) * 8)) = o;
      }
      {
        const int n = n0 + 96 + 4 * fq;
        uint2 o;
        o.x = act2(acc[6][j][0], acc[6][j][1], bb3.x);
        o.y = act2(acc[6][j][2], acc[6][j][3], bb3.y);
        if (n < F_) *(uint2*)(arow + (n >> 6) * 4096 + ((((n >> 3) & 7) ^ (m & 7)) * 8) + (n & 7)) = o;
      }
    }
    STAMP(13)
    STAMPK_DUMP
    return;
  }
  const int cw = wave - 4, ct = tid - 256;
  const int ch2 = (bid < 128) ? 512 + bid : 384 + bid;
  uint4 kr0, kr1, uv; uint2 up; u16 dk;
  auto fetch = [&](int d) {
    const u16* us = xnT + (size_t)d * L_ + ct * 8;
    uv = *(const uint4*)us;
    up = *(const uint2*)(us - (ct ? 4 : 0));
    const uint4* ks = (const uint4*)(Kbuf + (size_t)d * 4096);
    kr0 = ks[ct];
    kr1 = ks[ct + 256];
    dk = Dskip[d];
  };
  auto put = [&]() {
    const uint32_t pm2 = ct ? up.x : 0u, pm1 = ct ? up.y : 0u;
    const uint32_t am = __builtin_amdgcn_alignbit(pm1, pm2, 16), a0 = __builtin_amdgcn_alignbit(uv.x, pm1, 16);
    const uint32_t a1 = __builtin_amdgcn_alignbit(uv.y, uv.x, 16), a2 = __builtin_amdgcn_alignbit(uv.z, uv.y, 16);
    const uint32_t a3 = __builtin_amdgcn_alignbit(uv.w, uv.z, 16);
    char* ud = smem + K2_U + 64 + ct * 16;
    *(uint4*)(ud) = uv;
    *(uint4*)(ud + 4160) = make_uint4(a0, a1, a2, a3);
    *(uint4*)(ud + 2 * 4160) = make_uint4(pm1, uv.x, uv.y, uv.z);
    *(uint4*)(ud + 3 * 4160) = make_uint4(am, a0, a1, a2);
    *(uint4*)(smem + K2_KM + 512 + ct * 16) = kr0;
    *(uint4*)(smem + K2_KM + 4608 + 512 + ct * 16) = kr1;
  };
  fetch(bid);
  if (ct < 64) *(uint4*)(smem + K2_KM + (ct >> 5) * 4608 + (ct & 31) * 16) = make_uint4(0, 0, 0, 0);
  else if (ct < 80) *(uint4*)(smem + K2_U + ((ct >> 2) & 3) * 4160 + (ct & 3) * 16) = make_uint4(0, 0, 0, 0);
  __syncthreads();
  const int fr = lane & 15, kg = lane >> 4;
  lds_cptr ub = (lds_cptr)(smem + K2_U + ((lane >> 2) & 3) * 4160 + 32 * (kg >> 1) - 16 * (kg & 1) + (lane & 3) * 8 + 56 + 64 * cw);
  lds_cptr ka = (lds_cptr)(smem + K2_KM + 512 + 32 * fr - 32 * (kg >> 1) + 16 * (kg & 1) - 64 * cw - 256);
  float* P = (float*)(smem + K2_P);
  uint32_t ph = 0;
  put();
  uint4 ucur = uv; u16 dcur = dk;
#pragma unroll 1
  for (int task = 0; task < 3; ++task) {
    const int d = (task == 0) ? bid : (task == 1) ? bid + 256 : ch2;
    STAMP(16 + task * 5)
    ph += 4; gbar(cnt + 8, ph, lane);
    STAMP(17 + task * 5)
    if (task < 2) fetch(task == 0 ? bid + 256 : ch2);
    f32x4 acc[8];
#pragma unroll
    for (int i = 0; i < 8; ++i) acc[i] = (f32x4){0.f, 0.f, 0.f, 0.f};
    const int mask = (task < 2) ? 0xFF : (bid < 128 ? 0xC3 : 0x3C);
    if (mask == 0xFF) conv_mfma<0xFF>(acc, ka, ub);
    else if (mask == 0xC3) conv_mfma<0xC3>(acc, ka, ub);
    else conv_mfma<0x3C>(acc, ka, ub);
    STAMP(18 + task * 5)
    {
      float* pa = P + cw * 2048 + kg * 64 + fr;
#pragma unroll
      for (int i = 0; i < 8; ++i)
        if ((mask >> i) & 1) {
#pragma unroll
          for (int r = 0; r < 4; ++r) pa[i * 256 + r * 16] = acc[i][r];
        }
    }
    ph += 4; gbar(cnt + 8, ph, lane);
    STAMP(19 + task * 5)
    if (task < 2) put();
    if ((mask >> (ct >> 5)) & 1) {
      const f32x4* p0 = (const f32x4*)(P + ct * 8);
      float y[8];
#pragma unroll
      for (int i = 0; i < 2; ++i) {
        f32x4 s = (p0[i] + p0[512 + i]) + (p0[1024 + i] + p0[1536 + i]);
        y[4 * i] = s[0]; y[4 * i + 1] = s[1]; y[4 * i + 2] = s[2]; y[4 * i + 3] = s[3];
      }
      const float dsk = bf2f(dcur);
      const uint32_t uu[4] = {ucur.x, ucur.y, ucur.z, ucur.w};
      uint32_t o4[4];
#pragma unroll
      for (int i = 0; i < 4; ++i) {
        float r0 = bfr(y[2 * i]) + bfr(bflo(uu[i]) * dsk);
        float r1 = bfr(y[2 * i + 1]) + bfr(bfhi(uu[i]) * dsk);
        o4[i] = pack2(r0, r1);
      }
      *(uint4*)(AoutT + (size_t)d * L_ + ct * 8) = make_uint4(o4[0], o4[1], o4[2], o4[3]);
    }
    ucur = uv; dcur = dk;
    STAMP(20 + task * 5)
  }
}

__global__ __launch_bounds__(512, 1) void k2_conv_gemm1(
    const u16* __restrict__ xn, const u16* __restrict__ xnT, const u16* __restrict__ Kbuf,
    const u16* __restrict__ Dskip, const u16* __restrict__ w1T, const u16* __restrict__ b1,
    const u16* __restrict__ tbl, u16* __restrict__ act, u16* __restrict__ AoutT) {
  TS_BEGIN(1)
  k2_body(xn, xnT, Kbuf, Dskip, w1T, b1, tbl, act, AoutT);
  TS_END(1)
}

constexpr int K3_STAGE = 144 * 128;
constexpr int K3_NBUF = 4;
constexpr int K3_AO_OFF = K3_STAGE * K3_NBUF;
constexpr int K3_LDS = K3_AO_OFF + 80 * 128;

__device__ __forceinline__ void k3_body(
    const u16* __restrict__ act, const u16* __restrict__ w2T, const u16* __restrict__ x,
    const u16* __restrict__ AoutT, u16* __restrict__ out) {
  __shared__ __attribute__((aligned(16))) char smem[K3_LDS];
  const int tid = threadIdx.x, lane = tid & 63;
  const int wave = __builtin_amdgcn_readfirstlane(tid >> 6);
  const int bid = blockIdx.x;
  const int xc = bid & 7, jj = bid >> 3;
  const int rb = (xc & 3) * 8 + (jj & 7), nt = (xc >> 2) * 4 + (jj >> 3);
  const int m0 = rb * 64, n0 = nt * 80;
  constexpr int NT = F_ / 64;
  f32x4 acc[5][2];
#pragma unroll
  for (int i = 0; i < 5; ++i)
#pragma unroll
    for (int j = 0; j < 2; ++j) acc[i][j] = (f32x4){0.f, 0.f, 0.f, 0.f};
  const int fr = lane & 15, fq = lane >> 4;
  const int wm = wave & 1, ksp = (wave >> 1) & 1;
  const int em0 = tid / 10, ec0 = (tid % 10) * 8;
  const int em1 = (512 + tid) / 10, ec1 = ((512 + tid) % 10) * 8;
  uint4 xv0, xv1 = make_uint4(0, 0, 0, 0);
  if (wave >= 4) {
    const int lw = wave - 4;
    const u16* gsrc[5];
#pragma unroll
    for (int i = 0; i < 5; ++i) {
      int ch = lw + 4 * i;
      if (ch > 17) ch = 17;
      gsrc[i] = (ch < 8 ? act + (size_t)rb * 27 * 4096 + ch * 512 : w2T + (size_t)nt * 27 * 5120 + (ch - 8) * 512) + lane * 8;
    }
    auto stage = [&](int kt, int buf) {
#pragma unroll
      for (int i = 0; i < 4; ++i)
        __builtin_amdgcn_global_load_lds((const uint32_t*)(gsrc[i] + kt * (i < 2 ? 4096 : 5120)),
                                         (uint32_t*)(smem + buf * K3_STAGE + (lw + 4 * i) * 1024), 16, 0, 0);
      if (lw < 2)
        __builtin_amdgcn_global_load_lds((const uint32_t*)(gsrc[4] + kt * 5120),
                                         (uint32_t*)(smem + buf * K3_STAGE + (lw + 16) * 1024), 16, 0, 0);
    };
    stage(0, 0); stage(1, 1); stage(2, 2);
    for (int kt = 0; kt < NT; ++kt) {
      const int rem = NT - 1 - kt;
      if (rem >= 2) {
        if (lw < 2) asm volatile("s_waitcnt vmcnt(10)" ::: "memory");
        else asm volatile("s_waitcnt vmcnt(8)" ::: "memory");
      } else if (rem == 1) {
        if (lw < 2) asm volatile("s_waitcnt vmcnt(5)" ::: "memory");
        else asm volatile("s_waitcnt vmcnt(4)" ::: "memory");
      } else {
        asm volatile("s_waitcnt vmcnt(0)" ::: "memory");
      }
      __builtin_amdgcn_s_barrier();
      if (kt + 3 < NT) stage(kt + 3, (kt + 3) & 3);
    }
    xv0 = *(const uint4*)(x + (size_t)(m0 + em0) * D_ + n0 + ec0);
  } else {
#pragma unroll
    for (int i = 0; i < 3; ++i) {
      const int ch = wave + 4 * i;
      if (ch < 10) {
        const int row = ch * 8 + (lane >> 3);
        __builtin_amdgcn_global_load_lds((const uint32_t*)(AoutT + (size_t)(n0 + row) * L_ + m0 + (lane & 7) * 8),
                                         (uint32_t*)(smem + K3_AO_OFF + ch * 1024), 16, 0, 0);
      }
    }
    xv0 = *(const uint4*)(x + (size_t)(m0 + em0) * D_ + n0 + ec0);
    if (tid < 128) xv1 = *(const uint4*)(x + (size_t)(m0 + em1) * D_ + n0 + ec1);
    int aoff[5], boff[2];
#pragma unroll
    for (int i = 0; i < 5; ++i) {
      const int row = 64 + i * 16 + fr;
      aoff[i] = row * 128 + (((ksp * 4 + fq) ^ (row & 7)) * 16);
    }
#pragma unroll
    for (int j = 0; j < 2; ++j) {
      const int row = wm * 32 + j * 16 + fr;
      boff[j] = row * 128 + (((ksp * 4 + fq) ^ (row & 7)) * 16);
    }
    for (int kt = 0; kt < NT; ++kt) {
      asm volatile("s_waitcnt lgkmcnt(0)" ::: "memory");
      __builtin_amdgcn_s_barrier();
      const char* base = smem + (kt & 3) * K3_STAGE;
      bf16x8 af[5], bfm[2];
#pragma unroll
      for (int j = 0; j < 2; ++j) bfm[j] = *(const bf16x8*)(base + boff[j]);
#pragma unroll
      for (int i = 0; i < 5; ++i) af[i] = *(const bf16x8*)(base + aoff[i]);
#pragma unroll
      for (int i = 0; i < 5; ++i)
#pragma unroll
        for (int j = 0; j < 2; ++j)
          acc[i][j] = __builtin_amdgcn_mfma_f32_16x16x32_bf16(af[i], bfm[j], acc[i][j], 0, 0, 0);
    }
  }
  __syncthreads();
  float* red = (float*)smem;
  if (wave < 4) {
#pragma unroll
    for (int i = 0; i < 5; ++i)
#pragma unroll
      for (int j = 0; j < 2; ++j) {
        const int m = wm * 32 + j * 16 + fr, n = i * 16 + fq * 4;
        *(f32x4*)(red + (ksp * 64 + m) * 84 + n) = acc[i][j];
      }
  }
  __syncthreads();
  const u16* ao = (const u16*)(smem + K3_AO_OFF);
#pragma unroll
  for (int it = 0; it < 2; ++it) {
    if (it == 1 && tid >= 128) break;
    const int m = it ? em1 : em0, c8 = it ? ec1 : ec0;
    const uint4 xv = it ? xv1 : xv0;
    const f32x4* r0 = (const f32x4*)(red + m * 84 + c8);
    const f32x4* r1 = (const f32x4*)(red + (64 + m) * 84 + c8);
    const f32x4 s0 = r0[0] + r1[0], s1 = r0[1] + r1[1];
    const float s[8] = {s0[0], s0[1], s0[2], s0[3], s1[0], s1[1], s1[2], s1[3]};
    const uint32_t xx[4] = {xv.x, xv.y, xv.z, xv.w};
    uint32_t o[4];
#pragma unroll
    for (int i = 0; i < 4; ++i) {
      float a0 = bf2f(ao[(c8 + 2 * i) * 64 + m]), a1 = bf2f(ao[(c8 + 2 * i + 1) * 64 + m]);
      float v0 = bfr(bflo(xx[i]) + a0) + bfr(s[2 * i]);
      float v1 = bfr(bfhi(xx[i]) + a1) + bfr(s[2 * i + 1]);
      o[i] = pack2(v0, v1);
    }
    *(uint4*)(out + (size_t)(m0 + m) * D_ + n0 + c8) = make_uint4(o[0], o[1], o[2], o[3]);
  }
}

__global__ __launch_bounds__(512, 1) void k3_gemm2(
    const u16* __restrict__ act, const u16* __restrict__ w2T, const u16* __restrict__ x,
    const u16* __restrict__ AoutT, u16* __restrict__ out) {
  TS_BEGIN(2)
  k3_body(act, w2T, x, AoutT, out);
  TS_END(2)
}

}

extern "C" void kernel_launch(void* const* d_in, const int* in_sizes, int n_in,
                              void* d_out, int out_size, void* d_ws, size_t ws_size,
                              hipStream_t stream) {
  const u16* x = (const u16*)d_in[0];
  const u16* logAr = (const u16*)d_in[1];
  const u16* Aim = (const u16*)d_in[2];
  const u16* Cre = (const u16*)d_in[3];
  const u16* Cim = (const u16*)d_in[4];
  const u16* logdt = (const u16*)d_in[5];
  const u16* Dskip = (const u16*)d_in[6];
  const u16* w1 = (const u16*)d_in[8];
  const u16* b1 = (const u16*)d_in[9];
  const u16* w2 = (const u16*)d_in[10];
  u16* out = (u16*)d_out;
  u16* ws = (u16*)d_ws;
  u16* xn = ws;
  u16* xnT = xn + (size_t)L_ * D_;
  u16* Kbuf = xnT + (size_t)L_ * D_;
  u16* w1T = Kbuf + (size_t)D_ * 4096;
  u16* w2T = w1T + (size_t)D_ * 1792;
  u16* act = w2T + (size_t)D_ * F_;
  u16* AoutT = act + (size_t)L_ * F_;
  u16* tbl = AoutT + (size_t)L_ * D_;
  k1_prep<<<K1_BLOCKS, 256, 0, stream>>>(x, logAr, Aim, Cre, Cim, logdt, w1, w2, xn, xnT, Kbuf, w1T, w2T, tbl);
  k2_conv_gemm1<<<256, 512, 0, stream>>>(xn, xnT, Kbuf, Dskip, w1T, b1, tbl, act, AoutT);
  k3_gemm2<<<256, 512, 0, stream>>>(act, w2T, x, AoutT, out);
}
```

```cpp
#include <hip/hip_runtime.h>
#include <hip/hip_bf16.h>
#include <stdint.h>
#include <type_traits>

#define TS_BEGIN(k)
#define TS_END(k)
#define STAMP(i)
#define STAMPK(i)
#define STAMP_DUMP(a, b)

#define DMAV 0

namespace {

constexpr int L_ = 2048, D_ = 640, NS_ = 128, F_ = 1728;

typedef __attribute__((ext_vector_type(8))) short bf16x8;
typedef __attribute__((ext_vector_type(4))) float f32x4;
typedef __attribute__((ext_vector_type(16))) float f32x16;
typedef __attribute__((ext_vector_type(2))) float f32x2;
typedef __attribute__((ext_vector_type(2))) __bf16 bf16x2;
typedef unsigned short u16;

constexpr float LOG2E = 1.4426950408889634f;
constexpr float LN2 = 0.6931471805599453f;
constexpr float INV2PI = 0.15915494309189535f;

__device__ __forceinline__ float bf2f(u16 v) { return __uint_as_float(((uint32_t)v) << 16); }
__device__ __forceinline__ float bflo(uint32_t v) { return __uint_as_float(v << 16); }
__device__ __forceinline__ float bfhi(uint32_t v) { return __uint_as_float(v & 0xffff0000u); }
__device__ __forceinline__ u16 f2bf(float x) {
  __bf16 h = (__bf16)x;
  return __builtin_bit_cast(u16, h);
}
__device__ __forceinline__ float bfr(float x) { return bf2f(f2bf(x)); }
__device__ __forceinline__ uint32_t pack2(float a, float b) {
  f32x2 v = {a, b};
  bf16x2 r = __builtin_convertvector(v, bf16x2);
  return __builtin_bit_cast(uint32_t, r);
}

__device__ __forceinline__ float tanh_sp_bf(float h) {
  float e = bfr(__builtin_amdgcn_exp2f(-fabsf(h) * LOG2E));
  float s = e * (1.f - e * (0.5f - e * (1.f / 3.f)));
  float lg = __builtin_amdgcn_logf(1.f + e) * LN2;
  float l = bfr(e < 0.015625f ? s : lg);
  float sp = bfr(l + fmaxf(h, 0.f));
  float q = __builtin_amdgcn_exp2f(sp * (2.f * LOG2E));
  float t = 1.f - 2.f * __builtin_amdgcn_rcpf(q + 1.f);
  float ts = sp * (1.f - sp * sp * (1.f / 3.f));
  return bfr(sp < 0.03125f ? ts : t);
}
constexpr int TB_LO = 103 << 7, TB_HI = 134 << 7;
constexpr int TB_N = TB_HI - TB_LO + 1;
constexpr int TB_STRIDE = 4096;

template <int N, int I = 0, class Fn>
__device__ __forceinline__ void static_for(Fn&& f) {
  if constexpr (I < N) { f(std::integral_constant<int, I>{}); static_for<N, I + 1>(f); }
}

template <int CTRL>
__device__ __forceinline__ float dppf(float v) {
  return __builtin_bit_cast(float, __builtin_amdgcn_update_dpp(0, __builtin_bit_cast(int, v), CTRL, 0xF, 0xF, true));
}
__device__ __forceinline__ float rdl(float v, int l) {
  return __builtin_bit_cast(float, __builtin_amdgcn_readlane(__builtin_bit_cast(int, v), l));
}
__device__ __forceinline__ float wave_sum(float v) {
  v += dppf<0xB1>(v);
  v += dppf<0x4E>(v);
  v += dppf<0x141>(v);
  v += dppf<0x140>(v);
  return (rdl(v, 0) + rdl(v, 16)) + (rdl(v, 32) + rdl(v, 48));
}

constexpr int K1_KGEN = D_;
constexpr int K1_LN = L_ / 16;
constexpr int K1_T1 = (D_ / 64) * (F_ / 64);
constexpr int K1_BLOCKS = K1_KGEN + K1_LN + 2 * K1_T1 + 1;

__device__ __forceinline__ void k1_body(
    const u16* __restrict__ x, const u16* __restrict__ logAr, const u16* __restrict__ Aim,
    const u16* __restrict__ Cre, const u16* __restrict__ Cim, const u16* __restrict__ logdt,
    const u16* __restrict__ w1, const u16* __restrict__ w2,
    u16* __restrict__ xn, u16* __restrict__ xnT, u16* __restrict__ Kbuf,
    u16* __restrict__ w1T, u16* __restrict__ w2T, u16* __restrict__ tbl) {
  __shared__ __attribute__((aligned(16))) char smem[16 * 1296];
  const int tid = threadIdx.x, lane = tid & 63, wave = tid >> 6;
  int b = blockIdx.x;
  if (b < K1_KGEN) {
    const int d = b;
    float4* prm = (float4*)smem;
    float* part = (float*)(smem + 2048);
    float* kp = (float*)(smem + 2048 + 12288);
    if (tid < NS_) {
      float dt = bfr(expf(bf2f(logdt[d])));
      float ar = -bfr(expf(bf2f(logAr[d * NS_ + tid]))) * dt;
      float ai = bf2f(Aim[d * NS_ + tid]) * dt;
      float cr = bf2f(Cre[d * NS_ + tid]) * dt;
      float ci = bf2f(Cim[d * NS_ + tid]) * dt;
      prm[tid] = make_float4(ar * LOG2E, ai * INV2PI, 2.f * cr, 2.f * ci);
    }
    __syncthreads();
    const int r = lane & 15, kq = lane >> 4;
    float p0 = (float)r;
    float p1 = (r < 8) ? (float)(256 + 2 * r) : (float)(512 + 4 * ((r - 8) & 3) + 256 * ((r - 8) >> 2));
    float p2 = (r < 8) ? (float)(1024 + 8 * (r & 1) + 256 * (r >> 1)) : 2048.f;
    float pb = (float)(16 * r);
    f32x4 acc0 = {0.f, 0.f, 0.f, 0.f}, acc1 = acc0, acc2 = acc0;
#pragma unroll 2
    for (int g = 0; g < 8; ++g) {
      const float4 P = prm[wave * 32 + g * 4 + kq];
      float mb = __builtin_amdgcn_exp2f(P.x * pb);
      float rb = __builtin_amdgcn_fractf(P.y * pb);
      float fr_ = mb * __builtin_amdgcn_cosf(rb), fi_ = mb * __builtin_amdgcn_sinf(rb);
      float br = P.z * fr_ - P.w * fi_;
      float bi = -(P.z * fi_ + P.w * fr_);
      {
        float m = __builtin_amdgcn_exp2f(P.x * p0);
        float rv = __builtin_amdgcn_fractf(P.y * p0);
        float er = m * __builtin_amdgcn_cosf(rv), ei = m * __builtin_amdgcn_sinf(rv);
        acc0 = __builtin_amdgcn_mfma_f32_16x16x4f32(er, br, acc0, 0, 0, 0);
        acc0 = __builtin_amdgcn_mfma_f32_16x16x4f32(ei, bi, acc0, 0, 0, 0);
      }
      {
        float m = __builtin_amdgcn_exp2f(P.x * p1);
        float rv = __builtin_amdgcn_fractf(P.y * p1);
        float er = m * __builtin_amdgcn_cosf(rv), ei = m * __builtin_amdgcn_sinf(rv);
        acc1 = __builtin_amdgcn_mfma_f32_16x16x4f32(er, br, acc1, 0, 0, 0);
        acc1 = __builtin_amdgcn_mfma_f32_16x16x4f32(ei, bi, acc1, 0, 0, 0);
      }
      {
        float m = __builtin_amdgcn_exp2f(P.x * p2);
        float rv = __builtin_amdgcn_fractf(P.y * p2);
        float er = m * __builtin_amdgcn_cosf(rv), ei = m * __builtin_amdgcn_sinf(rv);
        acc2 = __builtin_amdgcn_mfma_f32_16x16x4f32(er, br, acc2, 0, 0, 0);
        acc2 = __builtin_amdgcn_mfma_f32_16x16x4f32(ei, bi, acc2, 0, 0, 0);
      }
    }
#pragma unroll
    for (int i = 0; i < 4; ++i) {
      part[((wave * 3 + 0) * 16 + kq * 4 + i) * 16 + r] = acc0[i];
      part[((wave * 3 + 1) * 16 + kq * 4 + i) * 16 + r] = acc1[i];
      part[((wave * 3 + 2) * 16 + kq * 4 + i) * 16 + r] = acc2[i];
    }
    __syncthreads();
    {
      int o;
      o = (tid & 15) * 16 + (tid >> 4);
      kp[tid] = (part[o] + part[o + 768]) + (part[o + 1536] + part[o + 2304]);
      if (tid < 128) { o = (16 + (tid & 7)) * 16 + (tid >> 3); }
      else { int m = tid - 128, j2 = m >> 2; o = (16 + 8 + (j2 >> 4) * 4 + (m & 3)) * 16 + (j2 & 15); }
      kp[256 + tid] = (part[o] + part[o + 768]) + (part[o + 1536] + part[o + 2304]);
      if (tid <= 128) {
        int j2 = tid >> 1;
        o = (tid == 128) ? (32 + 8) * 16 : (32 + (j2 >> 4) * 2 + (tid & 1)) * 16 + (j2 & 15);
        kp[512 + tid] = (part[o] + part[o + 768]) + (part[o + 1536] + part[o + 2304]);
      }
    }
    __syncthreads();
    float kv[8];
    {
      const int j0 = tid * 8;
      const int s = (j0 < 256) ? 0 : (j0 < 512) ? 1 : (j0 < 1024) ? 2 : 3;
      const int x0 = s ? j0 - (128 << s) : j0;
      const int bidx = s ? 128 + 128 * s : 0;
      const int hm1 = s ? (1 << (s - 1)) - 1 : 0;
#pragma unroll
      for (int jj = 0; jj < 8; ++jj) {
        const int xx = x0 + jj;
        const int q = s ? ((xx + hm1 + ((xx >> s) & 1)) >> s) : xx;
        kv[jj] = kp[bidx + q];
      }
    }
    uint32_t hi[4], lo[4];
#pragma unroll
    for (int i = 0; i < 4; ++i) {
      float a0 = kv[2 * i], a1 = kv[2 * i + 1];
      uint32_t h = pack2(a0, a1);
      hi[i] = h;
      lo[i] = pack2(a0 - bflo(h), a1 - bfhi(h));
    }
    u16* dst = Kbuf + (size_t)d * 4096 + tid * 8;
    *(uint4*)dst = make_uint4(hi[0], hi[1], hi[2], hi[3]);
    *(uint4*)(dst + 2048) = make_uint4(lo[0], lo[1], lo[2], lo[3]);
    return;
  }
  b -= K1_KGEN;
  if (b < K1_LN) {
    const int xc = b & 7, kk = b >> 3;
    const int l0 = ((xc * 4 + (kk >> 2)) * 4 + (kk & 3)) * 16;
    uint32_t* tile = (uint32_t*)smem;
    uint32_t v[4][5];
#pragma unroll
    for (int rr = 0; rr < 4; ++rr) {
      const uint32_t* xr = (const uint32_t*)(x + (size_t)(l0 + wave * 4 + rr) * D_);
#pragma unroll
      for (int i = 0; i < 5; ++i) v[rr][i] = xr[lane + 64 * i];
    }
    float mean[4], q[4];
#pragma unroll
    for (int rr = 0; rr < 4; ++rr) {
      float s = 0.f;
#pragma unroll
      for (int i = 0; i < 5; ++i) s += bflo(v[rr][i]) + bfhi(v[rr][i]);
      mean[rr] = wave_sum(s) * (1.f / D_);
    }
#pragma unroll
    for (int rr = 0; rr < 4; ++rr) {
      float s = 0.f;
#pragma unroll
      for (int i = 0; i < 5; ++i) {
        float a = bflo(v[rr][i]) - mean[rr], c = bfhi(v[rr][i]) - mean[rr];
        s += a * a + c * c;
      }
      q[rr] = wave_sum(s);
    }
#pragma unroll
    for (int rr = 0; rr < 4; ++rr) {
      const int row = wave * 4 + rr;
      const float mb = bfr(mean[rr]);
      const float vb = bfr(q[rr] * (1.f / D_));
      const float sd = bfr(sqrtf(bfr(vb + 1e-5f)));
      const int l = l0 + row, r = l & 127;
      uint32_t* xo = (uint32_t*)xn + ((size_t)((l >> 7) * 10 + (lane >> 5)) * 128 + r) * 32 +
                     ((((lane >> 2) & 7) ^ ((r >> 1) & 7)) * 4) + (lane & 3);
#pragma unroll
      for (int i = 0; i < 5; ++i) {
        float a = bfr(bflo(v[rr][i]) - mb) / sd, c = bfr(bfhi(v[rr][i]) - mb) / sd;
        uint32_t o = pack2(a, c);
        xo[i * 2 * 128 * 32] = o;
        tile[row * 324 + lane + 64 * i] = o;
      }
    }
    __syncthreads();
    const u16* t16 = (const u16*)smem;
    for (int d = tid; d < D_; d += 256) {
      uint32_t o[8];
#pragma unroll
      for (int i = 0; i < 8; ++i)
        o[i] = (uint32_t)t16[(2 * i) * 648 + d] | ((uint32_t)t16[(2 * i + 1) * 648 + d] << 16);
      uint4* dst = (uint4*)(xnT + (size_t)d * L_ + l0);
      dst[0] = make_uint4(o[0], o[1], o[2], o[3]);
      dst[1] = make_uint4(o[4], o[5], o[6], o[7]);
    }
    return;
  }
  b -= K1_LN;
  if (b == 2 * K1_T1) {
    for (int i = tid; i < 2 * TB_STRIDE; i += 256) {
      const int sg = i >= TB_STRIDE;
      int m = i - sg * TB_STRIDE;
      if (m > TB_N - 1) m = TB_N - 1;
      const u16 bits = (u16)((TB_LO + m) | (sg << 15));
      const float hv = bf2f(bits);
      tbl[i] = f2bf(hv * tanh_sp_bf(hv));
    }
    return;
  }
  {
    const u16* src; u16* dst; int R, C;
    const bool first = b < K1_T1;
    if (first) { src = w1; dst = w1T; R = D_; C = F_; }
    else { b -= K1_T1; src = w2; dst = w2T; R = F_; C = D_; }
    const int tc = C / 64;
    const int r0 = (b / tc) * 64, c0 = (b % tc) * 64;
    u16* t16 = (u16*)smem;
#pragma unroll
    for (int i = 0; i < 2; ++i) {
      int rr = (tid >> 3) + 32 * i, ch = tid & 7;
      uint4 v = *(const uint4*)(src + (size_t)(r0 + rr) * C + c0 + ch * 8);
      *(uint4*)(t16 + rr * 72 + ch * 8) = v;
    }
    __syncthreads();
#pragma unroll
    for (int i = 0; i < 2; ++i) {
      int cc = (tid >> 3) + 32 * i, ch = tid & 7;
      uint32_t o[4];
#pragma unroll
      for (int k = 0; k < 4; ++k)
        o[k] = (uint32_t)t16[(ch * 8 + 2 * k) * 72 + cc] | ((uint32_t)t16[(ch * 8 + 2 * k + 1) * 72 + cc] << 16);
      const int n = c0 + cc, kt = r0 >> 6;
      size_t off;
      if (first) {
        const int tn = n / 112, r2 = n - tn * 112;
        const int sw = (r2 < 96) ? (((r2 >> 1) & 1) | (((r2 >> 3) & 3) << 1)) : ((r2 >> 1) & 7);
        off = ((size_t)(tn * 10 + kt) * 112 + r2) * 64 + ((ch ^ sw) * 8);
      } else {
        const int nt = n / 80, r = n - nt * 80;
        off = ((size_t)(nt * 27 + kt) * 80 + r) * 64 + ((ch ^ (r & 7)) * 8);
      }
      *(uint4*)(dst + off) = make_uint4(o[0], o[1], o[2], o[3]);
    }
  }
}

__global__ __launch_bounds__(256) void k1_prep(
    const u16* __restrict__ x, const u16* __restrict__ logAr, const u16* __restrict__ Aim,
    const u16* __restrict__ Cre, const u16* __restrict__ Cim, const u16* __restrict__ logdt,
    const u16* __restrict__ w1, const u16* __restrict__ w2,
    u16* __restrict__ xn, u16* __restrict__ xnT, u16* __restrict__ Kbuf,
    u16* __restrict__ w1T, u16* __restrict__ w2T, u16* __restrict__ tbl) {
  TS_BEGIN(0)
  k1_body(x, logAr, Aim, Cre, Cim, logdt, w1, w2, xn, xnT, Kbuf, w1T, w2T, tbl);
  TS_END(0)
}

constexpr int G1_STAGE = 240 * 128;
constexpr int K2_SY = 1024, K2_SX = 34816, K2_SZ = 66560;
constexpr int K2_U = 100352;
constexpr int K2_KM = K2_U + 4 * 4160;
constexpr int K2_P = K2_KM + 2 * 4608;
constexpr int K2_CNT = 16;
constexpr int K2_ST = K2_P + 32768;
constexpr int K2_LDS = K2_ST + 256;

typedef __attribute__((address_space(3))) const char* lds_cptr;
typedef short v4i16_t __attribute__((ext_vector_type(4)));
typedef unsigned short u16x2 __attribute__((ext_vector_type(2)));
__device__ __forceinline__ v4i16_t trd(lds_cptr p) {
  return __builtin_amdgcn_ds_read_tr16_b64_v4i16((__attribute__((address_space(3))) v4i16_t*)p);
}
__device__ __forceinline__ bf16x8 rd16(lds_cptr p) { return *(const __attribute__((address_space(3))) bf16x8*)p; }
#define SBAR() __builtin_amdgcn_sched_barrier(0)

template <int OFF>
__device__ __forceinline__ void garrive(uint32_t addr, int lane) {
  asm volatile("" ::: "memory");
  __builtin_amdgcn_s_waitcnt(0xC07F);
  asm volatile("" ::: "memory");
  if (lane == 0) asm volatile("ds_add_u32 %0, %1 offset:%2" ::"v"(addr), "v"(1u), "n"(OFF) : "memory");
}
template <int OFF>
__device__ __forceinline__ void gwait(uint32_t addr, uint32_t target) {
  while (true) {
    uint32_t c;
    asm volatile("ds_read_b32 %0, %1 offset:%2\n\ts_waitcnt lgkmcnt(0)" : "=v"(c) : "v"(addr), "n"(OFF) : "memory");
    if ((uint32_t)__builtin_amdgcn_readfirstlane(c) >= target) break;
    __builtin_amdgcn_s_sleep(1);
  }
}

template <int MASK, class Fill>
__device__ __forceinline__ void conv_mfma(f32x4 (&acc)[8], lds_cptr ka, lds_cptr ub, Fill&& fill) {
  constexpr int T = (MASK & 0x80) ? 7 : 5;
  bf16x8 ah[2][8], al[2][8];
  v4i16_t bx[3], by[3];
#define CAT(x, y) __builtin_shufflevector(x, y, 0, 1, 2, 3, 4, 5, 6, 7)
  asm volatile("; conv %0" ::"n"(MASK) : "memory");
  bx[0] = trd(ub + 8); by[0] = trd(ub);
  bx[1] = trd(ub + 512 + 8); by[1] = trd(ub + 512);
#pragma unroll
  for (int b = 0; b <= T; ++b) { ah[0][b] = rd16(ka + 256 + 512 * b); al[0][b] = rd16(ka + 256 + 512 * b + 4608); }
  SBAR();
#pragma unroll
  for (int rr = 0; rr < 2; ++rr) {
#pragma unroll
    for (int a = 0; a <= T; ++a) {
      const int st = rr * (T + 1) + a, cur = st % 3, nxt = (st + 2) % 3;
      const int nrr = (st + 2) / (T + 1), na = (st + 2) % (T + 1);
      const bf16x8 bf = CAT(bx[cur], by[cur]);
      int cnt = 0, pend = -1;
#pragma unroll
      for (int bb = 0; bb <= T - a; ++bb) {
        const int b = (rr == 1 && a == 0) ? T - bb : bb;
        if (!((MASK >> (a + b)) & 1)) continue;
        acc[a + b] = __builtin_amdgcn_mfma_f32_16x16x32_bf16(bf, ah[rr][b], acc[a + b], 0, 0, 0);
        if (cnt == 0 && nrr < 2) { bx[nxt] = trd(ub + nrr * 256 + 512 * na + 8); by[nxt] = trd(ub + nrr * 256 + 512 * na); }
        if (cnt == 0 && rr == 1) {
          if (a == T - 4) fill(std::integral_constant<int, 0>{});
          if (a == T - 3) fill(std::integral_constant<int, 1>{});
          if (a == T - 2) fill(std::integral_constant<int, 2>{});
          if (a == T) fill(std::integral_constant<int, 3>{});
        }
        SBAR();
        if (pend >= 0) {
          acc[a + pend] = __builtin_amdgcn_mfma_f32_16x16x32_bf16(bf, al[rr][pend], acc[a + pend], 0, 0, 0);
          SBAR();
        }
        pend = b; ++cnt;
      }
      acc[a + pend] = __builtin_amdgcn_mfma_f32_16x16x32_bf16(bf, al[rr][pend], acc[a + pend], 0, 0, 0);
      if (rr == 0) { ah[1][T - a] = rd16(ka + 512 * (T - a)); al[1][T - a] = rd16(ka + 512 * (T - a) + 4608); }
      SBAR();
    }
  }
#undef CAT
}

__device__ __forceinline__ void k2_body(
    const u16* __restrict__ xn, const u16* __restrict__ xnT, const u16* __restrict__ Kbuf,
    const u16* __restrict__ Dskip, const u16* __restrict__ w1T, const u16* __restrict__ b1,
    const u16* __restrict__ tbl, u16* __restrict__ act, u16* __restrict__ AoutT) {
  __shared__ __attribute__((aligned(16))) char smem[K2_LDS];
  const int tid = threadIdx.x, lane = tid & 63;
  const int wave = __builtin_amdgcn_readfirstlane(tid >> 6);
  const int bid = blockIdx.x;
  const uint32_t sbase = (uint32_t)(size_t)(lds_cptr)smem;
  constexpr int cF = K2_CNT, cE = cF + 48, cA = cE + 48, cB = cA + 4, cC = cA + 8;
  const int xc = bid & 7, jj = bid >> 3;
  const int tm = (xc & 3) * 4 + (jj & 3), tn = (xc >> 2) * 8 + (jj >> 2);
  const int m0 = tm * 128, n0 = tn * 112;
  if (wave >= 8) {
    const int lw = wave - 8;
    const uint32_t vo = (uint32_t)lane * 16u;
    const char* ga = (const char*)(xn + (size_t)tm * 10 * 8192);
    const char* gb = (const char*)(w1T + (size_t)tn * 10 * 7168);
    const int nvalid = (tn == 15) ? 6 : 14;
    auto stage = [&](int kt, int off) {
      const char* pa = ga + kt * 16384;
      const char* pb = gb + kt * 14336;
#pragma unroll
      for (int i = 0; i < 16; ++i)
        __builtin_amdgcn_global_load_lds((const uint32_t*)(pa + i * 1024 + vo), (uint32_t*)(smem + off + i * 1024), 16, 0, 0);
#pragma unroll
      for (int i = 0; i < 14; ++i)
        __builtin_amdgcn_global_load_lds((const uint32_t*)(pb + (i < nvalid ? i * 1024 : 0) + vo),
                                         (uint32_t*)(smem + off + (16 + i) * 1024), 16, 0, 0);
    };
    __builtin_amdgcn_s_setprio(3);
    if (lw < 3) stage(lw, lw == 0 ? K2_SX : lw == 1 ? K2_SY : K2_SZ);
    asm volatile("s_waitcnt lgkmcnt(0)" ::: "memory");
    __builtin_amdgcn_s_barrier();
#pragma unroll 1
    for (int kt = lw; kt < 12; kt += 4) {
      if (kt >= 3) {
        gwait<cE>(sbase + (kt - 3) * 4, 4);
        if (kt < 10) {
          const int r3 = kt % 3;
          stage(kt, r3 == 0 ? K2_SX : r3 == 1 ? K2_SY : K2_SZ);
        } else {
          const int h = kt - 10;
#pragma unroll
          for (int i = 0; i < 8; ++i)
            __builtin_amdgcn_global_load_lds((const uint32_t*)((const char*)(tbl + h * TB_STRIDE) + i * 1024 + vo),
                                             (uint32_t*)(smem + h * 65536 + TB_LO * 2 + i * 1024), 16, 0, 0);
        }
      }
      asm volatile("s_waitcnt vmcnt(0)" ::: "memory");
      garrive<cF>(sbase + (kt < 10 ? kt : 10) * 4, lane);
    }
    return;
  }
  if (wave < 4) {
    const int fr = lane & 15, fq = lane >> 4;
    STAMP(0)
    if (tid < 28) *(uint32_t*)(smem + K2_CNT + tid * 4) = 0;
    if (tid == 28) *(uint32_t*)(smem) = 0;
    if (tid == 29) *(uint32_t*)(smem + 65536) = 0x8000u;
    asm volatile("s_waitcnt lgkmcnt(0)" ::: "memory");
    __builtin_amdgcn_s_barrier();
#define PG 2
#define PC 0
    __builtin_amdgcn_s_setprio(PG);
    uint4 bb[3]; uint2 bb3;
#pragma unroll
    for (int p = 0; p < 3; ++p) {
      int n = n0 + 32 * p + 8 * fq;
      if (n > F_ - 8) n = F_ - 8;
      bb[p] = *(const uint4*)(b1 + n);
    }
    {
      int n = n0 + 96 + 4 * fq;
      if (n > F_ - 4) n = F_ - 4;
      bb3 = *(const uint2*)(b1 + n);
    }
    STAMP(1)
    int aoff[7], boff[2];
#pragma unroll
    for (int i = 0; i < 7; ++i) {
      const int r2 = (i < 6) ? (32 * (i >> 1) + 8 * (fr >> 2) + 4 * (i & 1) + (fr & 3)) : (96 + fr);
      const int sw = (i < 6) ? (((r2 >> 1) & 1) | (((r2 >> 3) & 3) << 1)) : ((r2 >> 1) & 7);
      aoff[i] = (128 + r2) * 128 + ((fq ^ sw) * 16);
    }
#pragma unroll
    for (int j = 0; j < 2; ++j) {
      const int row = wave * 32 + j * 16 + fr;
      boff[j] = row * 128 + ((fq ^ ((row >> 1) & 7)) * 16);
    }
    f32x4 acc[7][2];
#pragma unroll
    for (int i = 0; i < 7; ++i)
#pragma unroll
      for (int j = 0; j < 2; ++j) acc[i][j] = (f32x4){0.f, 0.f, 0.f, 0.f};
    int off = K2_SX;
    bf16x8 af[2][7], bfm[2][2];
    gwait<cF>(sbase, 1);
    {
      const char* base = smem + off;
#pragma unroll
      for (int j = 0; j < 2; ++j) bfm[0][j] = *(const bf16x8*)(base + boff[j]);
#pragma unroll
      for (int i = 0; i < 7; ++i) af[0][i] = *(const bf16x8*)(base + aoff[i]);
    }
#pragma unroll 1
    for (int kt = 0; kt < 10; ++kt) {
      STAMPK(2 + kt)
      const char* base = smem + off;
      off = (off == K2_SX) ? K2_SY : (off == K2_SY) ? K2_SZ : K2_SX;
      uint32_t pc = 0;
      SBAR();
#pragma unroll
      for (int ks = 0; ks < 2; ++ks) {
#pragma unroll
        for (int i = 0; i < 7; ++i) {
          acc[i][0] = __builtin_amdgcn_mfma_f32_16x16x32_bf16(af[ks][i], bfm[ks][0], acc[i][0], 0, 0, 0);
          if (ks == 0) {
            if (i < 2) bfm[1][i] = *(const bf16x8*)(base + (boff[i] ^ 64));
            af[1][i] = *(const bf16x8*)(base + (aoff[i] ^ 64));
          }
          if (ks == 1 && i == 0)
            asm volatile("ds_read_b32 %0, %1 offset:%2" : "+v"(pc) : "v"(sbase + kt * 4), "n"(cF + 4) : "memory");
          SBAR();
          acc[i][1] = __builtin_amdgcn_mfma_f32_16x16x32_bf16(af[ks][i], bfm[ks][1], acc[i][1], 0, 0, 0);
          if (ks == 1 && i == 2) {
            garrive<cE>(sbase + kt * 4, lane);
            asm volatile("" : "+v"(pc)::"memory");
            if (kt < 9) {
              if (__builtin_amdgcn_readfirstlane(pc) == 0) gwait<cF + 4>(sbase + kt * 4, 1);
              const char* nb = smem + off;
#pragma unroll
              for (int j = 0; j < 2; ++j) bfm[0][j] = *(const bf16x8*)(nb + boff[j]);
#pragma unroll
              for (int i2 = 0; i2 < 7; ++i2) af[0][i2] = *(const bf16x8*)(nb + aoff[i2]);
            }
          }
          SBAR();
        }
      }
    }
    gwait<cF>(sbase + 40, 2);
    STAMP(12)
    u16* dummy = (u16*)tbl + 2 * TB_STRIDE;
    u16x2 vmax = {0, 0}, vmin = {0xffff, 0xffff};
    auto epi = [&](auto slow_tag) {
      constexpr bool SLOW = decltype(slow_tag)::value;
      auto hval = [&](float a0, float a1, uint32_t bias) -> uint32_t {
        const uint32_t r = pack2(a0, a1);
        return pack2(bflo(r) + bflo(bias), bfhi(r) + bfhi(bias));
      };
      auto act2 = [&](uint32_t h) -> uint32_t {
        if (SLOW) {
          return pack2(bflo(h) * tanh_sp_bf(bflo(h)), bfhi(h) * tanh_sp_bf(bfhi(h)));
        } else {
          const u16x2 m = __builtin_bit_cast(u16x2, h & 0x7fff7fffu);
          vmax = __builtin_elementwise_max(vmax, m);
          vmin = __builtin_elementwise_min(vmin, (u16x2)(m - (u16x2){1, 1}));
          uint32_t alo, ahi;
          asm("v_lshlrev_b32_sdwa %0, %1, %2 dst_sel:DWORD dst_unused:UNUSED_PAD src0_sel:DWORD src1_sel:WORD_0" : "=v"(alo) : "v"(1u), "v"(h));
          asm("v_lshlrev_b32_sdwa %0, %1, %2 dst_sel:DWORD dst_unused:UNUSED_PAD src0_sel:DWORD src1_sel:WORD_1" : "=v"(ahi) : "v"(1u), "v"(h));
          const uint32_t lo = *(const u16*)(smem + alo);
          const uint32_t hi = *(const u16*)(smem + ahi);
          return lo | (hi << 16);
        }
      };
#pragma unroll
      for (int j = 0; j < 2; ++j) {
        const int m = m0 + wave * 32 + j * 16 + fr;
        u16* arow = act + ((size_t)(m >> 6) * 27 * 64 + (m & 63)) * 64;
        uint32_t h[14];
#pragma unroll
        for (int p = 0; p < 3; ++p) {
          h[4 * p + 0] = hval(acc[2 * p][j][0], acc[2 * p][j][1], bb[p].x);
          h[4 * p + 1] = hval(acc[2 * p][j][2], acc[2 * p][j][3], bb[p].y);
          h[4 * p + 2] = hval(acc[2 * p + 1][j][0], acc[2 * p + 1][j][1], bb[p].z);
          h[4 * p + 3] = hval(acc[2 * p + 1][j][2], acc[2 * p + 1][j][3], bb[p].w);
        }
        h[12] = hval(acc[6][j][0], acc[6][j][1], bb3.x);
        h[13] = hval(acc[6][j][2], acc[6][j][3], bb3.y);
#pragma unroll
        for (int i = 0; i < 14; ++i) h[i] = act2(h[i]);
#pragma unroll
        for (int p = 0; p < 3; ++p) {
          const int n = n0 + 32 * p + 8 * fq;
          u16* dst = (n < F_) ? arow + (n >> 6) * 4096 + ((((n >> 3) & 7) ^ (m & 7)) * 8) : dummy;
          *(uint4*)dst = make_uint4(h[4 * p], h[4 * p + 1], h[4 * p + 2], h[4 * p + 3]);
        }
        {
          const int n = n0 + 96 + 4 * fq;
          u16* dst = (n < F_) ? arow + (n >> 6) * 4096 + ((((n >> 3) & 7) ^ (m & 7)) * 8) + (n & 7) : dummy;
          *(uint2*)dst = make_uint2(h[12], h[13]);
        }
      }
    };
    epi(std::false_type{});
    STAMP(14)
    {
      const uint32_t mx = max((uint32_t)vmax.x, (uint32_t)vmax.y), mn = min((uint32_t)vmin.x, (uint32_t)vmin.y);
      if (__builtin_amdgcn_ballot_w64(mx > (uint32_t)TB_HI || mn < (uint32_t)(TB_LO - 1)) != 0) epi(std::true_type{});
    }
    STAMP(13)
    STAMP_DUMP(0, 15)
    return;
  }
  const int cw = wave - 4, ct = tid - 256;
  const int ch2 = (bid < 128) ? 512 + bid : 384 + bid;
  uint4 kr0, kr1, uv; uint2 up;
  const uint32_t o16 = (uint32_t)ct * 16u;
  auto fetch = [&](int d) {
    const char* us = (const char*)(xnT + (size_t)d * L_);
    uv = *(const uint4*)(us + o16);
    up = *(const uint2*)(us + (o16 ? o16 - 8u : 0u));
    const char* ks = (const char*)(Kbuf + (size_t)d * 4096);
    kr0 = *(const uint4*)(ks + o16);
    kr1 = *(const uint4*)(ks + 4096 + o16);
  };
  auto put = [&]() {
    const uint32_t pm2 = ct ? up.x : 0u, pm1 = ct ? up.y : 0u;
    const uint32_t am = __builtin_amdgcn_alignbit(pm1, pm2, 16), a0 = __builtin_amdgcn_alignbit(uv.x, pm1, 16);
    const uint32_t a1 = __builtin_amdgcn_alignbit(uv.y, uv.x, 16), a2 = __builtin_amdgcn_alignbit(uv.z, uv.y, 16);
    const uint32_t a3 = __builtin_amdgcn_alignbit(uv.w, uv.z, 16);
    char* ud = smem + K2_U + 64 + ct * 16;
    *(uint4*)(ud) = uv;
    *(uint4*)(ud + 4160) = make_uint4(a0, a1, a2, a3);
    *(uint4*)(ud + 2 * 4160) = make_uint4(pm1, uv.x, uv.y, uv.z);
    *(uint4*)(ud + 3 * 4160) = make_uint4(am, a0, a1, a2);
    *(uint4*)(smem + K2_KM + 512 + ct * 16) = kr0;
    *(uint4*)(smem + K2_KM + 4608 + 512 + ct * 16) = kr1;
  };
  fetch(bid);
  if (ct < 64) *(uint4*)(smem + K2_KM + (ct >> 5) * 4608 + (ct & 31) * 16) = make_uint4(0, 0, 0, 0);
  else if (ct < 80) *(uint4*)(smem + K2_U + ((ct >> 2) & 3) * 4160 + (ct & 3) * 16) = make_uint4(0, 0, 0, 0);
  asm volatile("s_waitcnt lgkmcnt(0)" ::: "memory");
  __builtin_amdgcn_s_barrier();
  __builtin_amdgcn_s_setprio(PC);
  const int fr = lane & 15, kg = lane >> 4;
  lds_cptr ub = (lds_cptr)(smem + K2_U + ((lane >> 2) & 3) * 4160 + 32 * (kg >> 1) - 16 * (kg & 1) + (lane & 3) * 8 + 56 + 64 * cw);
  lds_cptr ka = (lds_cptr)(smem + K2_KM + 512 + 32 * fr - 32 * (kg >> 1) + 16 * (kg & 1) - 64 * cw - 256);
  char* Pw = smem + K2_P + cw * 8192 + fr * 64 + ((kg ^ (fr >> 2)) * 16);
  const char* Pr = smem + K2_P + (ct >> 1) * 64;
  const int rsw = (ct >> 3) & 3;
  const int rc0 = (((ct & 1) * 2) ^ rsw) * 16, rc1 = (((ct & 1) * 2 + 1) ^ rsw) * 16;
  put();
  garrive<cA>(sbase, lane);
  uint4 ucur = make_uint4(0, 0, 0, 0);
  f32x4 s0, s1;
  auto red_load = [&]() {
    s0 = (*(const f32x4*)(Pr + rc0) + *(const f32x4*)(Pr + 8192 + rc0)) + (*(const f32x4*)(Pr + 16384 + rc0) + *(const f32x4*)(Pr + 24576 + rc0));
    s1 = (*(const f32x4*)(Pr + rc1) + *(const f32x4*)(Pr + 8192 + rc1)) + (*(const f32x4*)(Pr + 16384 + rc1) + *(const f32x4*)(Pr + 24576 + rc1));
  };
  auto red_store = [&](int d) {
    const uint32_t dw = ((const uint32_t*)Dskip)[d >> 1];
    const float y[8] = {s0[0], s0[1], s0[2], s0[3], s1[0], s1[1], s1[2], s1[3]};
    const float dsk = (d & 1) ? bfhi(dw) : bflo(dw);
    const uint32_t uu[4] = {ucur.x, ucur.y, ucur.z, ucur.w};
    uint32_t o4[4];
#pragma unroll
    for (int i = 0; i < 4; ++i) {
      float r0 = bfr(y[2 * i]) + bfr(bflo(uu[i]) * dsk);
      float r1 = bfr(y[2 * i + 1]) + bfr(bfhi(uu[i]) * dsk);
      o4[i] = pack2(r0, r1);
    }
    *(uint4*)((char*)(AoutT + (size_t)d * L_) + o16) = make_uint4(o4[0], o4[1], o4[2], o4[3]);
  };
#pragma unroll 1
  for (int task = 0; task < 3; ++task) {
    STAMP(16 + task * 5)
    gwait<cA>(sbase, 4 * (task + 1));
    STAMP(17 + task * 5)
    if (task < 2) fetch(task == 0 ? bid + 256 : ch2);
    f32x4 acc[8];
#pragma unroll
    for (int i = 0; i < 8; ++i) acc[i] = (f32x4){0.f, 0.f, 0.f, 0.f};
    const int mask = (task < 2) ? 0xFF : (bid < 128 ? 0xC3 : 0x3C);
    auto fill = [&](auto K) {
      constexpr int k = decltype(K)::value;
      if (k == 0) { if (task > 0) red_load(); }
      if (k == 1) { if (task > 0) garrive<cC>(sbase, lane); }
      if (k == 2) { if (task > 0) red_store(bid + 256 * (task - 1)); }
      if (k == 3) garrive<cB>(sbase, lane);
    };
    if (mask == 0xFF) conv_mfma<0xFF>(acc, ka, ub, fill);
    else if (mask == 0xC3) conv_mfma<0xC3>(acc, ka, ub, fill);
    else conv_mfma<0x3C>(acc, ka, ub, fill);
    STAMP(18 + task * 5)
    if (task > 0) gwait<cC>(sbase, 4 * task);
#pragma unroll
    for (int i = 0; i < 8; ++i)
      if ((mask >> i) & 1) *(f32x4*)(Pw + i * 1024) = acc[i];
    ucur = *(const uint4*)(smem + K2_U + 64 + ct * 16);
    STAMP(19 + task * 5)
    if (task < 2) {
      gwait<cB>(sbase, 4 * (task + 1));
      put();
    }
    garrive<cA>(sbase, lane);
    STAMP(20 + task * 5)
  }
  gwait<cA>(sbase, 16);
  if ((((bid < 128) ? 0xC3 : 0x3C) >> (ct >> 5)) & 1) {
    red_load();
    red_store(ch2);
  }
  STAMP(31)
  STAMP_DUMP(16, 32)
}

__global__ __launch_bounds__(768, 1) void k2_conv_gemm1(
    const u16* __restrict__ xn, const u16* __restrict__ xnT, const u16* __restrict__ Kbuf,
    const u16* __restrict__ Dskip, const u16* __restrict__ w1T, const u16* __restrict__ b1,
    const u16* __restrict__ tbl, u16* __restrict__ act, u16* __restrict__ AoutT) {
  TS_BEGIN(1)
  k2_body(xn, xnT, Kbuf, Dskip, w1T, b1, tbl, act, AoutT);
  TS_END(1)
}

constexpr int K3_STAGE = 144 * 128;
#define K3_NB 4
#define K3_PF 0
constexpr int K3_NBUF = K3_NB;
constexpr int K3_AO_OFF = K3_STAGE * K3_NBUF;
constexpr int K3_LDS = K3_AO_OFF + 80 * 128;

__device__ __forceinline__ void k3_body(
    const u16* __restrict__ act, const u16* __restrict__ w2T, const u16* __restrict__ x,
    const u16* __restrict__ AoutT, u16* __restrict__ out) {
  __shared__ __attribute__((aligned(16))) char smem[K3_LDS];
  const int tid = threadIdx.x, lane = tid & 63;
  const int wave = __builtin_amdgcn_readfirstlane(tid >> 6);
  const int bid = blockIdx.x;
  const int xc = bid & 7, jj = bid >> 3;
  const int rb = (xc & 3) * 8 + (jj & 7), nt = (xc >> 2) * 4 + (jj >> 3);
  const int m0 = rb * 64, n0 = nt * 80;
  constexpr int NT = F_ / 64;
  f32x4 acc[5][2];
#pragma unroll
  for (int i = 0; i < 5; ++i)
#pragma unroll
    for (int j = 0; j < 2; ++j) acc[i][j] = (f32x4){0.f, 0.f, 0.f, 0.f};
  const int fr = lane & 15, fq = lane >> 4;
  const int wm = wave & 1, ksp = (wave >> 1) & 1;
  const int em0 = tid / 10, ec0 = (tid % 10) * 8;
  const int em1 = (512 + tid) / 10, ec1 = ((512 + tid) % 10) * 8;
  uint4 xv0, xv1 = make_uint4(0, 0, 0, 0);
  if (wave >= 4) {
    const int lw = wave - 4;
    const u16* gsrc[5];
#pragma unroll
    for (int i = 0; i < 5; ++i) {
      int ch = lw + 4 * i;
      if (ch > 17) ch = 17;
      gsrc[i] = (ch < 8 ? act + (size_t)rb * 27 * 4096 + ch * 512 : w2T + (size_t)nt * 27 * 5120 + (ch - 8) * 512) + lane * 8;
    }
    auto stage = [&](int kt, int buf) {
#pragma unroll
      for (int i = 0; i < 4; ++i)
        __builtin_amdgcn_global_load_lds((const uint32_t*)(gsrc[i] + kt * (i < 2 ? 4096 : 5120)),
                                         (uint32_t*)(smem + buf * K3_STAGE + (lw + 4 * i) * 1024), 16, 0, 0);
      if (lw < 2)
        __builtin_amdgcn_global_load_lds((const uint32_t*)(gsrc[4] + kt * 5120),
                                         (uint32_t*)(smem + buf * K3_STAGE + (lw + 16) * 1024), 16, 0, 0);
    };
#pragma unroll
    for (int i = 0; i < K3_NBUF - 1; ++i) stage(i, i);
    int buf = K3_NBUF - 1;
    for (int kt = 0; kt < NT - (K3_NBUF - 1); ++kt) {
      if (lw < 2) asm volatile("s_waitcnt vmcnt(%0)" ::"n"(5 * (K3_NBUF - 2)) : "memory");
      else asm volatile("s_waitcnt vmcnt(%0)" ::"n"(4 * (K3_NBUF - 2)) : "memory");
      __builtin_amdgcn_s_barrier();
      stage(kt + K3_NBUF - 1, buf);
      buf = (buf == K3_NBUF - 1) ? 0 : buf + 1;
    }
    static_for<K3_NBUF - 1>([&](auto I) {
      constexpr int rem = K3_NBUF - 2 - decltype(I)::value;
      if (lw < 2) asm volatile("s_waitcnt vmcnt(%0)" ::"n"(5 * rem) : "memory");
      else asm volatile("s_waitcnt vmcnt(%0)" ::"n"(4 * rem) : "memory");
      __builtin_amdgcn_s_barrier();
    });
    xv0 = *(const uint4*)(x + (size_t)(m0 + em0) * D_ + n0 + ec0);
  } else {
#pragma unroll
    for (int i = 0; i < 3; ++i) {
      const int ch = wave + 4 * i;
      if (ch < 10) {
        const int row = ch * 8 + (lane >> 3);
        __builtin_amdgcn_global_load_lds((const uint32_t*)(AoutT + (size_t)(n0 + row) * L_ + m0 + (lane & 7) * 8),
                                         (uint32_t*)(smem + K3_AO_OFF + ch * 1024), 16, 0, 0);
      }
    }
    xv0 = *(const uint4*)(x + (size_t)(m0 + em0) * D_ + n0 + ec0);
    if (tid < 128) xv1 = *(const uint4*)(x + (size_t)(m0 + em1) * D_ + n0 + ec1);
    int aoff[5], boff[2];
#pragma unroll
    for (int i = 0; i < 5; ++i) {
      const int row = 64 + i * 16 + fr;
      aoff[i] = row * 128 + (((ksp * 4 + fq) ^ (row & 7)) * 16);
    }
#pragma unroll
    for (int j = 0; j < 2; ++j) {
      const int row = wm * 32 + j * 16 + fr;
      boff[j] = row * 128 + (((ksp * 4 + fq) ^ (row & 7)) * 16);
    }
    int buf = 0;
    for (int kt = 0; kt < NT; ++kt) {
      asm volatile("s_waitcnt lgkmcnt(0)" ::: "memory");
      __builtin_amdgcn_s_barrier();
      const char* base = smem + buf * K3_STAGE;
      buf = (buf == K3_NBUF - 1) ? 0 : buf + 1;
      bf16x8 af[5], bfm[2];
#pragma unroll
      for (int j = 0; j < 2; ++j) bfm[j] = *(const bf16x8*)(base + boff[j]);
#pragma unroll
      for (int i = 0; i < 5; ++i) af[i] = *(const bf16x8*)(base + aoff[i]);
#pragma unroll
      for (int i = 0; i < 5; ++i)
#pragma unroll
        for (int j = 0; j < 2; ++j)
          acc[i][j] = __builtin_amdgcn_mfma_f32_16x16x32_bf16(af[i], bfm[j], acc[i][j], 0, 0, 0);
    }
  }
  __syncthreads();
  float* red = (float*)smem;
  if (wave < 4) {
#pragma unroll
    for (int i = 0; i < 5; ++i)
#pragma unroll
      for (int j = 0; j < 2; ++j) {
        const int m = wm * 32 + j * 16 + fr, n = i * 16 + fq * 4;
        *(f32x4*)(red + (ksp * 64 + m) * 84 + n) = acc[i][j];
      }
  }
  __syncthreads();
  const u16* ao = (const u16*)(smem + K3_AO_OFF);
#pragma unroll
  for (int it = 0; it < 2; ++it) {
    if (it == 1 && tid >= 128) break;
    const int m = it ? em1 : em0, c8 = it ? ec1 : ec0;
    const uint4 xv = it ? xv1 : xv0;
    const f32x4* r0 = (const f32x4*)(red + m * 84 + c8);
    const f32x4* r1 = (const f32x4*)(red + (64 + m) * 84 + c8);
    const f32x4 s0 = r0[0] + r1[0], s1 = r0[1] + r1[1];
    const float s[8] = {s0[0], s0[1], s0[2], s0[3], s1[0], s1[1], s1[2], s1[3]};
    const uint32_t xx[4] = {xv.x, xv.y, xv.z, xv.w};
    uint32_t o[4];
#pragma unroll
    for (int i = 0; i < 4; ++i) {
      float a0 = bf2f(ao[(c8 + 2 * i) * 64 + m]), a1 = bf2f(ao[(c8 + 2 * i + 1) * 64 + m]);
      float v0 = bfr(bflo(xx[i]) + a0) + bfr(s[2 * i]);
      float v1 = bfr(bfhi(xx[i]) + a1) + bfr(s[2 * i + 1]);
      o[i] = pack2(v0, v1);
    }
    *(uint4*)(out + (size_t)(m0 + m) * D_ + n0 + c8) = make_uint4(o[0], o[1], o[2], o[3]);
  }
}

__global__ __launch_bounds__(512, 1) void k3_gemm2(
    const u16* __restrict__ act, const u16* __restrict__ w2T, const u16* __restrict__ x,
    const u16* __restrict__ AoutT, u16* __restrict__ out) {
  TS_BEGIN(2)
  k3_body(act, w2T, x, AoutT, out);
  TS_END(2)
}

}

extern "C" void kernel_launch(void* const* d_in, const int* in_sizes, int n_in,
                              void* d_out, int out_size, void* d_ws, size_t ws_size,
                              hipStream_t stream) {
  const u16* x = (const u16*)d_in[0];
  const u16* logAr = (const u16*)d_in[1];
  const u16* Aim = (const u16*)d_in[2];
  const u16* Cre = (const u16*)d_in[3];
  const u16* Cim = (const u16*)d_in[4];
  const u16* logdt = (const u16*)d_in[5];
  const u16* Dskip = (const u16*)d_in[6];
  const u16* w1 = (const u16*)d_in[8];
  const u16* b1 = (const u16*)d_in[9];
  const u16* w2 = (const u16*)d_in[10];
  u16* out = (u16*)d_out;
  u16* ws = (u16*)d_ws;
  u16* xn = ws;
  u16* xnT = xn + (size_t)L_ * D_;
  u16* Kbuf = xnT + (size_t)L_ * D_;
  u16* w1T = Kbuf + (size_t)D_ * 4096;
  u16* w2T = w1T + (size_t)D_ * 1792;
  u16* act = w2T + (size_t)D_ * F_;
  u16* AoutT = act + (size_t)L_ * F_;
  u16* tbl = AoutT + (size_t)L_ * D_;
  k1_prep<<<K1_BLOCKS, 256, 0, stream>>>(x, logAr, Aim, Cre, Cim, logdt, w1, w2, xn, xnT, Kbuf, w1T, w2T, tbl);
  k2_conv_gemm1<<<256, 768, 0, stream>>>(xn, xnT, Kbuf, Dskip, w1T, b1, tbl, act, AoutT);
  k3_gemm2<<<256, 512, 0, stream>>>(act, w2T, x, AoutT, out);
}
```

```cpp
#include <hip/hip_runtime.h>
#include <hip/hip_bf16.h>
#include <stdint.h>
#include <type_traits>

#define TS_BEGIN(k)
#define TS_END(k)
#define STAMP(i)
#define STAMPK(i)
#define STAMP_DUMP(a, b)

#define DMAV 0

namespace {

constexpr int L_ = 2048, D_ = 640, NS_ = 128, F_ = 1728;

typedef __attribute__((ext_vector_type(8))) short bf16x8;
typedef __attribute__((ext_vector_type(4))) float f32x4;
typedef __attribute__((ext_vector_type(16))) float f32x16;
typedef __attribute__((ext_vector_type(2))) float f32x2;
typedef __attribute__((ext_vector_type(2))) __bf16 bf16x2;
typedef unsigned short u16;

constexpr float LOG2E = 1.4426950408889634f;
constexpr float LN2 = 0.6931471805599453f;
constexpr float INV2PI = 0.15915494309189535f;

__device__ __forceinline__ float bf2f(u16 v) { return __uint_as_float(((uint32_t)v) << 16); }
__device__ __forceinline__ float bflo(uint32_t v) { return __uint_as_float(v << 16); }
__device__ __forceinline__ float bfhi(uint32_t v) { return __uint_as_float(v & 0xffff0000u); }
__device__ __forceinline__ u16 f2bf(float x) {
  __bf16 h = (__bf16)x;
  return __builtin_bit_cast(u16, h);
}
__device__ __forceinline__ float bfr(float x) { return bf2f(f2bf(x)); }
__device__ __forceinline__ uint32_t pack2(float a, float b) {
  f32x2 v = {a, b};
  bf16x2 r = __builtin_convertvector(v, bf16x2);
  return __builtin_bit_cast(uint32_t, r);
}

__device__ __forceinline__ float tanh_sp_bf(float h) {
  float e = bfr(__builtin_amdgcn_exp2f(-fabsf(h) * LOG2E));
  float s = e * (1.f - e * (0.5f - e * (1.f / 3.f)));
  float lg = __builtin_amdgcn_logf(1.f + e) * LN2;
  float l = bfr(e < 0.015625f ? s : lg);
  float sp = bfr(l + fmaxf(h, 0.f));
  float q = __builtin_amdgcn_exp2f(sp * (2.f * LOG2E));
  float t = 1.f - 2.f * __builtin_amdgcn_rcpf(q + 1.f);
  float ts = sp * (1.f - sp * sp * (1.f / 3.f));
  return bfr(sp < 0.03125f ? ts : t);
}
constexpr int TB_LO = 103 << 7, TB_HI = 134 << 7;
constexpr int TB_N = TB_HI - TB_LO + 1;
constexpr int TB_STRIDE = 4096;

template <int N, int I = 0, class Fn>
__device__ __forceinline__ void static_for(Fn&& f) {
  if constexpr (I < N) { f(std::integral_constant<int, I>{}); static_for<N, I + 1>(f); }
}

template <int CTRL>
__device__ __forceinline__ float dppf(float v) {
  return __builtin_bit_cast(float, __builtin_amdgcn_update_dpp(0, __builtin_bit_cast(int, v), CTRL, 0xF, 0xF, true));
}
__device__ __forceinline__ float rdl(float v, int l) {
  return __builtin_bit_cast(float, __builtin_amdgcn_readlane(__builtin_bit_cast(int, v), l));
}
__device__ __forceinline__ float wave_sum(float v) {
  v += dppf<0xB1>(v);
  v += dppf<0x4E>(v);
  v += dppf<0x141>(v);
  v += dppf<0x140>(v);
  return (rdl(v, 0) + rdl(v, 16)) + (rdl(v, 32) + rdl(v, 48));
}

constexpr int K1_KGEN = D_;
constexpr int K1_LN = L_ / 16;
constexpr int K1_T1 = (D_ / 64) * (F_ / 64);
constexpr int K1_BLOCKS = K1_KGEN + K1_LN + 2 * K1_T1 + 1;

__device__ __forceinline__ void k1_body(
    const u16* __restrict__ x, const u16* __restrict__ logAr, const u16* __restrict__ Aim,
    const u16* __restrict__ Cre, const u16* __restrict__ Cim, const u16* __restrict__ logdt,
    const u16* __restrict__ w1, const u16* __restrict__ w2,
    u16* __restrict__ xn, u16* __restrict__ xnT, u16* __restrict__ Kbuf,
    u16* __restrict__ w1T, u16* __restrict__ w2T, u16* __restrict__ tbl) {
  __shared__ __attribute__((aligned(16))) char smem[16 * 1296];
  const int tid = threadIdx.x, lane = tid & 63, wave = tid >> 6;
  int b = blockIdx.x;
  if (b < K1_KGEN) {
    const int d = b;
    float4* prm = (float4*)smem;
    float* part = (float*)(smem + 2048);
    float* kp = (float*)(smem + 2048 + 12288);
    if (tid < NS_) {
      float dt = bfr(expf(bf2f(logdt[d])));
      float ar = -bfr(expf(bf2f(logAr[d * NS_ + tid]))) * dt;
      float ai = bf2f(Aim[d * NS_ + tid]) * dt;
      float cr = bf2f(Cre[d * NS_ + tid]) * dt;
      float ci = bf2f(Cim[d * NS_ + tid]) * dt;
      prm[tid] = make_float4(ar * LOG2E, ai * INV2PI, 2.f * cr, 2.f * ci);
    }
    __syncthreads();
    const int r = lane & 15, kq = lane >> 4;
    float p0 = (float)r;
    float p1 = (r < 8) ? (float)(256 + 2 * r) : (float)(512 + 4 * ((r - 8) & 3) + 256 * ((r - 8) >> 2));
    float p2 = (r < 8) ? (float)(1024 + 8 * (r & 1) + 256 * (r >> 1)) : 2048.f;
    float pb = (float)(16 * r);
    f32x4 acc0 = {0.f, 0.f, 0.f, 0.f}, acc1 = acc0, acc2 = acc0;
#pragma unroll 2
    for (int g = 0; g < 8; ++g) {
      const float4 P = prm[wave * 32 + g * 4 + kq];
      float mb = __builtin_amdgcn_exp2f(P.x * pb);
      float rb = __builtin_amdgcn_fractf(P.y * pb);
      float fr_ = mb * __builtin_amdgcn_cosf(rb), fi_ = mb * __builtin_amdgcn_sinf(rb);
      float br = P.z * fr_ - P.w * fi_;
      float bi = -(P.z * fi_ + P.w * fr_);
      {
        float m = __builtin_amdgcn_exp2f(P.x * p0);
        float rv = __builtin_amdgcn_fractf(P.y * p0);
        float er = m * __builtin_amdgcn_cosf(rv), ei = m * __builtin_amdgcn_sinf(rv);
        acc0 = __builtin_amdgcn_mfma_f32_16x16x4f32(er, br, acc0, 0, 0, 0);
        acc0 = __builtin_amdgcn_mfma_f32_16x16x4f32(ei, bi, acc0, 0, 0, 0);
      }
      {
        float m = __builtin_amdgcn_exp2f(P.x * p1);
        float rv = __builtin_amdgcn_fractf(P.y * p1);
        float er = m * __builtin_amdgcn_cosf(rv), ei = m * __builtin_amdgcn_sinf(rv);
        acc1 = __builtin_amdgcn_mfma_f32_16x16x4f32(er, br, acc1, 0, 0, 0);
        acc1 = __builtin_amdgcn_mfma_f32_16x16x4f32(ei, bi, acc1, 0, 0, 0);
      }
      {
        float m = __builtin_amdgcn_exp2f(P.x * p2);
        float rv = __builtin_amdgcn_fractf(P.y * p2);
        float er = m * __builtin_amdgcn_cosf(rv), ei = m * __builtin_amdgcn_sinf(rv);
        acc2 = __builtin_amdgcn_mfma_f32_16x16x4f32(er, br, acc2, 0, 0, 0);
        acc2 = __builtin_amdgcn_mfma_f32_16x16x4f32(ei, bi, acc2, 0, 0, 0);
      }
    }
#pragma unroll
    for (int i = 0; i < 4; ++i) {
      part[((wave * 3 + 0) * 16 + kq * 4 + i) * 16 + r] = acc0[i];
      part[((wave * 3 + 1) * 16 + kq * 4 + i) * 16 + r] = acc1[i];
      part[((wave * 3 + 2) * 16 + kq * 4 + i) * 16 + r] = acc2[i];
    }
    __syncthreads();
    {
      int o;
      o = (tid & 15) * 16 + (tid >> 4);
      kp[tid] = (part[o] + part[o + 768]) + (part[o + 1536] + part[o + 2304]);
      if (tid < 128) { o = (16 + (tid & 7)) * 16 + (tid >> 3); }
      else { int m = tid - 128, j2 = m >> 2; o = (16 + 8 + (j2 >> 4) * 4 + (m & 3)) * 16 + (j2 & 15); }
      kp[256 + tid] = (part[o] + part[o + 768]) + (part[o + 1536] + part[o + 2304]);
      if (tid <= 128) {
        int j2 = tid >> 1;
        o = (tid == 128) ? (32 + 8) * 16 : (32 + (j2 >> 4) * 2 + (tid & 1)) * 16 + (j2 & 15);
        kp[512 + tid] = (part[o] + part[o + 768]) + (part[o + 1536] + part[o + 2304]);
      }
    }
    __syncthreads();
    float kv[8];
    {
      const int j0 = tid * 8;
      const int s = (j0 < 256) ? 0 : (j0 < 512) ? 1 : (j0 < 1024) ? 2 : 3;
      const int x0 = s ? j0 - (128 << s) : j0;
      const int bidx = s ? 128 + 128 * s : 0;
      const int hm1 = s ? (1 << (s - 1)) - 1 : 0;
#pragma unroll
      for (int jj = 0; jj < 8; ++jj) {
        const int xx = x0 + jj;
        const int q = s ? ((xx + hm1 + ((xx >> s) & 1)) >> s) : xx;
        kv[jj] = kp[bidx + q];
      }
    }
    uint32_t hi[4], lo[4];
#pragma unroll
    for (int i = 0; i < 4; ++i) {
      float a0 = kv[2 * i], a1 = kv[2 * i + 1];
      uint32_t h = pack2(a0, a1);
      hi[i] = h;
      lo[i] = pack2(a0 - bflo(h), a1 - bfhi(h));
    }
    u16* dst = Kbuf + (size_t)d * 4096 + tid * 8;
    *(uint4*)dst = make_uint4(hi[0], hi[1], hi[2], hi[3]);
    *(uint4*)(dst + 2048) = make_uint4(lo[0], lo[1], lo[2], lo[3]);
    return;
  }
  b -= K1_KGEN;
  if (b < K1_LN) {
    const int xc = b & 7, kk = b >> 3;
    const int l0 = ((xc * 4 + (kk >> 2)) * 4 + (kk & 3)) * 16;
    uint32_t* tile = (uint32_t*)smem;
    uint32_t v[4][5];
#pragma unroll
    for (int rr = 0; rr < 4; ++rr) {
      const uint32_t* xr = (const uint32_t*)(x + (size_t)(l0 + wave * 4 + rr) * D_);
#pragma unroll
      for (int i = 0; i < 5; ++i) v[rr][i] = xr[lane + 64 * i];
    }
    float mean[4], q[4];
#pragma unroll
    for (int rr = 0; rr < 4; ++rr) {
      float s = 0.f;
#pragma unroll
      for (int i = 0; i < 5; ++i) s += bflo(v[rr][i]) + bfhi(v[rr][i]);
      mean[rr] = wave_sum(s) * (1.f / D_);
    }
#pragma unroll
    for (int rr = 0; rr < 4; ++rr) {
      float s = 0.f;
#pragma unroll
      for (int i = 0; i < 5; ++i) {
        float a = bflo(v[rr][i]) - mean[rr], c = bfhi(v[rr][i]) - mean[rr];
        s += a * a + c * c;
      }
      q[rr] = wave_sum(s);
    }
#pragma unroll
    for (int rr = 0; rr < 4; ++rr) {
      const int row = wave * 4 + rr;
      const float mb = bfr(mean[rr]);
      const float vb = bfr(q[rr] * (1.f / D_));
      const float sd = bfr(sqrtf(bfr(vb + 1e-5f)));
      const int l = l0 + row, r = l & 127;
      uint32_t* xo = (uint32_t*)xn + ((size_t)((l >> 7) * 10 + (lane >> 5)) * 128 + r) * 32 +
                     ((((lane >> 2) & 7) ^ ((r >> 1) & 7)) * 4) + (lane & 3);
#pragma unroll
      for (int i = 0; i < 5; ++i) {
        float a = bfr(bflo(v[rr][i]) - mb) / sd, c = bfr(bfhi(v[rr][i]) - mb) / sd;
        uint32_t o = pack2(a, c);
        xo[i * 2 * 128 * 32] = o;
        tile[row * 324 + lane + 64 * i] = o;
      }
    }
    __syncthreads();
    const u16* t16 = (const u16*)smem;
    for (int d = tid; d < D_; d += 256) {
      uint32_t o[8];
#pragma unroll
      for (int i = 0; i < 8; ++i)
        o[i] = (uint32_t)t16[(2 * i) * 648 + d] | ((uint32_t)t16[(2 * i + 1) * 648 + d] << 16);
      uint4* dst = (uint4*)(xnT + (size_t)d * L_ + l0);
      dst[0] = make_uint4(o[0], o[1], o[2], o[3]);
      dst[1] = make_uint4(o[4], o[5], o[6], o[7]);
    }
    return;
  }
  b -= K1_LN;
  if (b == 2 * K1_T1) {
    for (int i = tid; i < 2 * TB_STRIDE; i += 256) {
      const int sg = i >= TB_STRIDE;
      int m = i - sg * TB_STRIDE;
      if (m > TB_N - 1) m = TB_N - 1;
      const u16 bits = (u16)((TB_LO + m) | (sg << 15));
      const float hv = bf2f(bits);
      tbl[i] = f2bf(hv * tanh_sp_bf(hv));
    }
    return;
  }
  {
    const u16* src; u16* dst; int R, C;
    const bool first = b < K1_T1;
    if (first) { src = w1; dst = w1T; R = D_; C = F_; }
    else { b -= K1_T1; src = w2; dst = w2T; R = F_; C = D_; }
    const int tc = C / 64;
    const int r0 = (b / tc) * 64, c0 = (b % tc) * 64;
    u16* t16 = (u16*)smem;
#pragma unroll
    for (int i = 0; i < 2; ++i) {
      int rr = (tid >> 3) + 32 * i, ch = tid & 7;
      uint4 v = *(const uint4*)(src + (size_t)(r0 + rr) * C + c0 + ch * 8);
      *(uint4*)(t16 + rr * 72 + ch * 8) = v;
    }
    __syncthreads();
#pragma unroll
    for (int i = 0; i < 2; ++i) {
      int cc = (tid >> 3) + 32 * i, ch = tid & 7;
      uint32_t o[4];
#pragma unroll
      for (int k = 0; k < 4; ++k)
        o[k] = (uint32_t)t16[(ch * 8 + 2 * k) * 72 + cc] | ((uint32_t)t16[(ch * 8 + 2 * k + 1) * 72 + cc] << 16);
      const int n = c0 + cc, kt = r0 >> 6;
      size_t off;
      if (first) {
        const int tn = n / 112, r2 = n - tn * 112;
        const int sw = (r2 < 96) ? (((r2 >> 1) & 1) | (((r2 >> 3) & 3) << 1)) : ((r2 >> 1) & 7);
        off = ((size_t)(tn * 10 + kt) * 112 + r2) * 64 + ((ch ^ sw) * 8);
      } else {
        const int nt = n / 80, r = n - nt * 80;
        off = ((size_t)(nt * 27 + kt) * 80 + r) * 64 + ((ch ^ (r & 7)) * 8);
      }
      *(uint4*)(dst + off) = make_uint4(o[0], o[1], o[2], o[3]);
    }
  }
}

__global__ __launch_bounds__(256) void k1_prep(
    const u16* __restrict__ x, const u16* __restrict__ logAr, const u16* __restrict__ Aim,
    const u16* __restrict__ Cre, const u16* __restrict__ Cim, const u16* __restrict__ logdt,
    const u16* __restrict__ w1, const u16* __restrict__ w2,
    u16* __restrict__ xn, u16* __restrict__ xnT, u16* __restrict__ Kbuf,
    u16* __restrict__ w1T, u16* __restrict__ w2T, u16* __restrict__ tbl) {
  TS_BEGIN(0)
  k1_body(x, logAr, Aim, Cre, Cim, logdt, w1, w2, xn, xnT, Kbuf, w1T, w2T, tbl);
  TS_END(0)
}

constexpr int G1_STAGE = 240 * 128;
constexpr int K2_SY = 1024, K2_SX = 34816, K2_SZ = 66560;
constexpr int K2_R0 = 100352;
constexpr int K2_IMG = 4 * 4160 + 2 * 4608;
constexpr int K2_R1 = K2_R0 + K2_IMG;
constexpr int K2_KM = 4 * 4160;
constexpr int K2_Q = K2_R1 + K2_IMG;
constexpr int K2_CNT = 16;
constexpr int K2_ST = K2_Q + 8192;
constexpr int K2_LDS = K2_ST + 256;

typedef __attribute__((address_space(3))) const char* lds_cptr;
typedef short v4i16_t __attribute__((ext_vector_type(4)));
typedef unsigned short u16x2 __attribute__((ext_vector_type(2)));
__device__ __forceinline__ v4i16_t trd(lds_cptr p) {
  return __builtin_amdgcn_ds_read_tr16_b64_v4i16((__attribute__((address_space(3))) v4i16_t*)p);
}
__device__ __forceinline__ bf16x8 rd16(lds_cptr p) { return *(const __attribute__((address_space(3))) bf16x8*)p; }
#define SBAR() __builtin_amdgcn_sched_barrier(0)

template <int OFF>
__device__ __forceinline__ void garrive(uint32_t addr, int lane) {
  asm volatile("" ::: "memory");
  __builtin_amdgcn_s_waitcnt(0xC07F);
  asm volatile("" ::: "memory");
  if (lane == 0) asm volatile("ds_add_u32 %0, %1 offset:%2" ::"v"(addr), "v"(1u), "n"(OFF) : "memory");
}
template <int OFF>
__device__ __forceinline__ void gwait(uint32_t addr, uint32_t target) {
  while (true) {
    uint32_t c;
    asm volatile("ds_read_b32 %0, %1 offset:%2\n\ts_waitcnt lgkmcnt(0)" : "=v"(c) : "v"(addr), "n"(OFF) : "memory");
    if ((uint32_t)__builtin_amdgcn_readfirstlane(c) >= target) break;
    __builtin_amdgcn_s_sleep(1);
  }
}

template <int MASK, class Fill>
__device__ __forceinline__ void conv_mfma(f32x4 (&acc)[8], lds_cptr ka, lds_cptr ub, Fill&& fill) {
  constexpr int T = (MASK & 0x80) ? 7 : 5;
  bf16x8 ah[2][8], al[2][8];
  v4i16_t bx[3], by[3];
#define CAT(x, y) __builtin_shufflevector(x, y, 0, 1, 2, 3, 4, 5, 6, 7)
  asm volatile("; conv %0" ::"n"(MASK) : "memory");
  bx[0] = trd(ub + 8); by[0] = trd(ub);
  bx[1] = trd(ub + 512 + 8); by[1] = trd(ub + 512);
#pragma unroll
  for (int b = 0; b <= T; ++b) { ah[0][b] = rd16(ka + 256 + 512 * b); al[0][b] = rd16(ka + 256 + 512 * b + 4608); }
  SBAR();
#pragma unroll
  for (int rr = 0; rr < 2; ++rr) {
#pragma unroll
    for (int a = 0; a <= T; ++a) {
      const int st = rr * (T + 1) + a, cur = st % 3, nxt = (st + 2) % 3;
      const int nrr = (st + 2) / (T + 1), na = (st + 2) % (T + 1);
      const bf16x8 bf = CAT(bx[cur], by[cur]);
      int cnt = 0, pend = -1;
#pragma unroll
      for (int bb = 0; bb <= T - a; ++bb) {
        const int b = (rr == 1 && a == 0) ? T - bb : bb;
        if (!((MASK >> (a + b)) & 1)) continue;
        acc[a + b] = __builtin_amdgcn_mfma_f32_16x16x32_bf16(bf, ah[rr][b], acc[a + b], 0, 0, 0);
        if (cnt == 0 && nrr < 2) { bx[nxt] = trd(ub + nrr * 256 + 512 * na + 8); by[nxt] = trd(ub + nrr * 256 + 512 * na); }
        if (cnt == 0 && rr == 1) {
          if (a == T - 4) fill(std::integral_constant<int, 0>{});
          if (a == T - 3) fill(std::integral_constant<int, 1>{});
          if (a == T - 2) fill(std::integral_constant<int, 2>{});
          if (a == T) fill(std::integral_constant<int, 3>{});
        }
        SBAR();
        if (pend >= 0) {
          acc[a + pend] = __builtin_amdgcn_mfma_f32_16x16x32_bf16(bf, al[rr][pend], acc[a + pend], 0, 0, 0);
          SBAR();
        }
        pend = b; ++cnt;
      }
      acc[a + pend] = __builtin_amdgcn_mfma_f32_16x16x32_bf16(bf, al[rr][pend], acc[a + pend], 0, 0, 0);
      if (rr == 0) { ah[1][T - a] = rd16(ka + 512 * (T - a)); al[1][T - a] = rd16(ka + 512 * (T - a) + 4608); }
      SBAR();
    }
  }
#undef CAT
}

struct ConvRow { uint4 uv, k0, k1; uint2 up; };
__device__ __forceinline__ void conv_fetch(ConvRow& r, const u16* __restrict__ xnT, const u16* __restrict__ Kbuf, int d, uint32_t o16) {
  const char* us = (const char*)(xnT + (size_t)d * L_);
  r.uv = *(const uint4*)(us + o16);
  r.up = *(const uint2*)(us + (o16 ? o16 - 8u : 0u));
  const char* ks = (const char*)(Kbuf + (size_t)d * 4096);
  r.k0 = *(const uint4*)(ks + o16);
  r.k1 = *(const uint4*)(ks + 4096 + o16);
}
__device__ __forceinline__ void conv_put(const ConvRow& r, char* img, int ct) {
  const uint32_t pm2 = ct ? r.up.x : 0u, pm1 = ct ? r.up.y : 0u;
  const uint32_t am = __builtin_amdgcn_alignbit(pm1, pm2, 16), a0 = __builtin_amdgcn_alignbit(r.uv.x, pm1, 16);
  const uint32_t a1 = __builtin_amdgcn_alignbit(r.uv.y, r.uv.x, 16), a2 = __builtin_amdgcn_alignbit(r.uv.z, r.uv.y, 16);
  const uint32_t a3 = __builtin_amdgcn_alignbit(r.uv.w, r.uv.z, 16);
  char* ud = img + 64 + ct * 16;
  *(uint4*)(ud) = r.uv;
  *(uint4*)(ud + 4160) = make_uint4(a0, a1, a2, a3);
  *(uint4*)(ud + 2 * 4160) = make_uint4(pm1, r.uv.x, r.uv.y, r.uv.z);
  *(uint4*)(ud + 3 * 4160) = make_uint4(am, a0, a1, a2);
  *(uint4*)(img + K2_KM + 512 + ct * 16) = r.k0;
  *(uint4*)(img + K2_KM + 4608 + 512 + ct * 16) = r.k1;
  if (ct < 64) *(uint4*)(img + K2_KM + (ct >> 5) * 4608 + (ct & 31) * 16) = make_uint4(0, 0, 0, 0);
  else if (ct < 80) *(uint4*)(img + ((ct >> 2) & 3) * 4160 + (ct & 3) * 16) = make_uint4(0, 0, 0, 0);
}

__device__ __forceinline__ void k2_body(
    const u16* __restrict__ xn, const u16* __restrict__ xnT, const u16* __restrict__ Kbuf,
    const u16* __restrict__ Dskip, const u16* __restrict__ w1T, const u16* __restrict__ b1,
    const u16* __restrict__ tbl, u16* __restrict__ act, u16* __restrict__ AoutT) {
  __shared__ __attribute__((aligned(16))) char smem[K2_LDS];
  const int tid = threadIdx.x, lane = tid & 63;
  const int wave = __builtin_amdgcn_readfirstlane(tid >> 6);
  const int bid = blockIdx.x;
  const uint32_t sbase = (uint32_t)(size_t)(lds_cptr)smem;
  constexpr int cF = K2_CNT, cE = cF + 48, cA = cE + 48, cB = cA + 12, cP = cB + 12, cR = cP + 12;
  const int ch2 = (bid < 128) ? 512 + bid : 384 + bid;
  const int xc = bid & 7, jj = bid >> 3;
  const int tm = (xc & 3) * 4 + (jj & 3), tn = (xc >> 2) * 8 + (jj >> 2);
  const int m0 = tm * 128, n0 = tn * 112;
  if (wave >= 8) {
    const int lw = wave - 8;
    const uint32_t vo = (uint32_t)lane * 16u;
    const char* ga = (const char*)(xn + (size_t)tm * 10 * 8192);
    const char* gb = (const char*)(w1T + (size_t)tn * 10 * 7168);
    const int nvalid = (tn == 15) ? 6 : 14;
    auto stage = [&](int kt, int off) {
      const char* pa = ga + kt * 16384;
      const char* pb = gb + kt * 14336;
#pragma unroll
      for (int i = 0; i < 16; ++i)
        __builtin_amdgcn_global_load_lds((const uint32_t*)(pa + i * 1024 + vo), (uint32_t*)(smem + off + i * 1024), 16, 0, 0);
#pragma unroll
      for (int i = 0; i < 14; ++i)
        __builtin_amdgcn_global_load_lds((const uint32_t*)(pb + (i < nvalid ? i * 1024 : 0) + vo),
                                         (uint32_t*)(smem + off + (16 + i) * 1024), 16, 0, 0);
    };
    __builtin_amdgcn_s_setprio(3);
    const int lt = tid - 512;
    const uint32_t o16 = (uint32_t)lt * 16u;
    ConvRow r2;
    uint4 u0, u1;
    if (lw < 3) stage(lw, lw == 0 ? K2_SX : lw == 1 ? K2_SY : K2_SZ);
    asm volatile("s_waitcnt lgkmcnt(0)" ::: "memory");
    __builtin_amdgcn_s_barrier();
#pragma unroll 1
    for (int kt = lw; kt < 12; kt += 4) {
      if (kt >= 3) {
        gwait<cE>(sbase + (kt - 3) * 4, 4);
        if (kt < 10) {
          const int r3 = kt % 3;
          stage(kt, r3 == 0 ? K2_SX : r3 == 1 ? K2_SY : K2_SZ);
        } else {
          const int h = kt - 10;
#pragma unroll
          for (int i = 0; i < 8; ++i)
            __builtin_amdgcn_global_load_lds((const uint32_t*)((const char*)(tbl + h * TB_STRIDE) + i * 1024 + vo),
                                             (uint32_t*)(smem + h * 65536 + TB_LO * 2 + i * 1024), 16, 0, 0);
        }
      }
      asm volatile("s_waitcnt vmcnt(0)" ::: "memory");
      garrive<cF>(sbase + (kt < 10 ? kt : 10) * 4, lane);
    }
    conv_fetch(r2, xnT, Kbuf, ch2, o16);
    u0 = *(const uint4*)((const char*)(xnT + (size_t)bid * L_) + o16);
    u1 = *(const uint4*)((const char*)(xnT + (size_t)(bid + 256) * L_) + o16);
    const int rsw = (lt >> 3) & 3;
    const int rc0 = (lt >> 1) * 64 + (((lt & 1) * 2) ^ rsw) * 16, rc1 = (lt >> 1) * 64 + (((lt & 1) * 2 + 1) ^ rsw) * 16;
    f32x4 s0, s1;
    auto red_load = [&](const char* Pr) {
      const char* Q = smem + K2_Q;
      s0 = (*(const f32x4*)(Pr + rc0) + *(const f32x4*)(Pr + 8192 + rc0)) + (*(const f32x4*)(Pr + 16384 + rc0) + *(const f32x4*)(Q + rc0));
      s1 = (*(const f32x4*)(Pr + rc1) + *(const f32x4*)(Pr + 8192 + rc1)) + (*(const f32x4*)(Pr + 16384 + rc1) + *(const f32x4*)(Q + rc1));
    };
    auto red_store = [&](int d, const uint4& ucur) {
      const uint32_t dw = ((const uint32_t*)Dskip)[d >> 1];
      const float y[8] = {s0[0], s0[1], s0[2], s0[3], s1[0], s1[1], s1[2], s1[3]};
      const float dsk = (d & 1) ? bfhi(dw) : bflo(dw);
      const uint32_t uu[4] = {ucur.x, ucur.y, ucur.z, ucur.w};
      uint32_t o4[4];
#pragma unroll
      for (int i = 0; i < 4; ++i) {
        float r0 = bfr(y[2 * i]) + bfr(bflo(uu[i]) * dsk);
        float r1 = bfr(y[2 * i + 1]) + bfr(bfhi(uu[i]) * dsk);
        o4[i] = pack2(r0, r1);
      }
      *(uint4*)((char*)(AoutT + (size_t)d * L_) + o16) = make_uint4(o4[0], o4[1], o4[2], o4[3]);
    };
    gwait<cP>(sbase, 4);
    red_load(smem + K2_R0);
    garrive<cR>(sbase, lane);
    gwait<cR>(sbase, 4);
    conv_put(r2, smem + K2_R0, lt);
    garrive<cA + 8>(sbase, lane);
    red_store(bid, u0);
    gwait<cP + 4>(sbase, 4);
    red_load(smem + K2_R1);
    garrive<cR + 4>(sbase, lane);
    red_store(bid + 256, u1);
    gwait<cP + 8>(sbase, 4);
    if ((((bid < 128) ? 0xC3 : 0x3C) >> (lt >> 5)) & 1) {
      red_load(smem + K2_R0);
      red_store(ch2, r2.uv);
    }
    return;
  }
  if (wave < 4) {
    const int fr = lane & 15, fq = lane >> 4;
    STAMP(0)
    if (tid < 36) *(uint32_t*)(smem + K2_CNT + tid * 4) = 0;
    if (tid == 40) *(uint32_t*)(smem) = 0;
    if (tid == 41) *(uint32_t*)(smem + 65536) = 0x8000u;
    asm volatile("s_waitcnt lgkmcnt(0)" ::: "memory");
    __builtin_amdgcn_s_barrier();
#define PG 2
#define PC 0
    __builtin_amdgcn_s_setprio(PG);
    uint4 bb[3]; uint2 bb3;
#pragma unroll
    for (int p = 0; p < 3; ++p) {
      int n = n0 + 32 * p + 8 * fq;
      if (n > F_ - 8) n = F_ - 8;
      bb[p] = *(const uint4*)(b1 + n);
    }
    {
      int n = n0 + 96 + 4 * fq;
      if (n > F_ - 4) n = F_ - 4;
      bb3 = *(const uint2*)(b1 + n);
    }
    STAMP(1)
    int aoff[7], boff[2];
#pragma unroll
    for (int i = 0; i < 7; ++i) {
      const int r2 = (i < 6) ? (32 * (i >> 1) + 8 * (fr >> 2) + 4 * (i & 1) + (fr & 3)) : (96 + fr);
      const int sw = (i < 6) ? (((r2 >> 1) & 1) | (((r2 >> 3) & 3) << 1)) : ((r2 >> 1) & 7);
      aoff[i] = (128 + r2) * 128 + ((fq ^ sw) * 16);
    }
#pragma unroll
    for (int j = 0; j < 2; ++j) {
      const int row = wave * 32 + j * 16 + fr;
      boff[j] = row * 128 + ((fq ^ ((row >> 1) & 7)) * 16);
    }
    f32x4 acc[7][2];
#pragma unroll
    for (int i = 0; i < 7; ++i)
#pragma unroll
      for (int j = 0; j < 2; ++j) acc[i][j] = (f32x4){0.f, 0.f, 0.f, 0.f};
    int off = K2_SX;
    bf16x8 af[2][7], bfm[2][2];
    gwait<cF>(sbase, 1);
    {
      const char* base = smem + off;
#pragma unroll
      for (int j = 0; j < 2; ++j) bfm[0][j] = *(const bf16x8*)(base + boff[j]);
#pragma unroll
      for (int i = 0; i < 7; ++i) af[0][i] = *(const bf16x8*)(base + aoff[i]);
    }
#pragma unroll 1
    for (int kt = 0; kt < 10; ++kt) {
      STAMPK(2 + kt)
      const char* base = smem + off;
      off = (off == K2_SX) ? K2_SY : (off == K2_SY) ? K2_SZ : K2_SX;
      uint32_t pc = 0;
      SBAR();
#pragma unroll
      for (int ks = 0; ks < 2; ++ks) {
#pragma unroll
        for (int i = 0; i < 7; ++i) {
          acc[i][0] = __builtin_amdgcn_mfma_f32_16x16x32_bf16(af[ks][i], bfm[ks][0], acc[i][0], 0, 0, 0);
          if (ks == 0) {
            if (i < 2) bfm[1][i] = *(const bf16x8*)(base + (boff[i] ^ 64));
            af[1][i] = *(const bf16x8*)(base + (aoff[i] ^ 64));
          }
          if (ks == 1 && i == 0)
            asm volatile("ds_read_b32 %0, %1 offset:%2" : "+v"(pc) : "v"(sbase + kt * 4), "n"(cF + 4) : "memory");
          SBAR();
          acc[i][1] = __builtin_amdgcn_mfma_f32_16x16x32_bf16(af[ks][i], bfm[ks][1], acc[i][1], 0, 0, 0);
          if (ks == 1 && i == 2) {
            garrive<cE>(sbase + kt * 4, lane);
            asm volatile("" : "+v"(pc)::"memory");
            if (kt < 9) {
              if (__builtin_amdgcn_readfirstlane(pc) == 0) gwait<cF + 4>(sbase + kt * 4, 1);
              const char* nb = smem + off;
#pragma unroll
              for (int j = 0; j < 2; ++j) bfm[0][j] = *(const bf16x8*)(nb + boff[j]);
#pragma unroll
              for (int i2 = 0; i2 < 7; ++i2) af[0][i2] = *(const bf16x8*)(nb + aoff[i2]);
            }
          }
          SBAR();
        }
      }
    }
    gwait<cF>(sbase + 40, 2);
    STAMP(12)
    u16* dummy = (u16*)tbl + 2 * TB_STRIDE;
    u16x2 vmax = {0, 0}, vmin = {0xffff, 0xffff};
    auto epi = [&](auto slow_tag) {
      constexpr bool SLOW = decltype(slow_tag)::value;
      auto hval = [&](float a0, float a1, uint32_t bias) -> uint32_t {
        const uint32_t r = pack2(a0, a1);
        return pack2(bflo(r) + bflo(bias), bfhi(r) + bfhi(bias));
      };
      auto act2 = [&](uint32_t h) -> uint32_t {
        if (SLOW) {
          return pack2(bflo(h) * tanh_sp_bf(bflo(h)), bfhi(h) * tanh_sp_bf(bfhi(h)));
        } else {
          const u16x2 m = __builtin_bit_cast(u16x2, h & 0x7fff7fffu);
          vmax = __builtin_elementwise_max(vmax, m);
          vmin = __builtin_elementwise_min(vmin, (u16x2)(m - (u16x2){1, 1}));
          uint32_t alo, ahi;
          asm("v_lshlrev_b32_sdwa %0, %1, %2 dst_sel:DWORD dst_unused:UNUSED_PAD src0_sel:DWORD src1_sel:WORD_0" : "=v"(alo) : "v"(1u), "v"(h));
          asm("v_lshlrev_b32_sdwa %0, %1, %2 dst_sel:DWORD dst_unused:UNUSED_PAD src0_sel:DWORD src1_sel:WORD_1" : "=v"(ahi) : "v"(1u), "v"(h));
          const uint32_t lo = *(const u16*)(smem + alo);
          const uint32_t hi = *(const u16*)(smem + ahi);
          return lo | (hi << 16);
        }
      };
#pragma unroll
      for (int j = 0; j < 2; ++j) {
        const int m = m0 + wave * 32 + j * 16 + fr;
        u16* arow = act + ((size_t)(m >> 6) * 27 * 64 + (m & 63)) * 64;
        uint32_t h[14];
#pragma unroll
        for (int p = 0; p < 3; ++p) {
          h[4 * p + 0] = hval(acc[2 * p][j][0], acc[2 * p][j][1], bb[p].x);
          h[4 * p + 1] = hval(acc[2 * p][j][2], acc[2 * p][j][3], bb[p].y);
          h[4 * p + 2] = hval(acc[2 * p + 1][j][0], acc[2 * p + 1][j][1], bb[p].z);
          h[4 * p + 3] = hval(acc[2 * p + 1][j][2], acc[2 * p + 1][j][3], bb[p].w);
        }
        h[12] = hval(acc[6][j][0], acc[6][j][1], bb3.x);
        h[13] = hval(acc[6][j][2], acc[6][j][3], bb3.y);
#pragma unroll
        for (int i = 0; i < 14; ++i) h[i] = act2(h[i]);
#pragma unroll
        for (int p = 0; p < 3; ++p) {
          const int n = n0 + 32 * p + 8 * fq;
          u16* dst = (n < F_) ? arow + (n >> 6) * 4096 + ((((n >> 3) & 7) ^ (m & 7)) * 8) : dummy;
          *(uint4*)dst = make_uint4(h[4 * p], h[4 * p + 1], h[4 * p + 2], h[4 * p + 3]);
        }
        {
          const int n = n0 + 96 + 4 * fq;
          u16* dst = (n < F_) ? arow + (n >> 6) * 4096 + ((((n >> 3) & 7) ^ (m & 7)) * 8) + (n & 7) : dummy;
          *(uint2*)dst = make_uint2(h[12], h[13]);
        }
      }
    };
    epi(std::false_type{});
    STAMP(14)
    {
      const uint32_t mx = max((uint32_t)vmax.x, (uint32_t)vmax.y), mn = min((uint32_t)vmin.x, (uint32_t)vmin.y);
      if (__builtin_amdgcn_ballot_w64(mx > (uint32_t)TB_HI || mn < (uint32_t)(TB_LO - 1)) != 0) epi(std::true_type{});
    }
    STAMP(13)
    STAMP_DUMP(0, 15)
    return;
  }
  const int cw = wave - 4, ct = tid - 256;
  {
    ConvRow r0, r1;
    conv_fetch(r0, xnT, Kbuf, bid, (uint32_t)ct * 16u);
    conv_fetch(r1, xnT, Kbuf, bid + 256, (uint32_t)ct * 16u);
    __builtin_amdgcn_s_barrier();
    __builtin_amdgcn_s_setprio(PC);
    conv_put(r0, smem + K2_R0, ct);
    garrive<cA>(sbase, lane);
    conv_put(r1, smem + K2_R1, ct);
    garrive<cA + 4>(sbase, lane);
  }
  const int fr = lane & 15, kg = lane >> 4;
  lds_cptr ub0 = (lds_cptr)(smem + K2_R0 + ((lane >> 2) & 3) * 4160 + 32 * (kg >> 1) - 16 * (kg & 1) + (lane & 3) * 8 + 56 + 64 * cw);
  lds_cptr ka0 = (lds_cptr)(smem + K2_R0 + K2_KM + 512 + 32 * fr - 32 * (kg >> 1) + 16 * (kg & 1) - 64 * cw - 256);
  const int pwo = fr * 64 + ((kg ^ (fr >> 2)) * 16);
#pragma unroll 1
  for (int task = 0; task < 3; ++task) {
    STAMP(16 + task * 5)
    gwait<cA>(sbase + task * 4, 4);
    STAMP(17 + task * 5)
    const int ro = (task == 1) ? K2_IMG : 0;
    lds_cptr ub = ub0 + ro, ka = ka0 + ro;
    f32x4 acc[8];
#pragma unroll
    for (int i = 0; i < 8; ++i) acc[i] = (f32x4){0.f, 0.f, 0.f, 0.f};
    const int mask = (task < 2) ? 0xFF : (bid < 128 ? 0xC3 : 0x3C);
    auto fill = [&](auto K) {
      if (decltype(K)::value == 3) garrive<cB>(sbase + task * 4, lane);
    };
    if (mask == 0xFF) conv_mfma<0xFF>(acc, ka, ub, fill);
    else if (mask == 0xC3) conv_mfma<0xC3>(acc, ka, ub, fill);
    else conv_mfma<0x3C>(acc, ka, ub, fill);
    STAMP(18 + task * 5)
    gwait<cB>(sbase + task * 4, 4);
    if (cw == 3 && task > 0) gwait<cR - 4>(sbase + task * 4, 4);
    STAMP(19 + task * 5)
    char* Pw = smem + ((cw == 3) ? K2_Q : K2_R0 + ro + cw * 8192) + pwo;
#pragma unroll
    for (int i = 0; i < 8; ++i)
      if ((mask >> i) & 1) *(f32x4*)(Pw + i * 1024) = acc[i];
    garrive<cP>(sbase + task * 4, lane);
    STAMP(20 + task * 5)
  }
  STAMP(31)
  STAMP_DUMP(16, 32)
}

__global__ __launch_bounds__(768, 1) void k2_conv_gemm1(
    const u16* __restrict__ xn, const u16* __restrict__ xnT, const u16* __restrict__ Kbuf,
    const u16* __restrict__ Dskip, const u16* __restrict__ w1T, const u16* __restrict__ b1,
    const u16* __restrict__ tbl, u16* __restrict__ act, u16* __restrict__ AoutT) {
  TS_BEGIN(1)
  k2_body(xn, xnT, Kbuf, Dskip, w1T, b1, tbl, act, AoutT);
  TS_END(1)
}

constexpr int K3_STAGE = 144 * 128;
#define K3_NB 4
#define K3_PF 0
constexpr int K3_NBUF = K3_NB;
constexpr int K3_AO_OFF = K3_STAGE * K3_NBUF;
constexpr int K3_LDS = K3_AO_OFF + 80 * 128;

__device__ __forceinline__ void k3_body(
    const u16* __restrict__ act, const u16* __restrict__ w2T, const u16* __restrict__ x,
    const u16* __restrict__ AoutT, u16* __restrict__ out) {
  __shared__ __attribute__((aligned(16))) char smem[K3_LDS];
  const int tid = threadIdx.x, lane = tid & 63;
  const int wave = __builtin_amdgcn_readfirstlane(tid >> 6);
  const int bid = blockIdx.x;
  const int xc = bid & 7, jj = bid >> 3;
  const int rb = (xc & 3) * 8 + (jj & 7), nt = (xc >> 2) * 4 + (jj >> 3);
  const int m0 = rb * 64, n0 = nt * 80;
  constexpr int NT = F_ / 64;
  f32x4 acc[5][2];
#pragma unroll
  for (int i = 0; i < 5; ++i)
#pragma unroll
    for (int j = 0; j < 2; ++j) acc[i][j] = (f32x4){0.f, 0.f, 0.f, 0.f};
  const int fr = lane & 15, fq = lane >> 4;
  const int wm = wave & 1, ksp = (wave >> 1) & 1;
  const int em0 = tid / 10, ec0 = (tid % 10) * 8;
  const int em1 = (512 + tid) / 10, ec1 = ((512 + tid) % 10) * 8;
  uint4 xv0, xv1 = make_uint4(0, 0, 0, 0);
  if (wave >= 4) {
    const int lw = wave - 4;
    const u16* gsrc[5];
#pragma unroll
    for (int i = 0; i < 5; ++i) {
      int ch = lw + 4 * i;
      if (ch > 17) ch = 17;
      gsrc[i] = (ch < 8 ? act + (size_t)rb * 27 * 4096 + ch * 512 : w2T + (size_t)nt * 27 * 5120 + (ch - 8) * 512) + lane * 8;
    }
    auto stage = [&](int kt, int buf) {
#pragma unroll
      for (int i = 0; i < 4; ++i)
        __builtin_amdgcn_global_load_lds((const uint32_t*)(gsrc[i] + kt * (i < 2 ? 4096 : 5120)),
                                         (uint32_t*)(smem + buf * K3_STAGE + (lw + 4 * i) * 1024), 16, 0, 0);
      if (lw < 2)
        __builtin_amdgcn_global_load_lds((const uint32_t*)(gsrc[4] + kt * 5120),
                                         (uint32_t*)(smem + buf * K3_STAGE + (lw + 16) * 1024), 16, 0, 0);
    };
#pragma unroll
    for (int i = 0; i < K3_NBUF - 1; ++i) stage(i, i);
    int buf = K3_NBUF - 1;
    for (int kt = 0; kt < NT - (K3_NBUF - 1); ++kt) {
      if (lw < 2) asm volatile("s_waitcnt vmcnt(%0)" ::"n"(5 * (K3_NBUF - 2)) : "memory");
      else asm volatile("s_waitcnt vmcnt(%0)" ::"n"(4 * (K3_NBUF - 2)) : "memory");
      __builtin_amdgcn_s_barrier();
      stage(kt + K3_NBUF - 1, buf);
      buf = (buf == K3_NBUF - 1) ? 0 : buf + 1;
    }
    static_for<K3_NBUF - 1>([&](auto I) {
      constexpr int rem = K3_NBUF - 2 - decltype(I)::value;
      if (lw < 2) asm volatile("s_waitcnt vmcnt(%0)" ::"n"(5 * rem) : "memory");
      else asm volatile("s_waitcnt vmcnt(%0)" ::"n"(4 * rem) : "memory");
      __builtin_amdgcn_s_barrier();
    });
    xv0 = *(const uint4*)(x + (size_t)(m0 + em0) * D_ + n0 + ec0);
  } else {
#pragma unroll
    for (int i = 0; i < 3; ++i) {
      const int ch = wave + 4 * i;
      if (ch < 10) {
        const int row = ch * 8 + (lane >> 3);
        __builtin_amdgcn_global_load_lds((const uint32_t*)(AoutT + (size_t)(n0 + row) * L_ + m0 + (lane & 7) * 8),
                                         (uint32_t*)(smem + K3_AO_OFF + ch * 1024), 16, 0, 0);
      }
    }
    xv0 = *(const uint4*)(x + (size_t)(m0 + em0) * D_ + n0 + ec0);
    if (tid < 128) xv1 = *(const uint4*)(x + (size_t)(m0 + em1) * D_ + n0 + ec1);
    int aoff[5], boff[2];
#pragma unroll
    for (int i = 0; i < 5; ++i) {
      const int row = 64 + i * 16 + fr;
      aoff[i] = row * 128 + (((ksp * 4 + fq) ^ (row & 7)) * 16);
    }
#pragma unroll
    for (int j = 0; j < 2; ++j) {
      const int row = wm * 32 + j * 16 + fr;
      boff[j] = row * 128 + (((ksp * 4 + fq) ^ (row & 7)) * 16);
    }
    int buf = 0;
    for (int kt = 0; kt < NT; ++kt) {
      asm volatile("s_waitcnt lgkmcnt(0)" ::: "memory");
      __builtin_amdgcn_s_barrier();
      const char* base = smem + buf * K3_STAGE;
      buf = (buf == K3_NBUF - 1) ? 0 : buf + 1;
      bf16x8 af[5], bfm[2];
#pragma unroll
      for (int j = 0; j < 2; ++j) bfm[j] = *(const bf16x8*)(base + boff[j]);
#pragma unroll
      for (int i = 0; i < 5; ++i) af[i] = *(const bf16x8*)(base + aoff[i]);
#pragma unroll
      for (int i = 0; i < 5; ++i)
#pragma unroll
        for (int j = 0; j < 2; ++j)
          acc[i][j] = __builtin_amdgcn_mfma_f32_16x16x32_bf16(af[i], bfm[j], acc[i][j], 0, 0, 0);
    }
  }
  __syncthreads();
  float* red = (float*)smem;
  if (wave < 4) {
#pragma unroll
    for (int i = 0; i < 5; ++i)
#pragma unroll
      for (int j = 0; j < 2; ++j) {
        const int m = wm * 32 + j * 16 + fr, n = i * 16 + fq * 4;
        *(f32x4*)(red + (ksp * 64 + m) * 84 + n) = acc[i][j];
      }
  }
  __syncthreads();
  const u16* ao = (const u16*)(smem + K3_AO_OFF);
#pragma unroll
  for (int it = 0; it < 2; ++it) {
    if (it == 1 && tid >= 128) break;
    const int m = it ? em1 : em0, c8 = it ? ec1 : ec0;
    const uint4 xv = it ? xv1 : xv0;
    const f32x4* r0 = (const f32x4*)(red + m * 84 + c8);
    const f32x4* r1 = (const f32x4*)(red + (64 + m) * 84 + c8);
    const f32x4 s0 = r0[0] + r1[0], s1 = r0[1] + r1[1];
    const float s[8] = {s0[0], s0[1], s0[2], s0[3], s1[0], s1[1], s1[2], s1[3]};
    const uint32_t xx[4] = {xv.x, xv.y, xv.z, xv.w};
    uint32_t o[4];
#pragma unroll
    for (int i = 0; i < 4; ++i) {
      float a0 = bf2f(ao[(c8 + 2 * i) * 64 + m]), a1 = bf2f(ao[(c8 + 2 * i + 1) * 64 + m]);
      float v0 = bfr(bflo(xx[i]) + a0) + bfr(s[2 * i]);
      float v1 = bfr(bfhi(xx[i]) + a1) + bfr(s[2 * i + 1]);
      o[i] = pack2(v0, v1);
    }
    *(uint4*)(out + (size_t)(m0 + m) * D_ + n0 + c8) = make_uint4(o[0], o[1], o[2], o[3]);
  }
}

__global__ __launch_bounds__(512, 1) void k3_gemm2(
    const u16* __restrict__ act, const u16* __restrict__ w2T, const u16* __restrict__ x,
    const u16* __restrict__ AoutT, u16* __restrict__ out) {
  TS_BEGIN(2)
  k3_body(act, w2T, x, AoutT, out);
  TS_END(2)
}

}

extern "C" void kernel_launch(void* const* d_in, const int* in_sizes, int n_in,
                              void* d_out, int out_size, void* d_ws, size_t ws_size,
                              hipStream_t stream) {
  const u16* x = (const u16*)d_in[0];
  const u16* logAr = (const u16*)d_in[1];
  const u16* Aim = (const u16*)d_in[2];
  const u16* Cre = (const u16*)d_in[3];
  const u16* Cim = (const u16*)d_in[4];
  const u16* logdt = (const u16*)d_in[5];
  const u16* Dskip = (const u16*)d_in[6];
  const u16* w1 = (const u16*)d_in[8];
  const u16* b1 = (const u16*)d_in[9];
  const u16* w2 = (const u16*)d_in[10];
  u16* out = (u16*)d_out;
  u16* ws = (u16*)d_ws;
  u16* xn = ws;
  u16* xnT = xn + (size_t)L_ * D_;
  u16* Kbuf = xnT + (size_t)L_ * D_;
  u16* w1T = Kbuf + (size_t)D_ * 4096;
  u16* w2T = w1T + (size_t)D_ * 1792;
  u16* act = w2T + (size_t)D_ * F_;
  u16* AoutT = act + (size_t)L_ * F_;
  u16* tbl = AoutT + (size_t)L_ * D_;
  k1_prep<<<K1_BLOCKS, 256, 0, stream>>>(x, logAr, Aim, Cre, Cim, logdt, w1, w2, xn, xnT, Kbuf, w1T, w2T, tbl);
  k2_conv_gemm1<<<256, 768, 0, stream>>>(xn, xnT, Kbuf, Dskip, w1T, b1, tbl, act, AoutT);
  k3_gemm2<<<256, 512, 0, stream>>>(act, w2T, x, AoutT, out);
}
```

```cpp
#include <hip/hip_runtime.h>
#include <hip/hip_bf16.h>
#include <stdint.h>
#include <type_traits>

#define TS_BEGIN(k)
#define TS_END(k)
#define STAMP(i)
#define STAMPK(i)
#define STAMP_DUMP(a, b)

#define DMAV 0

namespace {

constexpr int L_ = 2048, D_ = 640, NS_ = 128, F_ = 1728;

typedef __attribute__((ext_vector_type(8))) short bf16x8;
typedef __attribute__((ext_vector_type(4))) float f32x4;
typedef __attribute__((ext_vector_type(16))) float f32x16;
typedef __attribute__((ext_vector_type(2))) float f32x2;
typedef __attribute__((ext_vector_type(2))) __bf16 bf16x2;
typedef unsigned short u16;

constexpr float LOG2E = 1.4426950408889634f;
constexpr float LN2 = 0.6931471805599453f;
constexpr float INV2PI = 0.15915494309189535f;

__device__ __forceinline__ float bf2f(u16 v) { return __uint_as_float(((uint32_t)v) << 16); }
__device__ __forceinline__ float bflo(uint32_t v) { return __uint_as_float(v << 16); }
__device__ __forceinline__ float bfhi(uint32_t v) { return __uint_as_float(v & 0xffff0000u); }
__device__ __forceinline__ u16 f2bf(float x) {
  __bf16 h = (__bf16)x;
  return __builtin_bit_cast(u16, h);
}
__device__ __forceinline__ float bfr(float x) { return bf2f(f2bf(x)); }
__device__ __forceinline__ uint32_t pack2(float a, float b) {
  f32x2 v = {a, b};
  bf16x2 r = __builtin_convertvector(v, bf16x2);
  return __builtin_bit_cast(uint32_t, r);
}

__device__ __forceinline__ float tanh_sp_bf(float h) {
  float e = bfr(__builtin_amdgcn_exp2f(-fabsf(h) * LOG2E));
  float s = e * (1.f - e * (0.5f - e * (1.f / 3.f)));
  float lg = __builtin_amdgcn_logf(1.f + e) * LN2;
  float l = bfr(e < 0.015625f ? s : lg);
  float sp = bfr(l + fmaxf(h, 0.f));
  float q = __builtin_amdgcn_exp2f(sp * (2.f * LOG2E));
  float t = 1.f - 2.f * __builtin_amdgcn_rcpf(q + 1.f);
  float ts = sp * (1.f - sp * sp * (1.f / 3.f));
  return bfr(sp < 0.03125f ? ts : t);
}
constexpr int TB_LO = 103 << 7, TB_HI = 134 << 7;
constexpr int TB_N = TB_HI - TB_LO + 1;
constexpr int TB_STRIDE = 4096;

template <int N, int I = 0, class Fn>
__device__ __forceinline__ void static_for(Fn&& f) {
  if constexpr (I < N) { f(std::integral_constant<int, I>{}); static_for<N, I + 1>(f); }
}

template <int CTRL>
__device__ __forceinline__ float dppf(float v) {
  return __builtin_bit_cast(float, __builtin_amdgcn_update_dpp(0, __builtin_bit_cast(int, v), CTRL, 0xF, 0xF, true));
}
__device__ __forceinline__ float rdl(float v, int l) {
  return __builtin_bit_cast(float, __builtin_amdgcn_readlane(__builtin_bit_cast(int, v), l));
}
__device__ __forceinline__ float wave_sum(float v) {
  v += dppf<0xB1>(v);
  v += dppf<0x4E>(v);
  v += dppf<0x141>(v);
  v += dppf<0x140>(v);
  return (rdl(v, 0) + rdl(v, 16)) + (rdl(v, 32) + rdl(v, 48));
}

constexpr int K1_KGEN = D_;
constexpr int K1_LN = L_ / 16;
constexpr int K1_T1 = (D_ / 64) * (F_ / 64);
constexpr int K1_BLOCKS = K1_KGEN + K1_LN + 2 * K1_T1 + 1;

__device__ __forceinline__ void k1_body(
    const u16* __restrict__ x, const u16* __restrict__ logAr, const u16* __restrict__ Aim,
    const u16* __restrict__ Cre, const u16* __restrict__ Cim, const u16* __restrict__ logdt,
    const u16* __restrict__ w1, const u16* __restrict__ w2,
    u16* __restrict__ xn, u16* __restrict__ xnT, u16* __restrict__ Kbuf,
    u16* __restrict__ w1T, u16* __restrict__ w2T, u16* __restrict__ tbl) {
  __shared__ __attribute__((aligned(16))) char smem[16 * 1296];
  const int tid = threadIdx.x, lane = tid & 63, wave = tid >> 6;
  int b = blockIdx.x;
  if (b < K1_KGEN) {
    const int d = b;
    float4* prm = (float4*)smem;
    float* part = (float*)(smem + 2048);
    float* kp = (float*)(smem + 2048 + 12288);
    if (tid < NS_) {
      float dt = bfr(expf(bf2f(logdt[d])));
      float ar = -bfr(expf(bf2f(logAr[d * NS_ + tid]))) * dt;
      float ai = bf2f(Aim[d * NS_ + tid]) * dt;
      float cr = bf2f(Cre[d * NS_ + tid]) * dt;
      float ci = bf2f(Cim[d * NS_ + tid]) * dt;
      prm[tid] = make_float4(ar * LOG2E, ai * INV2PI, 2.f * cr, 2.f * ci);
    }
    __syncthreads();
    const int r = lane & 15, kq = lane >> 4;
    float p0 = (float)r;
    float p1 = (r < 8) ? (float)(256 + 2 * r) : (float)(512 + 4 * ((r - 8) & 3) + 256 * ((r - 8) >> 2));
    float p2 = (r < 8) ? (float)(1024 + 8 * (r & 1) + 256 * (r >> 1)) : 2048.f;
    float pb = (float)(16 * r);
    f32x4 acc0 = {0.f, 0.f, 0.f, 0.f}, acc1 = acc0, acc2 = acc0;
#pragma unroll 2
    for (int g = 0; g < 8; ++g) {
      const float4 P = prm[wave * 32 + g * 4 + kq];
      float mb = __builtin_amdgcn_exp2f(P.x * pb);
      float rb = __builtin_amdgcn_fractf(P.y * pb);
      float fr_ = mb * __builtin_amdgcn_cosf(rb), fi_ = mb * __builtin_amdgcn_sinf(rb);
      float br = P.z * fr_ - P.w * fi_;
      float bi = -(P.z * fi_ + P.w * fr_);
      {
        float m = __builtin_amdgcn_exp2f(P.x * p0);
        float rv = __builtin_amdgcn_fractf(P.y * p0);
        float er = m * __builtin_amdgcn_cosf(rv), ei = m * __builtin_amdgcn_sinf(rv);
        acc0 = __builtin_amdgcn_mfma_f32_16x16x4f32(er, br, acc0, 0, 0, 0);
        acc0 = __builtin_amdgcn_mfma_f32_16x16x4f32(ei, bi, acc0, 0, 0, 0);
      }
      {
        float m = __builtin_amdgcn_exp2f(P.x * p1);
        float rv = __builtin_amdgcn_fractf(P.y * p1);
        float er = m * __builtin_amdgcn_cosf(rv), ei = m * __builtin_amdgcn_sinf(rv);
        acc1 = __builtin_amdgcn_mfma_f32_16x16x4f32(er, br, acc1, 0, 0, 0);
        acc1 = __builtin_amdgcn_mfma_f32_16x16x4f32(ei, bi, acc1, 0, 0, 0);
      }
      {
        float m = __builtin_amdgcn_exp2f(P.x * p2);
        float rv = __builtin_amdgcn_fractf(P.y * p2);
        float er = m * __builtin_amdgcn_cosf(rv), ei = m * __builtin_amdgcn_sinf(rv);
        acc2 = __builtin_amdgcn_mfma_f32_16x16x4f32(er, br, acc2, 0, 0, 0);
        acc2 = __builtin_amdgcn_mfma_f32_16x16x4f32(ei, bi, acc2, 0, 0, 0);
      }
    }
#pragma unroll
    for (int i = 0; i < 4; ++i) {
      part[((wave * 3 + 0) * 16 + kq * 4 + i) * 16 + r] = acc0[i];
      part[((wave * 3 + 1) * 16 + kq * 4 + i) * 16 + r] = acc1[i];
      part[((wave * 3 + 2) * 16 + kq * 4 + i) * 16 + r] = acc2[i];
    }
    __syncthreads();
    {
      int o;
      o = (tid & 15) * 16 + (tid >> 4);
      kp[tid] = (part[o] + part[o + 768]) + (part[o + 1536] + part[o + 2304]);
      if (tid < 128) { o = (16 + (tid & 7)) * 16 + (tid >> 3); }
      else { int m = tid - 128, j2 = m >> 2; o = (16 + 8 + (j2 >> 4) * 4 + (m & 3)) * 16 + (j2 & 15); }
      kp[256 + tid] = (part[o] + part[o + 768]) + (part[o + 1536] + part[o + 2304]);
      if (tid <= 128) {
        int j2 = tid >> 1;
        o = (tid == 128) ? (32 + 8) * 16 : (32 + (j2 >> 4) * 2 + (tid & 1)) * 16 + (j2 & 15);
        kp[512 + tid] = (part[o] + part[o + 768]) + (part[o + 1536] + part[o + 2304]);
      }
    }
    __syncthreads();
    float kv[8];
    {
      const int j0 = tid * 8;
      const int s = (j0 < 256) ? 0 : (j0 < 512) ? 1 : (j0 < 1024) ? 2 : 3;
      const int x0 = s ? j0 - (128 << s) : j0;
      const int bidx = s ? 128 + 128 * s : 0;
      const int hm1 = s ? (1 << (s - 1)) - 1 : 0;
#pragma unroll
      for (int jj = 0; jj < 8; ++jj) {
        const int xx = x0 + jj;
        const int q = s ? ((xx + hm1 + ((xx >> s) & 1)) >> s) : xx;
        kv[jj] = kp[bidx + q];
      }
    }
    uint32_t hi[4], lo[4];
#pragma unroll
    for (int i = 0; i < 4; ++i) {
      float a0 = kv[2 * i], a1 = kv[2 * i + 1];
      uint32_t h = pack2(a0, a1);
      hi[i] = h;
      lo[i] = pack2(a0 - bflo(h), a1 - bfhi(h));
    }
    u16* dst = Kbuf + (size_t)d * 4096 + tid * 8;
    *(uint4*)dst = make_uint4(hi[0], hi[1], hi[2], hi[3]);
    *(uint4*)(dst + 2048) = make_uint4(lo[0], lo[1], lo[2], lo[3]);
    return;
  }
  b -= K1_KGEN;
  if (b < K1_LN) {
    const int xc = b & 7, kk = b >> 3;
    const int l0 = ((xc * 4 + (kk >> 2)) * 4 + (kk & 3)) * 16;
    uint32_t* tile = (uint32_t*)smem;
    uint32_t v[4][5];
#pragma unroll
    for (int rr = 0; rr < 4; ++rr) {
      const uint32_t* xr = (const uint32_t*)(x + (size_t)(l0 + wave * 4 + rr) * D_);
#pragma unroll
      for (int i = 0; i < 5; ++i) v[rr][i] = xr[lane + 64 * i];
    }
    float mean[4], q[4];
#pragma unroll
    for (int rr = 0; rr < 4; ++rr) {
      float s = 0.f;
#pragma unroll
      for (int i = 0; i < 5; ++i) s += bflo(v[rr][i]) + bfhi(v[rr][i]);
      mean[rr] = wave_sum(s) * (1.f / D_);
    }
#pragma unroll
    for (int rr = 0; rr < 4; ++rr) {
      float s = 0.f;
#pragma unroll
      for (int i = 0; i < 5; ++i) {
        float a = bflo(v[rr][i]) - mean[rr], c = bfhi(v[rr][i]) - mean[rr];
        s += a * a + c * c;
      }
      q[rr] = wave_sum(s);
    }
#pragma unroll
    for (int rr = 0; rr < 4; ++rr) {
      const int row = wave * 4 + rr;
      const float mb = bfr(mean[rr]);
      const float vb = bfr(q[rr] * (1.f / D_));
      const float sd = bfr(sqrtf(bfr(vb + 1e-5f)));
      const int l = l0 + row, r = l & 127;
      uint32_t* xo = (uint32_t*)xn + ((size_t)((l >> 7) * 10 + (lane >> 5)) * 128 + r) * 32 +
                     ((((lane >> 2) & 7) ^ ((r >> 1) & 7)) * 4) + (lane & 3);
#pragma unroll
      for (int i = 0; i < 5; ++i) {
        float a = bfr(bflo(v[rr][i]) - mb) / sd, c = bfr(bfhi(v[rr][i]) - mb) / sd;
        uint32_t o = pack2(a, c);
        xo[i * 2 * 128 * 32] = o;
        tile[row * 324 + lane + 64 * i] = o;
      }
    }
    __syncthreads();
    const u16* t16 = (const u16*)smem;
    for (int d = tid; d < D_; d += 256) {
      uint32_t o[8];
#pragma unroll
      for (int i = 0; i < 8; ++i)
        o[i] = (uint32_t)t16[(2 * i) * 648 + d] | ((uint32_t)t16[(2 * i + 1) * 648 + d] << 16);
      uint4* dst = (uint4*)(xnT + (size_t)d * L_ + l0);
      dst[0] = make_uint4(o[0], o[1], o[2], o[3]);
      dst[1] = make_uint4(o[4], o[5], o[6], o[7]);
    }
    return;
  }
  b -= K1_LN;
  if (b == 2 * K1_T1) {
    for (int i = tid; i < 2 * TB_STRIDE; i += 256) {
      const int sg = i >= TB_STRIDE;
      int m = i - sg * TB_STRIDE;
      if (m > TB_N - 1) m = TB_N - 1;
      const u16 bits = (u16)((TB_LO + m) | (sg << 15));
      const float hv = bf2f(bits);
      tbl[i] = f2bf(hv * tanh_sp_bf(hv));
    }
    return;
  }
  {
    const u16* src; u16* dst; int R, C;
    const bool first = b < K1_T1;
    if (first) { src = w1; dst = w1T; R = D_; C = F_; }
    else { b -= K1_T1; src = w2; dst = w2T; R = F_; C = D_; }
    const int tc = C / 64;
    const int r0 = (b / tc) * 64, c0 = (b % tc) * 64;
    u16* t16 = (u16*)smem;
#pragma unroll
    for (int i = 0; i < 2; ++i) {
      int rr = (tid >> 3) + 32 * i, ch = tid & 7;
      uint4 v = *(const uint4*)(src + (size_t)(r0 + rr) * C + c0 + ch * 8);
      *(uint4*)(t16 + rr * 72 + ch * 8) = v;
    }
    __syncthreads();
#pragma unroll
    for (int i = 0; i < 2; ++i) {
      int cc = (tid >> 3) + 32 * i, ch = tid & 7;
      uint32_t o[4];
#pragma unroll
      for (int k = 0; k < 4; ++k)
        o[k] = (uint32_t)t16[(ch * 8 + 2 * k) * 72 + cc] | ((uint32_t)t16[(ch * 8 + 2 * k + 1) * 72 + cc] << 16);
      const int n = c0 + cc, kt = r0 >> 6;
      size_t off;
      if (first) {
        const int tn = n / 112, r2 = n - tn * 112;
        const int sw = (r2 < 96) ? (((r2 >> 1) & 1) | (((r2 >> 3) & 3) << 1)) : ((r2 >> 1) & 7);
        off = ((size_t)(tn * 10 + kt) * 112 + r2) * 64 + ((ch ^ sw) * 8);
      } else {
        const int nt = n / 80, r = n - nt * 80;
        off = ((size_t)(nt * 27 + kt) * 80 + r) * 64 + ((ch ^ (r & 7)) * 8);
      }
      *(uint4*)(dst + off) = make_uint4(o[0], o[1], o[2], o[3]);
    }
  }
}

__global__ __launch_bounds__(256) void k1_prep(
    const u16* __restrict__ x, const u16* __restrict__ logAr, const u16* __restrict__ Aim,
    const u16* __restrict__ Cre, const u16* __restrict__ Cim, const u16* __restrict__ logdt,
    const u16* __restrict__ w1, const u16* __restrict__ w2,
    u16* __restrict__ xn, u16* __restrict__ xnT, u16* __restrict__ Kbuf,
    u16* __restrict__ w1T, u16* __restrict__ w2T, u16* __restrict__ tbl) {
  TS_BEGIN(0)
  k1_body(x, logAr, Aim, Cre, Cim, logdt, w1, w2, xn, xnT, Kbuf, w1T, w2T, tbl);
  TS_END(0)
}

constexpr int G1_STAGE = 240 * 128;
constexpr int K2_SY = 1024, K2_SX = 34816, K2_SZ = 66560;
constexpr int K2_R0 = 100352;
constexpr int K2_IMG = 4 * 4160 + 2 * 4608;
constexpr int K2_R1 = K2_R0 + K2_IMG;
constexpr int K2_KM = 4 * 4160;
constexpr int K2_Q = K2_R1 + K2_IMG;
constexpr int K2_CNT = 16;
constexpr int K2_ST = K2_Q + 8192;
constexpr int K2_LDS = K2_ST + 256;

typedef __attribute__((address_space(3))) const char* lds_cptr;
typedef short v4i16_t __attribute__((ext_vector_type(4)));
typedef unsigned short u16x2 __attribute__((ext_vector_type(2)));
__device__ __forceinline__ v4i16_t trd(lds_cptr p) {
  return __builtin_amdgcn_ds_read_tr16_b64_v4i16((__attribute__((address_space(3))) v4i16_t*)p);
}
__device__ __forceinline__ bf16x8 rd16(lds_cptr p) { return *(const __attribute__((address_space(3))) bf16x8*)p; }
#define SBAR() __builtin_amdgcn_sched_barrier(0)

template <int OFF>
__device__ __forceinline__ void garrive(uint32_t addr, int lane) {
  asm volatile("" ::: "memory");
  __builtin_amdgcn_s_waitcnt(0xC07F);
  asm volatile("" ::: "memory");
  if (lane == 0) asm volatile("ds_add_u32 %0, %1 offset:%2" ::"v"(addr), "v"(1u), "n"(OFF) : "memory");
}
template <int OFF>
__device__ __forceinline__ void gwait(uint32_t addr, uint32_t target) {
  while (true) {
    uint32_t c;
    asm volatile("ds_read_b32 %0, %1 offset:%2\n\ts_waitcnt lgkmcnt(0)" : "=v"(c) : "v"(addr), "n"(OFF) : "memory");
    if ((uint32_t)__builtin_amdgcn_readfirstlane(c) >= target) break;
    __builtin_amdgcn_s_sleep(1);
  }
}

template <int MASK, class Hook>
__device__ __forceinline__ void conv_mfma(f32x4 (&acc)[8], lds_cptr ka, lds_cptr ub, Hook&& hook) {
  constexpr int T = (MASK & 0x80) ? 7 : 5;
  bf16x8 ah[2][8], al[2][8];
  v4i16_t bx[2][8], by[2][8];
#define CAT(x, y) __builtin_shufflevector(x, y, 0, 1, 2, 3, 4, 5, 6, 7)
  asm volatile("; conv %0" ::"n"(MASK) : "memory");
  bx[0][0] = trd(ub + 8); by[0][0] = trd(ub);
#pragma unroll
  for (int b = 0; b <= T; ++b) { ah[0][b] = rd16(ka + 256 + 512 * b); al[0][b] = rd16(ka + 256 + 512 * b + 4608); }
#pragma unroll
  for (int a = 1; a <= T; ++a) { bx[0][a] = trd(ub + 512 * a + 8); by[0][a] = trd(ub + 512 * a); }
  SBAR();
  static_for<2>([&](auto RR) {
    constexpr int rr = decltype(RR)::value;
    static_for<T + 1>([&](auto AA) {
      constexpr int a = decltype(AA)::value;
      const bf16x8 bf = CAT(bx[rr][a], by[rr][a]);
      int cnt = 0, pend = -1;
#pragma unroll
      for (int bb = 0; bb <= T - a; ++bb) {
        const int b = (rr == 1 && a == 0) ? T - bb : bb;
        if (!((MASK >> (a + b)) & 1)) continue;
        acc[a + b] = __builtin_amdgcn_mfma_f32_16x16x32_bf16(bf, ah[rr][b], acc[a + b], 0, 0, 0);
        if (cnt == 0 && rr == 0) { bx[1][a] = trd(ub + 256 + 512 * a + 8); by[1][a] = trd(ub + 256 + 512 * a); }
        if (rr == 1 && cnt == (a == 0 ? 2 : 0)) hook(AA, std::integral_constant<int, MASK>{});
        SBAR();
        if (pend >= 0) {
          acc[a + pend] = __builtin_amdgcn_mfma_f32_16x16x32_bf16(bf, al[rr][pend], acc[a + pend], 0, 0, 0);
          SBAR();
        }
        pend = b; ++cnt;
      }
      acc[a + pend] = __builtin_amdgcn_mfma_f32_16x16x32_bf16(bf, al[rr][pend], acc[a + pend], 0, 0, 0);
      if (rr == 0) { ah[1][T - a] = rd16(ka + 512 * (T - a)); al[1][T - a] = rd16(ka + 512 * (T - a) + 4608); }
      SBAR();
    });
  });
#undef CAT
}

typedef uint32_t u32x4 __attribute__((ext_vector_type(4)));
typedef uint32_t u32x2 __attribute__((ext_vector_type(2)));
struct ConvRow { u32x4 uv, k0, k1; u32x2 up; };
__device__ __forceinline__ void conv_fetch(ConvRow& r, const u16* __restrict__ xnT, const u16* __restrict__ Kbuf, int d, uint32_t o16) {
  const char* us = (const char*)(xnT + (size_t)d * L_);
  const char* ks = (const char*)(Kbuf + (size_t)d * 4096);
  const uint32_t o8 = o16 ? o16 - 8u : 0u, o2 = o16 + 4096u;
  asm volatile("global_load_dwordx4 %0, %1, %2" : "=v"(r.uv) : "v"(o16), "s"(us) : "memory");
  asm volatile("global_load_dwordx2 %0, %1, %2" : "=v"(r.up) : "v"(o8), "s"(us) : "memory");
  asm volatile("global_load_dwordx4 %0, %1, %2" : "=v"(r.k0) : "v"(o16), "s"(ks) : "memory");
  asm volatile("global_load_dwordx4 %0, %1, %2" : "=v"(r.k1) : "v"(o2), "s"(ks) : "memory");
}
__device__ __forceinline__ void conv_ready(ConvRow& r) {
  asm volatile("s_waitcnt vmcnt(0)" : "+v"(r.uv), "+v"(r.up), "+v"(r.k0), "+v"(r.k1)::"memory");
}
__device__ __forceinline__ void conv_put(const ConvRow& r, char* img, int ct) {
  const uint32_t pm2 = ct ? r.up.x : 0u, pm1 = ct ? r.up.y : 0u;
  const uint32_t am = __builtin_amdgcn_alignbit(pm1, pm2, 16), a0 = __builtin_amdgcn_alignbit(r.uv.x, pm1, 16);
  const uint32_t a1 = __builtin_amdgcn_alignbit(r.uv.y, r.uv.x, 16), a2 = __builtin_amdgcn_alignbit(r.uv.z, r.uv.y, 16);
  const uint32_t a3 = __builtin_amdgcn_alignbit(r.uv.w, r.uv.z, 16);
  char* ud = img + 64 + ct * 16;
  *(u32x4*)(ud) = r.uv;
  *(uint4*)(ud + 4160) = make_uint4(a0, a1, a2, a3);
  *(uint4*)(ud + 2 * 4160) = make_uint4(pm1, r.uv.x, r.uv.y, r.uv.z);
  *(uint4*)(ud + 3 * 4160) = make_uint4(am, a0, a1, a2);
  *(u32x4*)(img + K2_KM + 512 + ct * 16) = r.k0;
  *(u32x4*)(img + K2_KM + 4608 + 512 + ct * 16) = r.k1;
  if (ct < 64) *(uint4*)(img + K2_KM + (ct >> 5) * 4608 + (ct & 31) * 16) = make_uint4(0, 0, 0, 0);
  else if (ct < 80) *(uint4*)(img + ((ct >> 2) & 3) * 4160 + (ct & 3) * 16) = make_uint4(0, 0, 0, 0);
}

__device__ __forceinline__ void k2_body(
    const u16* __restrict__ xn, const u16* __restrict__ xnT, const u16* __restrict__ Kbuf,
    const u16* __restrict__ Dskip, const u16* __restrict__ w1T, const u16* __restrict__ b1,
    const u16* __restrict__ tbl, u16* __restrict__ act, u16* __restrict__ AoutT) {
  __shared__ __attribute__((aligned(16))) char smem[K2_LDS];
  const int tid = threadIdx.x, lane = tid & 63;
  const int wave = __builtin_amdgcn_readfirstlane(tid >> 6);
  const int bid = blockIdx.x;
  const uint32_t sbase = (uint32_t)(size_t)(lds_cptr)smem;
  constexpr int cF = K2_CNT, cE = cF + 48, cA = cE + 48, cB = cA + 12, cP = cB + 12, cR = cP + 12;
  const int ch2 = (bid < 128) ? 512 + bid : 384 + bid;
  const int xc = bid & 7, jj = bid >> 3;
  const int tm = (xc & 3) * 4 + (jj & 3), tn = (xc >> 2) * 8 + (jj >> 2);
  const int m0 = tm * 128, n0 = tn * 112;
  if (wave >= 8) {
    const int lw = wave - 8;
    const uint32_t vo = (uint32_t)lane * 16u;
    const char* ga = (const char*)(xn + (size_t)tm * 10 * 8192);
    const char* gb = (const char*)(w1T + (size_t)tn * 10 * 7168);
    const int nvalid = (tn == 15) ? 6 : 14;
    auto stage = [&](int kt, int off) {
      const char* pa = ga + kt * 16384;
      const char* pb = gb + kt * 14336;
#pragma unroll
      for (int i = 0; i < 16; ++i)
        __builtin_amdgcn_global_load_lds((const uint32_t*)(pa + i * 1024 + vo), (uint32_t*)(smem + off + i * 1024), 16, 0, 0);
#pragma unroll
      for (int i = 0; i < 14; ++i)
        __builtin_amdgcn_global_load_lds((const uint32_t*)(pb + (i < nvalid ? i * 1024 : 0) + vo),
                                         (uint32_t*)(smem + off + (16 + i) * 1024), 16, 0, 0);
    };
    __builtin_amdgcn_s_setprio(3);
    const int lt = tid - 512;
    const uint32_t o16 = (uint32_t)lt * 16u;
    ConvRow r2;
    uint4 u0, u1;
#define D1 10
#define D2 24
#define D3 18
    if (lw == 0) stage(0, K2_SX);
    __builtin_amdgcn_s_barrier();
    if (lw == 1) __builtin_amdgcn_s_sleep(D1);
    else if (lw == 2) __builtin_amdgcn_s_sleep(D2);
    else if (lw == 3) __builtin_amdgcn_s_sleep(D3);
    if (lw == 1 || lw == 2) stage(lw, lw == 1 ? K2_SY : K2_SZ);
    conv_fetch(r2, xnT, Kbuf, bid + 256, o16);
    if (lw < 3) {
      asm volatile("s_waitcnt vmcnt(4)" ::: "memory");
      garrive<cF>(sbase + lw * 4, lane);
    }
    conv_ready(r2);
    conv_put(r2, smem + K2_R1, lt);
    garrive<cA + 4>(sbase, lane);
#pragma unroll 1
    for (int kt = (lw == 3) ? 3 : lw + 4; kt < 12; kt += 4) {
      {
        gwait<cE>(sbase + (kt - 3) * 4, 4);
        if (kt < 10) {
          const int r3 = kt % 3;
          stage(kt, r3 == 0 ? K2_SX : r3 == 1 ? K2_SY : K2_SZ);
        } else {
          const int h = kt - 10;
#pragma unroll
          for (int i = 0; i < 8; ++i)
            __builtin_amdgcn_global_load_lds((const uint32_t*)((const char*)(tbl + h * TB_STRIDE) + i * 1024 + vo),
                                             (uint32_t*)(smem + h * 65536 + TB_LO * 2 + i * 1024), 16, 0, 0);
        }
      }
      asm volatile("s_waitcnt vmcnt(0)" ::: "memory");
      garrive<cF>(sbase + (kt < 10 ? kt : 10) * 4, lane);
    }
    conv_fetch(r2, xnT, Kbuf, ch2, o16);
    u0 = *(const uint4*)((const char*)(xnT + (size_t)bid * L_) + o16);
    u1 = *(const uint4*)((const char*)(xnT + (size_t)(bid + 256) * L_) + o16);
    const int rsw = ((lt >> 2) & 3) ^ ((lt >> 4) & 1);
    const int rc0 = (lt >> 1) * 64 + (((lt & 1) * 2) ^ rsw) * 16, rc1 = (lt >> 1) * 64 + (((lt & 1) * 2 + 1) ^ rsw) * 16;
    f32x4 s0, s1;
    auto red_load = [&](const char* Pr) {
      const char* Q = smem + K2_Q;
      s0 = (*(const f32x4*)(Pr + rc0) + *(const f32x4*)(Pr + 8192 + rc0)) + (*(const f32x4*)(Pr + 16384 + rc0) + *(const f32x4*)(Q + rc0));
      s1 = (*(const f32x4*)(Pr + rc1) + *(const f32x4*)(Pr + 8192 + rc1)) + (*(const f32x4*)(Pr + 16384 + rc1) + *(const f32x4*)(Q + rc1));
    };
    auto red_store = [&](int d, const uint4& ucur) {
      const uint32_t dw = ((const uint32_t*)Dskip)[d >> 1];
      const float y[8] = {s0[0], s0[1], s0[2], s0[3], s1[0], s1[1], s1[2], s1[3]};
      const float dsk = (d & 1) ? bfhi(dw) : bflo(dw);
      const uint32_t uu[4] = {ucur.x, ucur.y, ucur.z, ucur.w};
      uint32_t o4[4];
#pragma unroll
      for (int i = 0; i < 4; ++i) {
        float r0 = bfr(y[2 * i]) + bfr(bflo(uu[i]) * dsk);
        float r1 = bfr(y[2 * i + 1]) + bfr(bfhi(uu[i]) * dsk);
        o4[i] = pack2(r0, r1);
      }
      *(uint4*)((char*)(AoutT + (size_t)d * L_) + o16) = make_uint4(o4[0], o4[1], o4[2], o4[3]);
    };
    gwait<cP>(sbase, 4);
    red_load(smem + K2_R0);
    garrive<cR>(sbase, lane);
    gwait<cR>(sbase, 4);
    conv_ready(r2);
    conv_put(r2, smem + K2_R0, lt);
    garrive<cA + 8>(sbase, lane);
    red_store(bid, u0);
    gwait<cP + 4>(sbase, 4);
    red_load(smem + K2_R1);
    garrive<cR + 4>(sbase, lane);
    red_store(bid + 256, u1);
    gwait<cP + 8>(sbase, 4);
    if ((((bid < 128) ? 0xC3 : 0x3C) >> (lt >> 5)) & 1) {
      red_load(smem + K2_R0);
      red_store(ch2, make_uint4(r2.uv.x, r2.uv.y, r2.uv.z, r2.uv.w));
    }
    return;
  }
  if (wave < 4) {
    const int fr = lane & 15, fq = lane >> 4;
    STAMP(0)
    if (tid < 36) *(uint32_t*)(smem + K2_CNT + tid * 4) = 0;
    if (tid == 40) *(uint32_t*)(smem) = 0;
    if (tid == 41) *(uint32_t*)(smem + 65536) = 0x8000u;
    asm volatile("s_waitcnt lgkmcnt(0)" ::: "memory");
    __builtin_amdgcn_s_barrier();
#define PG 2
#define PC 1
#define PE 0
    __builtin_amdgcn_s_setprio(PG);
    uint4 bb[3]; uint2 bb3;
#pragma unroll
    for (int p = 0; p < 3; ++p) {
      int n = n0 + 32 * p + 8 * fq;
      if (n > F_ - 8) n = F_ - 8;
      bb[p] = *(const uint4*)(b1 + n);
    }
    {
      int n = n0 + 96 + 4 * fq;
      if (n > F_ - 4) n = F_ - 4;
      bb3 = *(const uint2*)(b1 + n);
    }
    STAMP(1)
    int aoff[7], boff[2];
#pragma unroll
    for (int i = 0; i < 7; ++i) {
      const int r2 = (i < 6) ? (32 * (i >> 1) + 8 * (fr >> 2) + 4 * (i & 1) + (fr & 3)) : (96 + fr);
      const int sw = (i < 6) ? (((r2 >> 1) & 1) | (((r2 >> 3) & 3) << 1)) : ((r2 >> 1) & 7);
      aoff[i] = (128 + r2) * 128 + ((fq ^ sw) * 16);
    }
#pragma unroll
    for (int j = 0; j < 2; ++j) {
      const int row = wave * 32 + j * 16 + fr;
      boff[j] = row * 128 + ((fq ^ ((row >> 1) & 7)) * 16);
    }
    f32x4 acc[7][2];
#pragma unroll
    for (int i = 0; i < 7; ++i)
#pragma unroll
      for (int j = 0; j < 2; ++j) acc[i][j] = (f32x4){0.f, 0.f, 0.f, 0.f};
    int off = K2_SX;
    bf16x8 af[2][7], bfm[2][2];
    gwait<cF>(sbase, 1);
    {
      const char* base = smem + off;
#pragma unroll
      for (int j = 0; j < 2; ++j) bfm[0][j] = *(const bf16x8*)(base + boff[j]);
#pragma unroll
      for (int i = 0; i < 7; ++i) af[0][i] = *(const bf16x8*)(base + aoff[i]);
    }
#pragma unroll 1
    for (int kt = 0; kt < 10; ++kt) {
      STAMPK(2 + kt)
      const char* base = smem + off;
      off = (off == K2_SX) ? K2_SY : (off == K2_SY) ? K2_SZ : K2_SX;
      uint32_t pc = 0;
      SBAR();
#pragma unroll
      for (int ks = 0; ks < 2; ++ks) {
#pragma unroll
        for (int i = 0; i < 7; ++i) {
          acc[i][0] = __builtin_amdgcn_mfma_f32_16x16x32_bf16(af[ks][i], bfm[ks][0], acc[i][0], 0, 0, 0);
          if (ks == 0) {
            if (i < 2) bfm[1][i] = *(const bf16x8*)(base + (boff[i] ^ 64));
            af[1][i] = *(const bf16x8*)(base + (aoff[i] ^ 64));
          }
          if (ks == 1 && i == 0)
            asm volatile("ds_read_b32 %0, %1 offset:%2" : "+v"(pc) : "v"(sbase + kt * 4), "n"(cF + 4) : "memory");
          SBAR();
          acc[i][1] = __builtin_amdgcn_mfma_f32_16x16x32_bf16(af[ks][i], bfm[ks][1], acc[i][1], 0, 0, 0);
          if (ks == 1 && i == 2) {
            garrive<cE>(sbase + kt * 4, lane);
            asm volatile("" : "+v"(pc)::"memory");
            if (kt < 9) {
              if (__builtin_amdgcn_readfirstlane(pc) == 0) gwait<cF + 4>(sbase + kt * 4, 1);
              const char* nb = smem + off;
#pragma unroll
              for (int j = 0; j < 2; ++j) bfm[0][j] = *(const bf16x8*)(nb + boff[j]);
#pragma unroll
              for (int i2 = 0; i2 < 7; ++i2) af[0][i2] = *(const bf16x8*)(nb + aoff[i2]);
            }
          }
          SBAR();
        }
      }
    }
    __builtin_amdgcn_s_setprio(PE);
    gwait<cF>(sbase + 40, 2);
    STAMP(12)
    u16* dummy = (u16*)tbl + 2 * TB_STRIDE;
    u16x2 vmax = {0, 0}, vmin = {0xffff, 0xffff};
    auto epi = [&](auto slow_tag) {
      constexpr bool SLOW = decltype(slow_tag)::value;
      auto hval = [&](float a0, float a1, uint32_t bias) -> uint32_t {
        const uint32_t r = pack2(a0, a1);
        return pack2(bflo(r) + bflo(bias), bfhi(r) + bfhi(bias));
      };
      auto act2 = [&](uint32_t h) -> uint32_t {
        if (SLOW) {
          return pack2(bflo(h) * tanh_sp_bf(bflo(h)), bfhi(h) * tanh_sp_bf(bfhi(h)));
        } else {
          const u16x2 m = __builtin_bit_cast(u16x2, h & 0x7fff7fffu);
          vmax = __builtin_elementwise_max(vmax, m);
          vmin = __builtin_elementwise_min(vmin, (u16x2)(m - (u16x2){1, 1}));
          uint32_t alo, ahi;
          asm("v_lshlrev_b32_sdwa %0, %1, %2 dst_sel:DWORD dst_unused:UNUSED_PAD src0_sel:DWORD src1_sel:WORD_0" : "=v"(alo) : "v"(1u), "v"(h));
          asm("v_lshlrev_b32_sdwa %0, %1, %2 dst_sel:DWORD dst_unused:UNUSED_PAD src0_sel:DWORD src1_sel:WORD_1" : "=v"(ahi) : "v"(1u), "v"(h));
          const uint32_t lo = *(const u16*)(smem + alo);
          const uint32_t hi = *(const u16*)(smem + ahi);
          return lo | (hi << 16);
        }
      };
#pragma unroll
      for (int j = 0; j < 2; ++j) {
        const int m = m0 + wave * 32 + j * 16 + fr;
        u16* arow = act + ((size_t)(m >> 6) * 27 * 64 + (m & 63)) * 64;
        uint32_t h[14];
#pragma unroll
        for (int p = 0; p < 3; ++p) {
          h[4 * p + 0] = hval(acc[2 * p][j][0], acc[2 * p][j][1], bb[p].x);
          h[4 * p + 1] = hval(acc[2 * p][j][2], acc[2 * p][j][3], bb[p].y);
          h[4 * p + 2] = hval(acc[2 * p + 1][j][0], acc[2 * p + 1][j][1], bb[p].z);
          h[4 * p + 3] = hval(acc[2 * p + 1][j][2], acc[2 * p + 1][j][3], bb[p].w);
        }
        h[12] = hval(acc[6][j][0], acc[6][j][1], bb3.x);
        h[13] = hval(acc[6][j][2], acc[6][j][3], bb3.y);
#pragma unroll
        for (int i = 0; i < 14; ++i) h[i] = act2(h[i]);
#pragma unroll
        for (int p = 0; p < 3; ++p) {
          const int n = n0 + 32 * p + 8 * fq;
          u16* dst = (n < F_) ? arow + (n >> 6) * 4096 + ((((n >> 3) & 7) ^ (m & 7)) * 8) : dummy;
          *(uint4*)dst = make_uint4(h[4 * p], h[4 * p + 1], h[4 * p + 2], h[4 * p + 3]);
        }
        {
          const int n = n0 + 96 + 4 * fq;
          u16* dst = (n < F_) ? arow + (n >> 6) * 4096 + ((((n >> 3) & 7) ^ (m & 7)) * 8) + (n & 7) : dummy;
          *(uint2*)dst = make_uint2(h[12], h[13]);
        }
      }
    };
    epi(std::false_type{});
    STAMP(14)
    {
      const uint32_t mx = max((uint32_t)vmax.x, (uint32_t)vmax.y), mn = min((uint32_t)vmin.x, (uint32_t)vmin.y);
      if (__builtin_amdgcn_ballot_w64(mx > (uint32_t)TB_HI || mn < (uint32_t)(TB_LO - 1)) != 0) epi(std::true_type{});
    }
    STAMP(13)
    STAMP_DUMP(0, 15)
    return;
  }
  const int cw = wave - 4, ct = tid - 256;
  {
    ConvRow r0;
    conv_fetch(r0, xnT, Kbuf, bid, (uint32_t)ct * 16u);
    __builtin_amdgcn_s_barrier();
    __builtin_amdgcn_s_setprio(PC);
    conv_ready(r0);
    conv_put(r0, smem + K2_R0, ct);
    garrive<cA>(sbase, lane);
  }
  const int fr = lane & 15, kg = lane >> 4;
  lds_cptr ub0 = (lds_cptr)(smem + K2_R0 + ((lane >> 2) & 3) * 4160 + 32 * (kg >> 1) - 16 * (kg & 1) + (lane & 3) * 8 + 56 + 64 * cw);
  lds_cptr ka0 = (lds_cptr)(smem + K2_R0 + K2_KM + 512 + 32 * fr - 32 * (kg >> 1) + 16 * (kg & 1) - 64 * cw - 256);
  const int pwo = fr * 64 + ((kg ^ ((fr >> 1) & 3) ^ (fr >> 3)) * 16);
  gwait<cA>(sbase, 4);
#pragma unroll 1
  for (int task = 0; task < 3; ++task) {
    STAMP(17 + task * 5)
    const int ro = (task == 1) ? K2_IMG : 0;
    lds_cptr ub = ub0 + ro, ka = ka0 + ro;
    f32x4 acc[8];
#pragma unroll
    for (int i = 0; i < 8; ++i) acc[i] = (f32x4){0.f, 0.f, 0.f, 0.f};
    const int mask = (task < 2) ? 0xFF : (bid < 128 ? 0xC3 : 0x3C);
    char* Pw = smem + ((cw == 3) ? K2_Q : K2_R0 + ro + cw * 8192) + pwo;
    const uint32_t qaddr = sbase + ((cw == 3 && task > 0) ? cR - 4 : cB) + task * 4;
    uint32_t pb = 0, pq = 0;
    bool early = false;
    auto hook = [&](auto AA, auto MK) {
      constexpr int a = decltype(AA)::value, mk = decltype(MK)::value;
      if constexpr (a == 0) garrive<cB>(sbase + task * 4, lane);
      if constexpr (a == 1) {
        asm volatile("ds_read_b32 %0, %1 offset:%2" : "+v"(pb) : "v"(sbase + task * 4), "n"(cB) : "memory");
        asm volatile("ds_read_b32 %0, %1" : "+v"(pq) : "v"(qaddr) : "memory");
      }
      if constexpr (a == 2) {
        __builtin_amdgcn_s_waitcnt(0xC07F);
        asm volatile("" : "+v"(pb), "+v"(pq)::"memory");
        early = __builtin_amdgcn_readfirstlane(min(pb, pq)) >= 4;
        if constexpr (mk & 1) { if (early) *(f32x4*)(Pw) = acc[0]; }
      }
      if constexpr (a > 2) {
        if (!early) {
          uint32_t c0, c1;
          asm volatile("ds_read_b32 %0, %2 offset:%4\n\tds_read_b32 %1, %3\n\ts_waitcnt lgkmcnt(0)"
                       : "=&v"(c0), "=&v"(c1) : "v"(sbase + task * 4), "v"(qaddr), "n"(cB) : "memory");
          early = __builtin_amdgcn_readfirstlane(min(c0, c1)) >= 4;
          if (early) {
#pragma unroll
            for (int i = 0; i < a - 1; ++i)
              if ((mk >> i) & 1) *(f32x4*)(Pw + i * 1024) = acc[i];
          }
        }
      }
      if constexpr (a >= 2) {
        if constexpr ((mk >> (a - 1)) & 1) { if (early) *(f32x4*)(Pw + (a - 1) * 1024) = acc[a - 1]; }
      }
    };
    if (mask == 0xFF) conv_mfma<0xFF>(acc, ka, ub, hook);
    else if (mask == 0xC3) conv_mfma<0xC3>(acc, ka, ub, hook);
    else conv_mfma<0x3C>(acc, ka, ub, hook);
    STAMP(18 + task * 5)
    if (early) {
      if (mask & 0x80) *(f32x4*)(Pw + 7 * 1024) = acc[7];
      else *(f32x4*)(Pw + 5 * 1024) = acc[5];
    } else {
      gwait<cB>(sbase + task * 4, 4);
      gwait<0>(qaddr, 4);
#pragma unroll
      for (int i = 0; i < 8; ++i)
        if ((mask >> i) & 1) *(f32x4*)(Pw + i * 1024) = acc[i];
    }
    uint32_t pa = 0;
    asm volatile("ds_read_b32 %0, %1 offset:%2" : "+v"(pa) : "v"(sbase + task * 4), "n"(cA + 4) : "memory");
    garrive<cP>(sbase + task * 4, lane);
    asm volatile("" : "+v"(pa)::"memory");
    STAMP(19 + task * 5)
    if (task < 2) {
      if ((uint32_t)__builtin_amdgcn_readfirstlane(pa) < 4u) gwait<cA + 4>(sbase + task * 4, 4);
    }
  }
  STAMP(31)
  STAMP_DUMP(16, 32)
}

__global__ __launch_bounds__(768, 1) void k2_conv_gemm1(
    const u16* __restrict__ xn, const u16* __restrict__ xnT, const u16* __restrict__ Kbuf,
    const u16* __restrict__ Dskip, const u16* __restrict__ w1T, const u16* __restrict__ b1,
    const u16* __restrict__ tbl, u16* __restrict__ act, u16* __restrict__ AoutT) {
  TS_BEGIN(1)
  k2_body(xn, xnT, Kbuf, Dskip, w1T, b1, tbl, act, AoutT);
  TS_END(1)
}

constexpr int K3_STAGE = 144 * 128;
#define K3_NB 4
#define K3_PF 0
constexpr int K3_NBUF = K3_NB;
constexpr int K3_AO_OFF = K3_STAGE * K3_NBUF;
constexpr int K3_LDS = K3_AO_OFF + 80 * 128;

__device__ __forceinline__ void k3_body(
    const u16* __restrict__ act, const u16* __restrict__ w2T, const u16* __restrict__ x,
    const u16* __restrict__ AoutT, u16* __restrict__ out) {
  __shared__ __attribute__((aligned(16))) char smem[K3_LDS];
  const int tid = threadIdx.x, lane = tid & 63;
  const int wave = __builtin_amdgcn_readfirstlane(tid >> 6);
  const int bid = blockIdx.x;
  const int xc = bid & 7, jj = bid >> 3;
  const int rb = (xc & 3) * 8 + (jj & 7), nt = (xc >> 2) * 4 + (jj >> 3);
  const int m0 = rb * 64, n0 = nt * 80;
  constexpr int NT = F_ / 64;
  f32x4 acc[5][2];
#pragma unroll
  for (int i = 0; i < 5; ++i)
#pragma unroll
    for (int j = 0; j < 2; ++j) acc[i][j] = (f32x4){0.f, 0.f, 0.f, 0.f};
  const int fr = lane & 15, fq = lane >> 4;
  const int wm = wave & 1, ksp = (wave >> 1) & 1;
  const int em0 = tid / 10, ec0 = (tid % 10) * 8;
  const int em1 = (512 + tid) / 10, ec1 = ((512 + tid) % 10) * 8;
  uint4 xv0, xv1 = make_uint4(0, 0, 0, 0);
  if (wave >= 4) {
    const int lw = wave - 4;
    const u16* gsrc[5];
#pragma unroll
    for (int i = 0; i < 5; ++i) {
      int ch = lw + 4 * i;
      if (ch > 17) ch = 17;
      gsrc[i] = (ch < 8 ? act + (size_t)rb * 27 * 4096 + ch * 512 : w2T + (size_t)nt * 27 * 5120 + (ch - 8) * 512) + lane * 8;
    }
    auto stage = [&](int kt, int buf) {
#pragma unroll
      for (int i = 0; i < 4; ++i)
        __builtin_amdgcn_global_load_lds((const uint32_t*)(gsrc[i] + kt * (i < 2 ? 4096 : 5120)),
                                         (uint32_t*)(smem + buf * K3_STAGE + (lw + 4 * i) * 1024), 16, 0, 0);
      if (lw < 2)
        __builtin_amdgcn_global_load_lds((const uint32_t*)(gsrc[4] + kt * 5120),
                                         (uint32_t*)(smem + buf * K3_STAGE + (lw + 16) * 1024), 16, 0, 0);
    };
#pragma unroll
    for (int i = 0; i < K3_NBUF - 1; ++i) stage(i, i);
    int buf = K3_NBUF - 1;
    for (int kt = 0; kt < NT - (K3_NBUF - 1); ++kt) {
      if (lw < 2) asm volatile("s_waitcnt vmcnt(%0)" ::"n"(5 * (K3_NBUF - 2)) : "memory");
      else asm volatile("s_waitcnt vmcnt(%0)" ::"n"(4 * (K3_NBUF - 2)) : "memory");
      __builtin_amdgcn_s_barrier();
      stage(kt + K3_NBUF - 1, buf);
      buf = (buf == K3_NBUF - 1) ? 0 : buf + 1;
    }
    static_for<K3_NBUF - 1>([&](auto I) {
      constexpr int rem = K3_NBUF - 2 - decltype(I)::value;
      if (lw < 2) asm volatile("s_waitcnt vmcnt(%0)" ::"n"(5 * rem) : "memory");
      else asm volatile("s_waitcnt vmcnt(%0)" ::"n"(4 * rem) : "memory");
      __builtin_amdgcn_s_barrier();
    });
    xv0 = *(const uint4*)(x + (size_t)(m0 + em0) * D_ + n0 + ec0);
  } else {
#pragma unroll
    for (int i = 0; i < 3; ++i) {
      const int ch = wave + 4 * i;
      if (ch < 10) {
        const int row = ch * 8 + (lane >> 3);
        __builtin_amdgcn_global_load_lds((const uint32_t*)(AoutT + (size_t)(n0 + row) * L_ + m0 + (lane & 7) * 8),
                                         (uint32_t*)(smem + K3_AO_OFF + ch * 1024), 16, 0, 0);
      }
    }
    xv0 = *(const uint4*)(x + (size_t)(m0 + em0) * D_ + n0 + ec0);
    if (tid < 128) xv1 = *(const uint4*)(x + (size_t)(m0 + em1) * D_ + n0 + ec1);
    int aoff[5], boff[2];
#pragma unroll
    for (int i = 0; i < 5; ++i) {
      const int row = 64 + i * 16 + fr;
      aoff[i] = row * 128 + (((ksp * 4 + fq) ^ (row & 7)) * 16);
    }
#pragma unroll
    for (int j = 0; j < 2; ++j) {
      const int row = wm * 32 + j * 16 + fr;
      boff[j] = row * 128 + (((ksp * 4 + fq) ^ (row & 7)) * 16);
    }
    int buf = 0;
    for (int kt = 0; kt < NT; ++kt) {
      asm volatile("s_waitcnt lgkmcnt(0)" ::: "memory");
      __builtin_amdgcn_s_barrier();
      const char* base = smem + buf * K3_STAGE;
      buf = (buf == K3_NBUF - 1) ? 0 : buf + 1;
      bf16x8 af[5], bfm[2];
#pragma unroll
      for (int j = 0; j < 2; ++j) bfm[j] = *(const bf16x8*)(base + boff[j]);
#pragma unroll
      for (int i = 0; i < 5; ++i) af[i] = *(const bf16x8*)(base + aoff[i]);
#pragma unroll
      for (int i = 0; i < 5; ++i)
#pragma unroll
        for (int j = 0; j < 2; ++j)
          acc[i][j] = __builtin_amdgcn_mfma_f32_16x16x32_bf16(af[i], bfm[j], acc[i][j], 0, 0, 0);
    }
  }
  __syncthreads();
  float* red = (float*)smem;
  if (wave < 4) {
#pragma unroll
    for (int i = 0; i < 5; ++i)
#pragma unroll
      for (int j = 0; j < 2; ++j) {
        const int m = wm * 32 + j * 16 + fr, n = i * 16 + fq * 4;
        *(f32x4*)(red + (ksp * 64 + m) * 84 + n) = acc[i][j];
      }
  }
  __syncthreads();
  const u16* ao = (const u16*)(smem + K3_AO_OFF);
#pragma unroll
  for (int it = 0; it < 2; ++it) {
    if (it == 1 && tid >= 128) break;
    const int m = it ? em1 : em0, c8 = it ? ec1 : ec0;
    const uint4 xv = it ? xv1 : xv0;
    const f32x4* r0 = (const f32x4*)(red + m * 84 + c8);
    const f32x4* r1 = (const f32x4*)(red + (64 + m) * 84 + c8);
    const f32x4 s0 = r0[0] + r1[0], s1 = r0[1] + r1[1];
    const float s[8] = {s0[0], s0[1], s0[2], s0[3], s1[0], s1[1], s1[2], s1[3]};
    const uint32_t xx[4] = {xv.x, xv.y, xv.z, xv.w};
    uint32_t o[4];
#pragma unroll
    for (int i = 0; i < 4; ++i) {
      float a0 = bf2f(ao[(c8 + 2 * i) * 64 + m]), a1 = bf2f(ao[(c8 + 2 * i + 1) * 64 + m]);
      float v0 = bfr(bflo(xx[i]) + a0) + bfr(s[2 * i]);
      float v1 = bfr(bfhi(xx[i]) + a1) + bfr(s[2 * i + 1]);
      o[i] = pack2(v0, v1);
    }
    *(uint4*)(out + (size_t)(m0 + m) * D_ + n0 + c8) = make_uint4(o[0], o[1], o[2], o[3]);
  }
}

__global__ __launch_bounds__(512, 1) void k3_gemm2(
    const u16* __restrict__ act, const u16* __restrict__ w2T, const u16* __restrict__ x,
    const u16* __restrict__ AoutT, u16* __restrict__ out) {
  TS_BEGIN(2)
  k3_body(act, w2T, x, AoutT, out);
  TS_END(2)
}

}

extern "C" void kernel_launch(void* const* d_in, const int* in_sizes, int n_in,
                              void* d_out, int out_size, void* d_ws, size_t ws_size,
                              hipStream_t stream) {
  const u16* x = (const u16*)d_in[0];
  const u16* logAr = (const u16*)d_in[1];
  const u16* Aim = (const u16*)d_in[2];
  const u16* Cre = (const u16*)d_in[3];
  const u16* Cim = (const u16*)d_in[4];
  const u16* logdt = (const u16*)d_in[5];
  const u16* Dskip = (const u16*)d_in[6];
  const u16* w1 = (const u16*)d_in[8];
  const u16* b1 = (const u16*)d_in[9];
  const u16* w2 = (const u16*)d_in[10];
  u16* out = (u16*)d_out;
  u16* ws = (u16*)d_ws;
  u16* xn = ws;
  u16* xnT = xn + (size_t)L_ * D_;
  u16* Kbuf = xnT + (size_t)L_ * D_;
  u16* w1T = Kbuf + (size_t)D_ * 4096;
  u16* w2T = w1T + (size_t)D_ * 1792;
  u16* act = w2T + (size_t)D_ * F_;
  u16* AoutT = act + (size_t)L_ * F_;
  u16* tbl = AoutT + (size_t)L_ * D_;
  k1_prep<<<K1_BLOCKS, 256, 0, stream>>>(x, logAr, Aim, Cre, Cim, logdt, w1, w2, xn, xnT, Kbuf, w1T, w2T, tbl);
  k2_conv_gemm1<<<256, 768, 0, stream>>>(xn, xnT, Kbuf, Dskip, w1T, b1, tbl, act, AoutT);
  k3_gemm2<<<256, 512, 0, stream>>>(act, w2T, x, AoutT, out);
}
```

```cpp
#define DIAG 0
#include <hip/hip_runtime.h>
#include <hip/hip_bf16.h>
#include <stdint.h>
#include <type_traits>

#define TS_BEGIN(k) if (threadIdx.x == 0) GD[k * 4096 + blockIdx.x * 2] = __builtin_amdgcn_s_memrealtime();
#define TS_END(k) asm volatile("s_waitcnt vmcnt(0) lgkmcnt(0)" ::: "memory"); __syncthreads(); if (threadIdx.x == 0) GD[k * 4096 + blockIdx.x * 2 + 1] = __builtin_amdgcn_s_memrealtime();
#define STAMP(i)
#define STAMPK(i)
#define STAMP_DUMP(a, b)

#define DMAV 0

namespace {

constexpr int L_ = 2048, D_ = 640, NS_ = 128, F_ = 1728;

typedef __attribute__((ext_vector_type(8))) short bf16x8;
typedef __attribute__((ext_vector_type(4))) float f32x4;
typedef __attribute__((ext_vector_type(16))) float f32x16;
typedef __attribute__((ext_vector_type(2))) float f32x2;
typedef __attribute__((ext_vector_type(2))) __bf16 bf16x2;
typedef unsigned short u16;

constexpr float LOG2E = 1.4426950408889634f;
constexpr float LN2 = 0.6931471805599453f;
constexpr float INV2PI = 0.15915494309189535f;

__device__ __forceinline__ float bf2f(u16 v) { return __uint_as_float(((uint32_t)v) << 16); }
__device__ __forceinline__ float bflo(uint32_t v) { return __uint_as_float(v << 16); }
__device__ __forceinline__ float bfhi(uint32_t v) { return __uint_as_float(v & 0xffff0000u); }
__device__ __forceinline__ u16 f2bf(float x) {
  __bf16 h = (__bf16)x;
  return __builtin_bit_cast(u16, h);
}
__device__ __forceinline__ float bfr(float x) { return bf2f(f2bf(x)); }
__device__ __forceinline__ uint32_t pack2(float a, float b) {
  f32x2 v = {a, b};
  bf16x2 r = __builtin_convertvector(v, bf16x2);
  return __builtin_bit_cast(uint32_t, r);
}

__device__ __forceinline__ float tanh_sp_bf(float h) {
  float e = bfr(__builtin_amdgcn_exp2f(-fabsf(h) * LOG2E));
  float s = e * (1.f - e * (0.5f - e * (1.f / 3.f)));
  float lg = __builtin_amdgcn_logf(1.f + e) * LN2;
  float l = bfr(e < 0.015625f ? s : lg);
  float sp = bfr(l + fmaxf(h, 0.f));
  float q = __builtin_amdgcn_exp2f(sp * (2.f * LOG2E));
  float t = 1.f - 2.f * __builtin_amdgcn_rcpf(q + 1.f);
  float ts = sp * (1.f - sp * sp * (1.f / 3.f));
  return bfr(sp < 0.03125f ? ts : t);
}
constexpr int TB_LO = 103 << 7, TB_HI = 134 << 7;
constexpr int TB_N = TB_HI - TB_LO + 1;
constexpr int TB_STRIDE = 4096;

template <int N, int I = 0, class Fn>
__device__ __forceinline__ void static_for(Fn&& f) {
  if constexpr (I < N) { f(std::integral_constant<int, I>{}); static_for<N, I + 1>(f); }
}

template <int CTRL>
__device__ __forceinline__ float dppf(float v) {
  return __builtin_bit_cast(float, __builtin_amdgcn_update_dpp(0, __builtin_bit_cast(int, v), CTRL, 0xF, 0xF, true));
}
__device__ __forceinline__ float rdl(float v, int l) {
  return __builtin_bit_cast(float, __builtin_amdgcn_readlane(__builtin_bit_cast(int, v), l));
}
__device__ __forceinline__ float wave_sum(float v) {
  v += dppf<0xB1>(v);
  v += dppf<0x4E>(v);
  v += dppf<0x141>(v);
  v += dppf<0x140>(v);
  return (rdl(v, 0) + rdl(v, 16)) + (rdl(v, 32) + rdl(v, 48));
}

constexpr int K1_KGEN = D_;
constexpr int K1_LN = L_ / 16;
constexpr int K1_T1 = (D_ / 64) * (F_ / 64);
constexpr int K1_BLOCKS = K1_KGEN + K1_LN + 2 * K1_T1 + 1;

__device__ __forceinline__ void k1_body(
    const u16* __restrict__ x, const u16* __restrict__ logAr, const u16* __restrict__ Aim,
    const u16* __restrict__ Cre, const u16* __restrict__ Cim, const u16* __restrict__ logdt,
    const u16* __restrict__ w1, const u16* __restrict__ w2,
    u16* __restrict__ xn, u16* __restrict__ xnT, u16* __restrict__ Kbuf,
    u16* __restrict__ w1T, u16* __restrict__ w2T, u16* __restrict__ tbl) {
  __shared__ __attribute__((aligned(16))) char smem[16 * 1296];
  const int tid = threadIdx.x, lane = tid & 63, wave = tid >> 6;
  int b = blockIdx.x;
  if (b < K1_KGEN) {
    const int d = b;
    float4* prm = (float4*)smem;
    float* part = (float*)(smem + 2048);
    float* kp = (float*)(smem + 2048 + 12288);
    if (tid < NS_) {
      float dt = bfr(expf(bf2f(logdt[d])));
      float ar = -bfr(expf(bf2f(logAr[d * NS_ + tid]))) * dt;
      float ai = bf2f(Aim[d * NS_ + tid]) * dt;
      float cr = bf2f(Cre[d * NS_ + tid]) * dt;
      float ci = bf2f(Cim[d * NS_ + tid]) * dt;
      prm[tid] = make_float4(ar * LOG2E, ai * INV2PI, 2.f * cr, 2.f * ci);
    }
    __syncthreads();
    const int r = lane & 15, kq = lane >> 4;
    float p0 = (float)r;
    float p1 = (r < 8) ? (float)(256 + 2 * r) : (float)(512 + 4 * ((r - 8) & 3) + 256 * ((r - 8) >> 2));
    float p2 = (r < 8) ? (float)(1024 + 8 * (r & 1) + 256 * (r >> 1)) : 2048.f;
    float pb = (float)(16 * r);
    f32x4 acc0 = {0.f, 0.f, 0.f, 0.f}, acc1 = acc0, acc2 = acc0;
#pragma unroll 2
    for (int g = 0; g < 8; ++g) {
      const float4 P = prm[wave * 32 + g * 4 + kq];
      float mb = __builtin_amdgcn_exp2f(P.x * pb);
      float rb = __builtin_amdgcn_fractf(P.y * pb);
      float fr_ = mb * __builtin_amdgcn_cosf(rb), fi_ = mb * __builtin_amdgcn_sinf(rb);
      float br = P.z * fr_ - P.w * fi_;
      float bi = -(P.z * fi_ + P.w * fr_);
      {
        float m = __builtin_amdgcn_exp2f(P.x * p0);
        float rv = __builtin_amdgcn_fractf(P.y * p0);
        float er = m * __builtin_amdgcn_cosf(rv), ei = m * __builtin_amdgcn_sinf(rv);
        acc0 = __builtin_amdgcn_mfma_f32_16x16x4f32(er, br, acc0, 0, 0, 0);
        acc0 = __builtin_amdgcn_mfma_f32_16x16x4f32(ei, bi, acc0, 0, 0, 0);
      }
      {
        float m = __builtin_amdgcn_exp2f(P.x * p1);
        float rv = __builtin_amdgcn_fractf(P.y * p1);
        float er = m * __builtin_amdgcn_cosf(rv), ei = m * __builtin_amdgcn_sinf(rv);
        acc1 = __builtin_amdgcn_mfma_f32_16x16x4f32(er, br, acc1, 0, 0, 0);
        acc1 = __builtin_amdgcn_mfma_f32_16x16x4f32(ei, bi, acc1, 0, 0, 0);
      }
      {
        float m = __builtin_amdgcn_exp2f(P.x * p2);
        float rv = __builtin_amdgcn_fractf(P.y * p2);
        float er = m * __builtin_amdgcn_cosf(rv), ei = m * __builtin_amdgcn_sinf(rv);
        acc2 = __builtin_amdgcn_mfma_f32_16x16x4f32(er, br, acc2, 0, 0, 0);
        acc2 = __builtin_amdgcn_mfma_f32_16x16x4f32(ei, bi, acc2, 0, 0, 0);
      }
    }
#pragma unroll
    for (int i = 0; i < 4; ++i) {
      part[((wave * 3 + 0) * 16 + kq * 4 + i) * 16 + r] = acc0[i];
      part[((wave * 3 + 1) * 16 + kq * 4 + i) * 16 + r] = acc1[i];
      part[((wave * 3 + 2) * 16 + kq * 4 + i) * 16 + r] = acc2[i];
    }
    __syncthreads();
    {
      int o;
      o = (tid & 15) * 16 + (tid >> 4);
      kp[tid] = (part[o] + part[o + 768]) + (part[o + 1536] + part[o + 2304]);
      if (tid < 128) { o = (16 + (tid & 7)) * 16 + (tid >> 3); }
      else { int m = tid - 128, j2 = m >> 2; o = (16 + 8 + (j2 >> 4) * 4 + (m & 3)) * 16 + (j2 & 15); }
      kp[256 + tid] = (part[o] + part[o + 768]) + (part[o + 1536] + part[o + 2304]);
      if (tid <= 128) {
        int j2 = tid >> 1;
        o = (tid == 128) ? (32 + 8) * 16 : (32 + (j2 >> 4) * 2 + (tid & 1)) * 16 + (j2 & 15);
        kp[512 + tid] = (part[o] + part[o + 768]) + (part[o + 1536] + part[o + 2304]);
      }
    }
    __syncthreads();
    float kv[8];
    {
      const int j0 = tid * 8;
      const int s = (j0 < 256) ? 0 : (j0 < 512) ? 1 : (j0 < 1024) ? 2 : 3;
      const int x0 = s ? j0 - (128 << s) : j0;
      const int bidx = s ? 128 + 128 * s : 0;
      const int hm1 = s ? (1 << (s - 1)) - 1 : 0;
#pragma unroll
      for (int jj = 0; jj < 8; ++jj) {
        const int xx = x0 + jj;
        const int q = s ? ((xx + hm1 + ((xx >> s) & 1)) >> s) : xx;
        kv[jj] = kp[bidx + q];
      }
    }
    uint32_t hi[4], lo[4];
#pragma unroll
    for (int i = 0; i < 4; ++i) {
      float a0 = kv[2 * i], a1 = kv[2 * i + 1];
      uint32_t h = pack2(a0, a1);
      hi[i] = h;
      lo[i] = pack2(a0 - bflo(h), a1 - bfhi(h));
    }
    u16* dst = Kbuf + (size_t)d * 4096 + tid * 8;
    *(uint4*)dst = make_uint4(hi[0], hi[1], hi[2], hi[3]);
    *(uint4*)(dst + 2048) = make_uint4(lo[0], lo[1], lo[2], lo[3]);
    return;
  }
  b -= K1_KGEN;
  if (b < K1_LN) {
    const int xc = b & 7, kk = b >> 3;
    const int l0 = ((xc * 4 + (kk >> 2)) * 4 + (kk & 3)) * 16;
    uint32_t* tile = (uint32_t*)smem;
    uint32_t v[4][5];
#pragma unroll
    for (int rr = 0; rr < 4; ++rr) {
      const uint32_t* xr = (const uint32_t*)(x + (size_t)(l0 + wave * 4 + rr) * D_);
#pragma unroll
      for (int i = 0; i < 5; ++i) v[rr][i] = xr[lane + 64 * i];
    }
    float mean[4], q[4];
#pragma unroll
    for (int rr = 0; rr < 4; ++rr) {
      float s = 0.f;
#pragma unroll
      for (int i = 0; i < 5; ++i) s += bflo(v[rr][i]) + bfhi(v[rr][i]);
      mean[rr] = wave_sum(s) * (1.f / D_);
    }
#pragma unroll
    for (int rr = 0; rr < 4; ++rr) {
      float s = 0.f;
#pragma unroll
      for (int i = 0; i < 5; ++i) {
        float a = bflo(v[rr][i]) - mean[rr], c = bfhi(v[rr][i]) - mean[rr];
        s += a * a + c * c;
      }
      q[rr] = wave_sum(s);
    }
#pragma unroll
    for (int rr = 0; rr < 4; ++rr) {
      const int row = wave * 4 + rr;
      const float mb = bfr(mean[rr]);
      const float vb = bfr(q[rr] * (1.f / D_));
      const float sd = bfr(sqrtf(bfr(vb + 1e-5f)));
      const int l = l0 + row, r = l & 127;
      uint32_t* xo = (uint32_t*)xn + ((size_t)((l >> 7) * 10 + (lane >> 5)) * 128 + r) * 32 +
                     ((((lane >> 2) & 7) ^ ((r >> 1) & 7)) * 4) + (lane & 3);
#pragma unroll
      for (int i = 0; i < 5; ++i) {
        float a = bfr(bflo(v[rr][i]) - mb) / sd, c = bfr(bfhi(v[rr][i]) - mb) / sd;
        uint32_t o = pack2(a, c);
        xo[i * 2 * 128 * 32] = o;
        tile[row * 324 + lane + 64 * i] = o;
      }
    }
    __syncthreads();
    const u16* t16 = (const u16*)smem;
    for (int d = tid; d < D_; d += 256) {
      uint32_t o[8];
#pragma unroll
      for (int i = 0; i < 8; ++i)
        o[i] = (uint32_t)t16[(2 * i) * 648 + d] | ((uint32_t)t16[(2 * i + 1) * 648 + d] << 16);
      uint4* dst = (uint4*)(xnT + (size_t)d * L_ + l0);
      dst[0] = make_uint4(o[0], o[1], o[2], o[3]);
      dst[1] = make_uint4(o[4], o[5], o[6], o[7]);
    }
    return;
  }
  b -= K1_LN;
  if (b == 2 * K1_T1) {
    for (int i = tid; i < 2 * TB_STRIDE; i += 256) {
      const int sg = i >= TB_STRIDE;
      int m = i - sg * TB_STRIDE;
      if (m > TB_N - 1) m = TB_N - 1;
      const u16 bits = (u16)((TB_LO + m) | (sg << 15));
      const float hv = bf2f(bits);
      tbl[i] = f2bf(hv * tanh_sp_bf(hv));
    }
    return;
  }
  {
    const u16* src; u16* dst; int R, C;
    const bool first = b < K1_T1;
    if (first) { src = w1; dst = w1T; R = D_; C = F_; }
    else { b -= K1_T1; src = w2; dst = w2T; R = F_; C = D_; }
    const int tc = C / 64;
    const int r0 = (b / tc) * 64, c0 = (b % tc) * 64;
    u16* t16 = (u16*)smem;
#pragma unroll
    for (int i = 0; i < 2; ++i) {
      int rr = (tid >> 3) + 32 * i, ch = tid & 7;
      uint4 v = *(const uint4*)(src + (size_t)(r0 + rr) * C + c0 + ch * 8);
      *(uint4*)(t16 + rr * 72 + ch * 8) = v;
    }
    __syncthreads();
#pragma unroll
    for (int i = 0; i < 2; ++i) {
      int cc = (tid >> 3) + 32 * i, ch = tid & 7;
      uint32_t o[4];
#pragma unroll
      for (int k = 0; k < 4; ++k)
        o[k] = (uint32_t)t16[(ch * 8 + 2 * k) * 72 + cc] | ((uint32_t)t16[(ch * 8 + 2 * k + 1) * 72 + cc] << 16);
      const int n = c0 + cc, kt = r0 >> 6;
      size_t off;
      if (first) {
        const int tn = n / 112, r2 = n - tn * 112;
        const int sw = (r2 < 96) ? (((r2 >> 1) & 1) | (((r2 >> 3) & 3) << 1)) : ((r2 >> 1) & 7);
        off = ((size_t)(tn * 10 + kt) * 112 + r2) * 64 + ((ch ^ sw) * 8);
      } else {
        const int nt = n / 80, r = n - nt * 80;
        off = ((size_t)(nt * 27 + kt) * 80 + r) * 64 + ((ch ^ (r & 7)) * 8);
      }
      *(uint4*)(dst + off) = make_uint4(o[0], o[1], o[2], o[3]);
    }
  }
}

__global__ __launch_bounds__(256) void k1_prep(
    const u16* __restrict__ x, const u16* __restrict__ logAr, const u16* __restrict__ Aim,
    const u16* __restrict__ Cre, const u16* __restrict__ Cim, const u16* __restrict__ logdt,
    const u16* __restrict__ w1, const u16* __restrict__ w2,
    u16* __restrict__ xn, u16* __restrict__ xnT, u16* __restrict__ Kbuf,
    u16* __restrict__ w1T, u16* __restrict__ w2T, u16* __restrict__ tbl) {
  unsigned long long* GD = (unsigned long long*)(xn + (32u << 20));
  TS_BEGIN(0)
  k1_body(x, logAr, Aim, Cre, Cim, logdt, w1, w2, xn, xnT, Kbuf, w1T, w2T, tbl);
  TS_END(0)
}

constexpr int G1_STAGE = 240 * 128;
constexpr int K2_SY = 1024, K2_SX = 34816, K2_SZ = 66560;
constexpr int K2_R0 = 100352;
constexpr int K2_IMG = 4 * 4160 + 2 * 4608;
constexpr int K2_R1 = K2_R0 + K2_IMG;
constexpr int K2_KM = 4 * 4160;
constexpr int K2_Q = K2_R1 + K2_IMG;
constexpr int K2_CNT = 16;
constexpr int K2_ST = K2_Q + 8192;
constexpr int K2_LDS = K2_ST + 256;

typedef __attribute__((address_space(3))) const char* lds_cptr;
typedef short v4i16_t __attribute__((ext_vector_type(4)));
typedef unsigned short u16x2 __attribute__((ext_vector_type(2)));
__device__ __forceinline__ v4i16_t trd(lds_cptr p) {
  return __builtin_amdgcn_ds_read_tr16_b64_v4i16((__attribute__((address_space(3))) v4i16_t*)p);
}
__device__ __forceinline__ bf16x8 rd16(lds_cptr p) { return *(const __attribute__((address_space(3))) bf16x8*)p; }
#define SBAR() __builtin_amdgcn_sched_barrier(0)

template <int OFF>
__device__ __forceinline__ void garrive(uint32_t addr, int lane) {
  asm volatile("" ::: "memory");
  __builtin_amdgcn_s_waitcnt(0xC07F);
  asm volatile("" ::: "memory");
  if (lane == 0) asm volatile("ds_add_u32 %0, %1 offset:%2" ::"v"(addr), "v"(1u), "n"(OFF) : "memory");
}
template <int OFF>
__device__ __forceinline__ void gwait(uint32_t addr, uint32_t target) {
  while (true) {
    uint32_t c;
    asm volatile("ds_read_b32 %0, %1 offset:%2\n\ts_waitcnt lgkmcnt(0)" : "=v"(c) : "v"(addr), "n"(OFF) : "memory");
    if ((uint32_t)__builtin_amdgcn_readfirstlane(c) >= target) break;
    __builtin_amdgcn_s_sleep(1);
  }
}

template <int MASK, class Hook>
__device__ __forceinline__ void conv_mfma(f32x4 (&acc)[8], lds_cptr ka, lds_cptr ub, Hook&& hook) {
  constexpr int T = (MASK & 0x80) ? 7 : 5;
  bf16x8 ah[2][8], al[2][8];
  v4i16_t bx[2][8], by[2][8];
#define CAT(x, y) __builtin_shufflevector(x, y, 0, 1, 2, 3, 4, 5, 6, 7)
  asm volatile("; conv %0" ::"n"(MASK) : "memory");
  bx[0][0] = trd(ub + 8); by[0][0] = trd(ub);
#pragma unroll
  for (int b = 0; b <= T; ++b) { ah[0][b] = rd16(ka + 256 + 512 * b); al[0][b] = rd16(ka + 256 + 512 * b + 4608); }
#pragma unroll
  for (int a = 1; a <= T; ++a) { bx[0][a] = trd(ub + 512 * a + 8); by[0][a] = trd(ub + 512 * a); }
  SBAR();
  static_for<2>([&](auto RR) {
    constexpr int rr = decltype(RR)::value;
    static_for<T + 1>([&](auto AA) {
      constexpr int a = decltype(AA)::value;
      const bf16x8 bf = CAT(bx[rr][a], by[rr][a]);
      int cnt = 0, pend = -1;
#pragma unroll
      for (int bb = 0; bb <= T - a; ++bb) {
        const int b = (rr == 1 && a == 0) ? T - bb : bb;
        if (!((MASK >> (a + b)) & 1)) continue;
        acc[a + b] = __builtin_amdgcn_mfma_f32_16x16x32_bf16(bf, ah[rr][b], acc[a + b], 0, 0, 0);
        if (cnt == 0 && rr == 0) { bx[1][a] = trd(ub + 256 + 512 * a + 8); by[1][a] = trd(ub + 256 + 512 * a); }
        if (rr == 1 && cnt == (a == 0 ? 2 : 0)) hook(AA, std::integral_constant<int, MASK>{});
        SBAR();
        if (pend >= 0) {
          acc[a + pend] = __builtin_amdgcn_mfma_f32_16x16x32_bf16(bf, al[rr][pend], acc[a + pend], 0, 0, 0);
          SBAR();
        }
        pend = b; ++cnt;
      }
      acc[a + pend] = __builtin_amdgcn_mfma_f32_16x16x32_bf16(bf, al[rr][pend], acc[a + pend], 0, 0, 0);
      if (rr == 0) { ah[1][T - a] = rd16(ka + 512 * (T - a)); al[1][T - a] = rd16(ka + 512 * (T - a) + 4608); }
      SBAR();
    });
  });
#undef CAT
}

typedef uint32_t u32x4 __attribute__((ext_vector_type(4)));
typedef uint32_t u32x2 __attribute__((ext_vector_type(2)));
struct ConvRow { u32x4 uv, k0, k1; u32x2 up; };
__device__ __forceinline__ void conv_fetch(ConvRow& r, const u16* __restrict__ xnT, const u16* __restrict__ Kbuf, int d, uint32_t o16) {
  const char* us = (const char*)(xnT + (size_t)d * L_);
  const char* ks = (const char*)(Kbuf + (size_t)d * 4096);
  const uint32_t o8 = o16 ? o16 - 8u : 0u, o2 = o16 + 4096u;
  asm volatile("global_load_dwordx4 %0, %1, %2" : "=v"(r.uv) : "v"(o16), "s"(us) : "memory");
  asm volatile("global_load_dwordx2 %0, %1, %2" : "=v"(r.up) : "v"(o8), "s"(us) : "memory");
  asm volatile("global_load_dwordx4 %0, %1, %2" : "=v"(r.k0) : "v"(o16), "s"(ks) : "memory");
  asm volatile("global_load_dwordx4 %0, %1, %2" : "=v"(r.k1) : "v"(o2), "s"(ks) : "memory");
}
__device__ __forceinline__ void conv_ready(ConvRow& r) {
  asm volatile("s_waitcnt vmcnt(0)" : "+v"(r.uv), "+v"(r.up), "+v"(r.k0), "+v"(r.k1)::"memory");
}
__device__ __forceinline__ void conv_put(const ConvRow& r, char* img, int ct) {
  const uint32_t pm2 = ct ? r.up.x : 0u, pm1 = ct ? r.up.y : 0u;
  const uint32_t am = __builtin_amdgcn_alignbit(pm1, pm2, 16), a0 = __builtin_amdgcn_alignbit(r.uv.x, pm1, 16);
  const uint32_t a1 = __builtin_amdgcn_alignbit(r.uv.y, r.uv.x, 16), a2 = __builtin_amdgcn_alignbit(r.uv.z, r.uv.y, 16);
  const uint32_t a3 = __builtin_amdgcn_alignbit(r.uv.w, r.uv.z, 16);
  char* ud = img + 64 + ct * 16;
  *(u32x4*)(ud) = r.uv;
  *(uint4*)(ud + 4160) = make_uint4(a0, a1, a2, a3);
  *(uint4*)(ud + 2 * 4160) = make_uint4(pm1, r.uv.x, r.uv.y, r.uv.z);
  *(uint4*)(ud + 3 * 4160) = make_uint4(am, a0, a1, a2);
  *(u32x4*)(img + K2_KM + 512 + ct * 16) = r.k0;
  *(u32x4*)(img + K2_KM + 4608 + 512 + ct * 16) = r.k1;
  if (ct < 64) *(uint4*)(img + K2_KM + (ct >> 5) * 4608 + (ct & 31) * 16) = make_uint4(0, 0, 0, 0);
  else if (ct < 80) *(uint4*)(img + ((ct >> 2) & 3) * 4160 + (ct & 3) * 16) = make_uint4(0, 0, 0, 0);
}

__device__ __forceinline__ void k2_body(
    const u16* __restrict__ xn, const u16* __restrict__ xnT, const u16* __restrict__ Kbuf,
    const u16* __restrict__ Dskip, const u16* __restrict__ w1T, const u16* __restrict__ b1,
    const u16* __restrict__ tbl, u16* __restrict__ act, u16* __restrict__ AoutT) {
  __shared__ __attribute__((aligned(16))) char smem[K2_LDS];
  const int tid = threadIdx.x, lane = tid & 63;
  const int wave = __builtin_amdgcn_readfirstlane(tid >> 6);
  const int bid = blockIdx.x;
  const uint32_t sbase = (uint32_t)(size_t)(lds_cptr)smem;
  constexpr int cF = K2_CNT, cE = cF + 48, cA = cE + 48, cB = cA + 12, cP = cB + 12, cR = cP + 12;
  const int ch2 = (bid < 128) ? 512 + bid : 384 + bid;
  const int xc = bid & 7, jj = bid >> 3;
  const int tm = (xc & 3) * 4 + (jj & 3), tn = (xc >> 2) * 8 + (jj >> 2);
  const int m0 = tm * 128, n0 = tn * 112;
  if (wave >= 8) {
    const int lw = wave - 8;
    const uint32_t vo = (uint32_t)lane * 16u;
    const char* ga = (const char*)(xn + (size_t)tm * 10 * 8192);
    const char* gb = (const char*)(w1T + (size_t)tn * 10 * 7168);
    const int nvalid = (tn == 15) ? 6 : 14;
    auto stage = [&](int kt, int off) {
      const char* pa = ga + kt * 16384;
      const char* pb = gb + kt * 14336;
#pragma unroll
      for (int i = 0; i < 16; ++i)
        __builtin_amdgcn_global_load_lds((const uint32_t*)(pa + i * 1024 + vo), (uint32_t*)(smem + off + i * 1024), 16, 0, 0);
#pragma unroll
      for (int i = 0; i < 14; ++i)
        __builtin_amdgcn_global_load_lds((const uint32_t*)(pb + (i < nvalid ? i * 1024 : 0) + vo),
                                         (uint32_t*)(smem + off + (16 + i) * 1024), 16, 0, 0);
    };
    __builtin_amdgcn_s_setprio(3);
    const int lt = tid - 512;
    const uint32_t o16 = (uint32_t)lt * 16u;
    ConvRow r2;
    uint4 u0, u1;
#define D1 10
#define D2 24
#define D3 18
    if (lw == 0) stage(0, K2_SX);
    __builtin_amdgcn_s_barrier();
    if (lw == 1) __builtin_amdgcn_s_sleep(D1);
    else if (lw == 2) __builtin_amdgcn_s_sleep(D2);
    else if (lw == 3) __builtin_amdgcn_s_sleep(D3);
    if (lw == 1 || lw == 2) stage(lw, lw == 1 ? K2_SY : K2_SZ);
    conv_fetch(r2, xnT, Kbuf, bid + 256, o16);
    if (lw < 3) {
      asm volatile("s_waitcnt vmcnt(4)" ::: "memory");
      garrive<cF>(sbase + lw * 4, lane);
    }
    conv_ready(r2);
    conv_put(r2, smem + K2_R1, lt);
    garrive<cA + 4>(sbase, lane);
#pragma unroll 1
    for (int kt = (lw == 3) ? 3 : lw + 4; kt < 12; kt += 4) {
      {
        gwait<cE>(sbase + (kt - 3) * 4, 4);
        if (kt < 10) {
          const int r3 = kt % 3;
          stage(kt, r3 == 0 ? K2_SX : r3 == 1 ? K2_SY : K2_SZ);
        } else {
          const int h = kt - 10;
#pragma unroll
          for (int i = 0; i < 8; ++i)
            __builtin_amdgcn_global_load_lds((const uint32_t*)((const char*)(tbl + h * TB_STRIDE) + i * 1024 + vo),
                                             (uint32_t*)(smem + h * 65536 + TB_LO * 2 + i * 1024), 16, 0, 0);
        }
      }
      asm volatile("s_waitcnt vmcnt(0)" ::: "memory");
      garrive<cF>(sbase + (kt < 10 ? kt : 10) * 4, lane);
    }
    conv_fetch(r2, xnT, Kbuf, ch2, o16);
    u0 = *(const uint4*)((const char*)(xnT + (size_t)bid * L_) + o16);
    u1 = *(const uint4*)((const char*)(xnT + (size_t)(bid + 256) * L_) + o16);
    const int rsw = ((lt >> 2) & 3) ^ ((lt >> 4) & 1);
    const int rc0 = (lt >> 1) * 64 + (((lt & 1) * 2) ^ rsw) * 16, rc1 = (lt >> 1) * 64 + (((lt & 1) * 2 + 1) ^ rsw) * 16;
    f32x4 s0, s1;
    auto red_load = [&](const char* Pr) {
      const char* Q = smem + K2_Q;
      s0 = (*(const f32x4*)(Pr + rc0) + *(const f32x4*)(Pr + 8192 + rc0)) + (*(const f32x4*)(Pr + 16384 + rc0) + *(const f32x4*)(Q + rc0));
      s1 = (*(const f32x4*)(Pr + rc1) + *(const f32x4*)(Pr + 8192 + rc1)) + (*(const f32x4*)(Pr + 16384 + rc1) + *(const f32x4*)(Q + rc1));
    };
    auto red_store = [&](int d, const uint4& ucur) {
      const uint32_t dw = ((const uint32_t*)Dskip)[d >> 1];
      const float y[8] = {s0[0], s0[1], s0[2], s0[3], s1[0], s1[1], s1[2], s1[3]};
      const float dsk = (d & 1) ? bfhi(dw) : bflo(dw);
      const uint32_t uu[4] = {ucur.x, ucur.y, ucur.z, ucur.w};
      uint32_t o4[4];
#pragma unroll
      for (int i = 0; i < 4; ++i) {
        float r0 = bfr(y[2 * i]) + bfr(bflo(uu[i]) * dsk);
        float r1 = bfr(y[2 * i + 1]) + bfr(bfhi(uu[i]) * dsk);
        o4[i] = pack2(r0, r1);
      }
      *(uint4*)((char*)(AoutT + (size_t)d * L_) + o16) = make_uint4(o4[0], o4[1], o4[2], o4[3]);
    };
    gwait<cP>(sbase, 4);
    red_load(smem + K2_R0);
    garrive<cR>(sbase, lane);
    gwait<cR>(sbase, 4);
    conv_ready(r2);
    conv_put(r2, smem + K2_R0, lt);
    garrive<cA + 8>(sbase, lane);
    red_store(bid, u0);
    gwait<cP + 4>(sbase, 4);
    red_load(smem + K2_R1);
    garrive<cR + 4>(sbase, lane);
    red_store(bid + 256, u1);
    gwait<cP + 8>(sbase, 4);
    if ((((bid < 128) ? 0xC3 : 0x3C) >> (lt >> 5)) & 1) {
      red_load(smem + K2_R0);
      red_store(ch2, make_uint4(r2.uv.x, r2.uv.y, r2.uv.z, r2.uv.w));
    }
    return;
  }
  if (wave < 4) {
    const int fr = lane & 15, fq = lane >> 4;
    STAMP(0)
    if (tid < 36) *(uint32_t*)(smem + K2_CNT + tid * 4) = 0;
    if (tid == 40) *(uint32_t*)(smem) = 0;
    if (tid == 41) *(uint32_t*)(smem + 65536) = 0x8000u;
    asm volatile("s_waitcnt lgkmcnt(0)" ::: "memory");
    __builtin_amdgcn_s_barrier();
#define PG 2
#define PC 1
#define PE 0
    __builtin_amdgcn_s_setprio(PG);
    uint4 bb[3]; uint2 bb3;
#pragma unroll
    for (int p = 0; p < 3; ++p) {
      int n = n0 + 32 * p + 8 * fq;
      if (n > F_ - 8) n = F_ - 8;
      bb[p] = *(const uint4*)(b1 + n);
    }
    {
      int n = n0 + 96 + 4 * fq;
      if (n > F_ - 4) n = F_ - 4;
      bb3 = *(const uint2*)(b1 + n);
    }
    STAMP(1)
    int aoff[7], boff[2];
#pragma unroll
    for (int i = 0; i < 7; ++i) {
      const int r2 = (i < 6) ? (32 * (i >> 1) + 8 * (fr >> 2) + 4 * (i & 1) + (fr & 3)) : (96 + fr);
      const int sw = (i < 6) ? (((r2 >> 1) & 1) | (((r2 >> 3) & 3) << 1)) : ((r2 >> 1) & 7);
      aoff[i] = (128 + r2) * 128 + ((fq ^ sw) * 16);
    }
#pragma unroll
    for (int j = 0; j < 2; ++j) {
      const int row = wave * 32 + j * 16 + fr;
      boff[j] = row * 128 + ((fq ^ ((row >> 1) & 7)) * 16);
    }
    f32x4 acc[7][2];
#pragma unroll
    for (int i = 0; i < 7; ++i)
#pragma unroll
      for (int j = 0; j < 2; ++j) acc[i][j] = (f32x4){0.f, 0.f, 0.f, 0.f};
    int off = K2_SX;
    bf16x8 af[2][7], bfm[2][2];
    gwait<cF>(sbase, 1);
    {
      const char* base = smem + off;
#pragma unroll
      for (int j = 0; j < 2; ++j) bfm[0][j] = *(const bf16x8*)(base + boff[j]);
#pragma unroll
      for (int i = 0; i < 7; ++i) af[0][i] = *(const bf16x8*)(base + aoff[i]);
    }
#pragma unroll 1
    for (int kt = 0; kt < 10; ++kt) {
      STAMPK(2 + kt)
      const char* base = smem + off;
      off = (off == K2_SX) ? K2_SY : (off == K2_SY) ? K2_SZ : K2_SX;
      uint32_t pc = 0;
      SBAR();
#pragma unroll
      for (int ks = 0; ks < 2; ++ks) {
#pragma unroll
        for (int i = 0; i < 7; ++i) {
          acc[i][0] = __builtin_amdgcn_mfma_f32_16x16x32_bf16(af[ks][i], bfm[ks][0], acc[i][0], 0, 0, 0);
          if (ks == 0) {
            if (i < 2) bfm[1][i] = *(const bf16x8*)(base + (boff[i] ^ 64));
            af[1][i] = *(const bf16x8*)(base + (aoff[i] ^ 64));
          }
          if (ks == 1 && i == 0)
            asm volatile("ds_read_b32 %0, %1 offset:%2" : "+v"(pc) : "v"(sbase + kt * 4), "n"(cF + 4) : "memory");
          SBAR();
          acc[i][1] = __builtin_amdgcn_mfma_f32_16x16x32_bf16(af[ks][i], bfm[ks][1], acc[i][1], 0, 0, 0);
          if (ks == 1 && i == 2) {
            garrive<cE>(sbase + kt * 4, lane);
            asm volatile("" : "+v"(pc)::"memory");
            if (kt < 9) {
              if (__builtin_amdgcn_readfirstlane(pc) == 0) gwait<cF + 4>(sbase + kt * 4, 1);
              const char* nb = smem + off;
#pragma unroll
              for (int j = 0; j < 2; ++j) bfm[0][j] = *(const bf16x8*)(nb + boff[j]);
#pragma unroll
              for (int i2 = 0; i2 < 7; ++i2) af[0][i2] = *(const bf16x8*)(nb + aoff[i2]);
            }
          }
          SBAR();
        }
      }
    }
    __builtin_amdgcn_s_setprio(PE);
    gwait<cF>(sbase + 40, 2);
    STAMP(12)
    u16* dummy = (u16*)tbl + 2 * TB_STRIDE;
    u16x2 vmax = {0, 0}, vmin = {0xffff, 0xffff};
    auto epi = [&](auto slow_tag) {
      constexpr bool SLOW = decltype(slow_tag)::value;
      auto hval = [&](float a0, float a1, uint32_t bias) -> uint32_t {
        const uint32_t r = pack2(a0, a1);
        return pack2(bflo(r) + bflo(bias), bfhi(r) + bfhi(bias));
      };
      auto act2 = [&](uint32_t h) -> uint32_t {
        if (SLOW) {
          return pack2(bflo(h) * tanh_sp_bf(bflo(h)), bfhi(h) * tanh_sp_bf(bfhi(h)));
        } else {
          const u16x2 m = __builtin_bit_cast(u16x2, h & 0x7fff7fffu);
          vmax = __builtin_elementwise_max(vmax, m);
          vmin = __builtin_elementwise_min(vmin, (u16x2)(m - (u16x2){1, 1}));
          uint32_t alo, ahi;
          asm("v_lshlrev_b32_sdwa %0, %1, %2 dst_sel:DWORD dst_unused:UNUSED_PAD src0_sel:DWORD src1_sel:WORD_0" : "=v"(alo) : "v"(1u), "v"(h));
          asm("v_lshlrev_b32_sdwa %0, %1, %2 dst_sel:DWORD dst_unused:UNUSED_PAD src0_sel:DWORD src1_sel:WORD_1" : "=v"(ahi) : "v"(1u), "v"(h));
          const uint32_t lo = *(const u16*)(smem + alo);
          const uint32_t hi = *(const u16*)(smem + ahi);
          return lo | (hi << 16);
        }
      };
#pragma unroll
      for (int j = 0; j < 2; ++j) {
        const int m = m0 + wave * 32 + j * 16 + fr;
        u16* arow = act + ((size_t)(m >> 6) * 27 * 64 + (m & 63)) * 64;
        uint32_t h[14];
#pragma unroll
        for (int p = 0; p < 3; ++p) {
          h[4 * p + 0] = hval(acc[2 * p][j][0], acc[2 * p][j][1], bb[p].x);
          h[4 * p + 1] = hval(acc[2 * p][j][2], acc[2 * p][j][3], bb[p].y);
          h[4 * p + 2] = hval(acc[2 * p + 1][j][0], acc[2 * p + 1][j][1], bb[p].z);
          h[4 * p + 3] = hval(acc[2 * p + 1][j][2], acc[2 * p + 1][j][3], bb[p].w);
        }
        h[12] = hval(acc[6][j][0], acc[6][j][1], bb3.x);
        h[13] = hval(acc[6][j][2], acc[6][j][3], bb3.y);
#pragma unroll
        for (int i = 0; i < 14; ++i) h[i] = act2(h[i]);
#pragma unroll
        for (int p = 0; p < 3; ++p) {
          const int n = n0 + 32 * p + 8 * fq;
          u16* dst = (n < F_) ? arow + (n >> 6) * 4096 + ((((n >> 3) & 7) ^ (m & 7)) * 8) : dummy;
          *(uint4*)dst = make_uint4(h[4 * p], h[4 * p + 1], h[4 * p + 2], h[4 * p + 3]);
        }
        {
          const int n = n0 + 96 + 4 * fq;
          u16* dst = (n < F_) ? arow + (n >> 6) * 4096 + ((((n >> 3) & 7) ^ (m & 7)) * 8) + (n & 7) : dummy;
          *(uint2*)dst = make_uint2(h[12], h[13]);
        }
      }
    };
    epi(std::false_type{});
    STAMP(14)
    {
      const uint32_t mx = max((uint32_t)vmax.x, (uint32_t)vmax.y), mn = min((uint32_t)vmin.x, (uint32_t)vmin.y);
      if (__builtin_amdgcn_ballot_w64(mx > (uint32_t)TB_HI || mn < (uint32_t)(TB_LO - 1)) != 0) epi(std::true_type{});
    }
    STAMP(13)
    STAMP_DUMP(0, 15)
    return;
  }
  const int cw = wave - 4, ct = tid - 256;
  {
    ConvRow r0;
    conv_fetch(r0, xnT, Kbuf, bid, (uint32_t)ct * 16u);
    __builtin_amdgcn_s_barrier();
    __builtin_amdgcn_s_setprio(PC);
    conv_ready(r0);
    conv_put(r0, smem + K2_R0, ct);
    garrive<cA>(sbase, lane);
  }
  const int fr = lane & 15, kg = lane >> 4;
  lds_cptr ub0 = (lds_cptr)(smem + K2_R0 + ((lane >> 2) & 3) * 4160 + 32 * (kg >> 1) - 16 * (kg & 1) + (lane & 3) * 8 + 56 + 64 * cw);
  lds_cptr ka0 = (lds_cptr)(smem + K2_R0 + K2_KM + 512 + 32 * fr - 32 * (kg >> 1) + 16 * (kg & 1) - 64 * cw - 256);
  const int pwo = fr * 64 + ((kg ^ ((fr >> 1) & 3) ^ (fr >> 3)) * 16);
  gwait<cA>(sbase, 4);
#pragma unroll 1
  for (int task = 0; task < 3; ++task) {
    STAMP(17 + task * 5)
    const int ro = (task == 1) ? K2_IMG : 0;
    lds_cptr ub = ub0 + ro, ka = ka0 + ro;
    f32x4 acc[8];
#pragma unroll
    for (int i = 0; i < 8; ++i) acc[i] = (f32x4){0.f, 0.f, 0.f, 0.f};
    const int mask = (task < 2) ? 0xFF : (bid < 128 ? 0xC3 : 0x3C);
    char* Pw = smem + ((cw == 3) ? K2_Q : K2_R0 + ro + cw * 8192) + pwo;
    const uint32_t qaddr = sbase + ((cw == 3 && task > 0) ? cR - 4 : cB) + task * 4;
    uint32_t pb = 0, pq = 0;
    bool early = false;
    auto hook = [&](auto AA, auto MK) {
      constexpr int a = decltype(AA)::value, mk = decltype(MK)::value;
      if constexpr (a == 0) garrive<cB>(sbase + task * 4, lane);
      if constexpr (a == 1) {
        asm volatile("ds_read_b32 %0, %1 offset:%2" : "+v"(pb) : "v"(sbase + task * 4), "n"(cB) : "memory");
        asm volatile("ds_read_b32 %0, %1" : "+v"(pq) : "v"(qaddr) : "memory");
      }
      if constexpr (a == 2) {
        __builtin_amdgcn_s_waitcnt(0xC07F);
        asm volatile("" : "+v"(pb), "+v"(pq)::"memory");
        early = __builtin_amdgcn_readfirstlane(min(pb, pq)) >= 4;
        if constexpr (mk & 1) { if (early) *(f32x4*)(Pw) = acc[0]; }
      }
      if constexpr (a > 2) {
        if (!early) {
          uint32_t c0, c1;
          asm volatile("ds_read_b32 %0, %2 offset:%4\n\tds_read_b32 %1, %3\n\ts_waitcnt lgkmcnt(0)"
                       : "=&v"(c0), "=&v"(c1) : "v"(sbase + task * 4), "v"(qaddr), "n"(cB) : "memory");
          early = __builtin_amdgcn_readfirstlane(min(c0, c1)) >= 4;
          if (early) {
#pragma unroll
            for (int i = 0; i < a - 1; ++i)
              if ((mk >> i) & 1) *(f32x4*)(Pw + i * 1024) = acc[i];
          }
        }
      }
      if constexpr (a >= 2) {
        if constexpr ((mk >> (a - 1)) & 1) { if (early) *(f32x4*)(Pw + (a - 1) * 1024) = acc[a - 1]; }
      }
    };
    if (mask == 0xFF) conv_mfma<0xFF>(acc, ka, ub, hook);
    else if (mask == 0xC3) conv_mfma<0xC3>(acc, ka, ub, hook);
    else conv_mfma<0x3C>(acc, ka, ub, hook);
    STAMP(18 + task * 5)
    if (early) {
      if (mask & 0x80) *(f32x4*)(Pw + 7 * 1024) = acc[7];
      else *(f32x4*)(Pw + 5 * 1024) = acc[5];
    } else {
      gwait<cB>(sbase + task * 4, 4);
      gwait<0>(qaddr, 4);
#pragma unroll
      for (int i = 0; i < 8; ++i)
        if ((mask >> i) & 1) *(f32x4*)(Pw + i * 1024) = acc[i];
    }
    uint32_t pa = 0;
    asm volatile("ds_read_b32 %0, %1 offset:%2" : "+v"(pa) : "v"(sbase + task * 4), "n"(cA + 4) : "memory");
    garrive<cP>(sbase + task * 4, lane);
    asm volatile("" : "+v"(pa)::"memory");
    STAMP(19 + task * 5)
    if (task < 2) {
      if ((uint32_t)__builtin_amdgcn_readfirstlane(pa) < 4u) gwait<cA + 4>(sbase + task * 4, 4);
    }
  }
  STAMP(31)
  STAMP_DUMP(16, 32)
}

__global__ __launch_bounds__(768, 1) void k2_conv_gemm1(
    const u16* __restrict__ xn, const u16* __restrict__ xnT, const u16* __restrict__ Kbuf,
    const u16* __restrict__ Dskip, const u16* __restrict__ w1T, const u16* __restrict__ b1,
    const u16* __restrict__ tbl, u16* __restrict__ act, u16* __restrict__ AoutT) {
  unsigned long long* GD = (unsigned long long*)(const_cast<u16*>(xn) + (32u << 20));
  TS_BEGIN(1)
  k2_body(xn, xnT, Kbuf, Dskip, w1T, b1, tbl, act, AoutT);
  TS_END(1)
}

constexpr int K3_STAGE = 144 * 128;
#define K3_NB 4
#define K3_PF 0
constexpr int K3_NBUF = K3_NB;
constexpr int K3_AO_OFF = K3_STAGE * K3_NBUF;
constexpr int K3_LDS = K3_AO_OFF + 80 * 128;

__device__ __forceinline__ void k3_body(
    const u16* __restrict__ act, const u16* __restrict__ w2T, const u16* __restrict__ x,
    const u16* __restrict__ AoutT, u16* __restrict__ out) {
  __shared__ __attribute__((aligned(16))) char smem[K3_LDS];
  const int tid = threadIdx.x, lane = tid & 63;
  const int wave = __builtin_amdgcn_readfirstlane(tid >> 6);
  const int bid = blockIdx.x;
  const int xc = bid & 7, jj = bid >> 3;
  const int rb = (xc & 3) * 8 + (jj & 7), nt = (xc >> 2) * 4 + (jj >> 3);
  const int m0 = rb * 64, n0 = nt * 80;
  constexpr int NT = F_ / 64;
  f32x4 acc[5][2];
#pragma unroll
  for (int i = 0; i < 5; ++i)
#pragma unroll
    for (int j = 0; j < 2; ++j) acc[i][j] = (f32x4){0.f, 0.f, 0.f, 0.f};
  const int fr = lane & 15, fq = lane >> 4;
  const int wm = wave & 1, ksp = (wave >> 1) & 1;
  const int em0 = tid / 10, ec0 = (tid % 10) * 8;
  const int em1 = (512 + tid) / 10, ec1 = ((512 + tid) % 10) * 8;
  uint4 xv0, xv1 = make_uint4(0, 0, 0, 0);
  if (wave >= 4) {
    const int lw = wave - 4;
    const u16* gsrc[5];
#pragma unroll
    for (int i = 0; i < 5; ++i) {
      int ch = lw + 4 * i;
      if (ch > 17) ch = 17;
      gsrc[i] = (ch < 8 ? act + (size_t)rb * 27 * 4096 + ch * 512 : w2T + (size_t)nt * 27 * 5120 + (ch - 8) * 512) + lane * 8;
    }
    auto stage = [&](int kt, int buf) {
#pragma unroll
      for (int i = 0; i < 4; ++i)
        __builtin_amdgcn_global_load_lds((const uint32_t*)(gsrc[i] + kt * (i < 2 ? 4096 : 5120)),
                                         (uint32_t*)(smem + buf * K3_STAGE + (lw + 4 * i) * 1024), 16, 0, 0);
      if (lw < 2)
        __builtin_amdgcn_global_load_lds((const uint32_t*)(gsrc[4] + kt * 5120),
                                         (uint32_t*)(smem + buf * K3_STAGE + (lw + 16) * 1024), 16, 0, 0);
    };
#pragma unroll
    for (int i = 0; i < K3_NBUF - 1; ++i) stage(i, i);
    int buf = K3_NBUF - 1;
    for (int kt = 0; kt < NT - (K3_NBUF - 1); ++kt) {
      if (lw < 2) asm volatile("s_waitcnt vmcnt(%0)" ::"n"(5 * (K3_NBUF - 2)) : "memory");
      else asm volatile("s_waitcnt vmcnt(%0)" ::"n"(4 * (K3_NBUF - 2)) : "memory");
      __builtin_amdgcn_s_barrier();
      stage(kt + K3_NBUF - 1, buf);
      buf = (buf == K3_NBUF - 1) ? 0 : buf + 1;
    }
    static_for<K3_NBUF - 1>([&](auto I) {
      constexpr int rem = K3_NBUF - 2 - decltype(I)::value;
      if (lw < 2) asm volatile("s_waitcnt vmcnt(%0)" ::"n"(5 * rem) : "memory");
      else asm volatile("s_waitcnt vmcnt(%0)" ::"n"(4 * rem) : "memory");
      __builtin_amdgcn_s_barrier();
    });
    xv0 = *(const uint4*)(x + (size_t)(m0 + em0) * D_ + n0 + ec0);
  } else {
#pragma unroll
    for (int i = 0; i < 3; ++i) {
      const int ch = wave + 4 * i;
      if (ch < 10) {
        const int row = ch * 8 + (lane >> 3);
        __builtin_amdgcn_global_load_lds((const uint32_t*)(AoutT + (size_t)(n0 + row) * L_ + m0 + (lane & 7) * 8),
                                         (uint32_t*)(smem + K3_AO_OFF + ch * 1024), 16, 0, 0);
      }
    }
    xv0 = *(const uint4*)(x + (size_t)(m0 + em0) * D_ + n0 + ec0);
    if (tid < 128) xv1 = *(const uint4*)(x + (size_t)(m0 + em1) * D_ + n0 + ec1);
    int aoff[5], boff[2];
#pragma unroll
    for (int i = 0; i < 5; ++i) {
      const int row = 64 + i * 16 + fr;
      aoff[i] = row * 128 + (((ksp * 4 + fq) ^ (row & 7)) * 16);
    }
#pragma unroll
    for (int j = 0; j < 2; ++j) {
      const int row = wm * 32 + j * 16 + fr;
      boff[j] = row * 128 + (((ksp * 4 + fq) ^ (row & 7)) * 16);
    }
    int buf = 0;
    for (int kt = 0; kt < NT; ++kt) {
      asm volatile("s_waitcnt lgkmcnt(0)" ::: "memory");
      __builtin_amdgcn_s_barrier();
      const char* base = smem + buf * K3_STAGE;
      buf = (buf == K3_NBUF - 1) ? 0 : buf + 1;
      bf16x8 af[5], bfm[2];
#pragma unroll
      for (int j = 0; j < 2; ++j) bfm[j] = *(const bf16x8*)(base + boff[j]);
#pragma unroll
      for (int i = 0; i < 5; ++i) af[i] = *(const bf16x8*)(base + aoff[i]);
#pragma unroll
      for (int i = 0; i < 5; ++i)
#pragma unroll
        for (int j = 0; j < 2; ++j)
          acc[i][j] = __builtin_amdgcn_mfma_f32_16x16x32_bf16(af[i], bfm[j], acc[i][j], 0, 0, 0);
    }
  }
  __syncthreads();
  float* red = (float*)smem;
  if (wave < 4) {
#pragma unroll
    for (int i = 0; i < 5; ++i)
#pragma unroll
      for (int j = 0; j < 2; ++j) {
        const int m = wm * 32 + j * 16 + fr, n = i * 16 + fq * 4;
        *(f32x4*)(red + (ksp * 64 + m) * 84 + n) = acc[i][j];
      }
  }
  __syncthreads();
  const u16* ao = (const u16*)(smem + K3_AO_OFF);
#pragma unroll
  for (int it = 0; it < 2; ++it) {
    if (it == 1 && tid >= 128) break;
    const int m = it ? em1 : em0, c8 = it ? ec1 : ec0;
    const uint4 xv = it ? xv1 : xv0;
    const f32x4* r0 = (const f32x4*)(red + m * 84 + c8);
    const f32x4* r1 = (const f32x4*)(red + (64 + m) * 84 + c8);
    const f32x4 s0 = r0[0] + r1[0], s1 = r0[1] + r1[1];
    const float s[8] = {s0[0], s0[1], s0[2], s0[3], s1[0], s1[1], s1[2], s1[3]};
    const uint32_t xx[4] = {xv.x, xv.y, xv.z, xv.w};
    uint32_t o[4];
#pragma unroll
    for (int i = 0; i < 4; ++i) {
      float a0 = bf2f(ao[(c8 + 2 * i) * 64 + m]), a1 = bf2f(ao[(c8 + 2 * i + 1) * 64 + m]);
      float v0 = bfr(bflo(xx[i]) + a0) + bfr(s[2 * i]);
      float v1 = bfr(bfhi(xx[i]) + a1) + bfr(s[2 * i + 1]);
      o[i] = pack2(v0, v1);
    }
    *(uint4*)(out + (size_t)(m0 + m) * D_ + n0 + c8) = make_uint4(o[0], o[1], o[2], o[3]);
  }
}

__global__ __launch_bounds__(512, 1) void k3_gemm2(
    const u16* __restrict__ act, const u16* __restrict__ w2T, const u16* __restrict__ x,
    const u16* __restrict__ AoutT, u16* __restrict__ out) {
  unsigned long long* GD = (unsigned long long*)(const_cast<u16*>(act) - ((size_t)L_ * D_ * 2 + (size_t)D_ * 4096 + (size_t)D_ * 1792 + (size_t)D_ * F_) + (32u << 20));
  const unsigned long long own0 = __builtin_amdgcn_s_memrealtime();
  k3_body(act, w2T, x, AoutT, out);
  asm volatile("s_waitcnt vmcnt(0) lgkmcnt(0)" ::: "memory"); __syncthreads();
  const unsigned long long own1 = __builtin_amdgcn_s_memrealtime();
  if (threadIdx.x < 64) {
    unsigned long long t0 = ~0ull, e1 = 0, s2 = ~0ull, e2 = 0;
    for (int i = threadIdx.x; i < K1_BLOCKS; i += 64) { unsigned long long a = GD[i * 2], b = GD[i * 2 + 1]; t0 = a < t0 ? a : t0; e1 = b > e1 ? b : e1; }
    for (int i = threadIdx.x; i < 256; i += 64) { unsigned long long a = GD[4096 + i * 2], b = GD[4096 + i * 2 + 1]; s2 = a < s2 ? a : s2; e2 = b > e2 ? b : e2; }
    for (int o = 32; o; o >>= 1) {
      unsigned long long x0 = __shfl_xor(t0, o), x1 = __shfl_xor(e1, o), x2 = __shfl_xor(s2, o), x3 = __shfl_xor(e2, o);
      t0 = x0 < t0 ? x0 : t0; e1 = x1 > e1 ? x1 : e1; s2 = x2 < s2 ? x2 : s2; e2 = x3 > e2 ? x3 : e2;
    }
    const unsigned long long q = DIAG == 0 ? t0 : DIAG == 1 ? e1 : DIAG == 2 ? s2 : DIAG == 3 ? e2 : DIAG == 4 ? own0 : own1;
    const unsigned long long tgt = q + 6000;
    for (int n = 0; n < 2000000; ++n) { if (__builtin_amdgcn_s_memrealtime() >= tgt) break; __builtin_amdgcn_s_sleep(1); }
  }
}

}

extern "C" void kernel_launch(void* const* d_in, const int* in_sizes, int n_in,
                              void* d_out, int out_size, void* d_ws, size_t ws_size,
                              hipStream_t stream) {
  const u16* x = (const u16*)d_in[0];
  const u16* logAr = (const u16*)d_in[1];
  const u16* Aim = (const u16*)d_in[2];
  const u16* Cre = (const u16*)d_in[3];
  const u16* Cim = (const u16*)d_in[4];
  const u16* logdt = (const u16*)d_in[5];
  const u16* Dskip = (const u16*)d_in[6];
  const u16* w1 = (const u16*)d_in[8];
  const u16* b1 = (const u16*)d_in[9];
  const u16* w2 = (const u16*)d_in[10];
  u16* out = (u16*)d_out;
  u16* ws = (u16*)d_ws;
  u16* xn = ws;
  u16* xnT = xn + (size_t)L_ * D_;
  u16* Kbuf = xnT + (size_t)L_ * D_;
  u16* w1T = Kbuf + (size_t)D_ * 4096;
  u16* w2T = w1T + (size_t)D_ * 1792;
  u16* act = w2T + (size_t)D_ * F_;
  u16* AoutT = act + (size_t)L_ * F_;
  u16* tbl = AoutT + (size_t)L_ * D_;
  k1_prep<<<K1_BLOCKS, 256, 0, stream>>>(x, logAr, Aim, Cre, Cim, logdt, w1, w2, xn, xnT, Kbuf, w1T, w2T, tbl);
  k2_conv_gemm1<<<256, 768, 0, stream>>>(xn, xnT, Kbuf, Dskip, w1T, b1, tbl, act, AoutT);
  k3_gemm2<<<256, 512, 0, stream>>>(act, w2T, x, AoutT, out);
}
```

```cpp
#include <hip/hip_runtime.h>
#include <hip/hip_bf16.h>
#include <stdint.h>
#include <type_traits>

#define TS_BEGIN(k)
#define TS_END(k)
#define STAMP(i)
#define STAMP_DUMP(a, b)
#define STAMP1(i)
#define STAMP_DUMP1(a, b)

#define NOPF
namespace {

constexpr int L_ = 2048, D_ = 640, NS_ = 128, F_ = 1728;

typedef __attribute__((ext_vector_type(8))) short bf16x8;
typedef __attribute__((ext_vector_type(4))) float f32x4;
typedef __attribute__((ext_vector_type(2))) float f32x2;
typedef __attribute__((ext_vector_type(2))) __bf16 bf16x2;
typedef unsigned short u16;

constexpr float LOG2E = 1.4426950408889634f;
constexpr float LN2 = 0.6931471805599453f;
constexpr float INV2PI = 0.15915494309189535f;

__device__ __forceinline__ float bf2f(u16 v) { return __uint_as_float(((uint32_t)v) << 16); }
__device__ __forceinline__ float bflo(uint32_t v) { return __uint_as_float(v << 16); }
__device__ __forceinline__ float bfhi(uint32_t v) { return __uint_as_float(v & 0xffff0000u); }
__device__ __forceinline__ u16 f2bf(float x) {
  __bf16 h = (__bf16)x;
  return __builtin_bit_cast(u16, h);
}
__device__ __forceinline__ float bfr(float x) { return bf2f(f2bf(x)); }
__device__ __forceinline__ uint32_t pack2(float a, float b) {
  f32x2 v = {a, b};
  bf16x2 r = __builtin_convertvector(v, bf16x2);
  return __builtin_bit_cast(uint32_t, r);
}

__device__ __forceinline__ float tanh_sp_bf(float h) {
  float e = bfr(__builtin_amdgcn_exp2f(-fabsf(h) * LOG2E));
  float s = e * (1.f - e * (0.5f - e * (1.f / 3.f)));
  float lg = __builtin_amdgcn_logf(1.f + e) * LN2;
  float l = bfr(e < 0.015625f ? s : lg);
  float sp = bfr(l + fmaxf(h, 0.f));
  float q = __builtin_amdgcn_exp2f(sp * (2.f * LOG2E));
  float t = 1.f - 2.f * __builtin_amdgcn_rcpf(q + 1.f);
  float ts = sp * (1.f - sp * sp * (1.f / 3.f));
  return bfr(sp < 0.03125f ? ts : t);
}
constexpr int TB_LO = 103 << 7, TB_HI = 134 << 7;
__device__ const uint32_t mish_tab[4096] = {
0x331a3319,0x331d331b,0x331f331e,0x33213320,0x33243323,0x33263325,0x33293327,0x332b332a,0x332d332c,0x3330332f,
0x33323331,0x33343333,0x33373336,0x33393338,0x333c333a,0x333e333d,0x3340333f,0x33433342,0x33453344,0x33483346,
0x334a3349,0x334c334b,0x334f334e,0x33513350,0x33543352,0x33563355,0x33583357,0x335b335a,0x335d335c,0x3360335e,
0x33623361,0x33643363,0x33673366,0x33693368,0x336b336a,0x336e336d,0x3370336f,0x33733371,0x33753374,0x33773376,
0x337a3379,0x337c337b,0x337f337d,0x33803380,0x33823381,0x33833382,0x33843383,0x33853385,0x33863386,0x33883387,
0x33893388,0x338a3389,0x338b338b,0x338c338c,0x338e338d,0x338f338e,0x3390338f,0x33913391,0x33923392,0x33943393,
0x33953394,0x33963395,0x33973397,0x33983398,0x339a3399,0x339d339b,0x339f339e,0x33a133a0,0x33a433a3,0x33a633a5,
0x33a933a7,0x33ab33aa,0x33ad33ac,0x33b033af,0x33b233b1,0x33b433b3,0x33b733b6,0x33b933b8,0x33bc33ba,0x33be33bd,
0x33c033bf,0x33c333c2,0x33c533c4,0x33c833c6,0x33ca33c9,0x33cc33cb,0x33cf33ce,0x33d133d0,0x33d433d2,0x33d633d5,
0x33d833d7,0x33db33da,0x33dd33dc,0x33e033de,0x33e233e1,0x33e433e3,0x33e733e6,0x33e933e8,0x33eb33ea,0x33ee33ed,
0x33f033ef,0x33f333f1,0x33f533f4,0x33f733f6,0x33fa33f9,0x33fc33fb,0x33ff33fd,0x34003400,0x34023401,0x34033402,
0x34043403,0x34053405,0x34063406,0x34083407,0x34093408,0x340a3409,0x340b340b,0x340c340c,0x340e340d,0x340f340e,
0x3410340f,0x34113411,0x34123412,0x34143413,0x34153414,0x34163415,0x34173417,0x34183418,0x341a3419,0x341d341b,
0x341f341e,0x34213420,0x34243423,0x34263425,0x34293427,0x342b342a,0x342d342c,0x3430342f,0x34323431,0x34343433,
0x34373436,0x34393438,0x343c343a,0x343e343d,0x3440343f,0x34433442,0x34453444,0x34483446,0x344a3449,0x344c344b,
0x344f344e,0x34513450,0x34543452,0x34563455,0x34583457,0x345b345a,0x345d345c,0x3460345e,0x34623461,0x34643463,
0x34673466,0x34693468,0x346b346a,0x346e346d,0x3470346f,0x34733471,0x34753474,0x34773476,0x347a3479,0x347c347b,
0x347f347d,0x34803480,0x34823481,0x34833482,0x34843483,0x34853485,0x34863486,0x34883487,0x34893488,0x348a3489,
0x348b348b,0x348c348c,0x348e348d,0x348f348e,0x3490348f,0x34913491,0x34923492,0x34943493,0x34953494,0x34963495,
0x34973497,0x34983498,0x349a3499,0x349d349b,0x349f349e,0x34a134a0,0x34a434a3,0x34a634a5,0x34a934a7,0x34ab34aa,
0x34ad34ac,0x34b034af,0x34b234b1,0x34b434b3,0x34b734b6,0x34b934b8,0x34bc34ba,0x34be34bd,0x34c034bf,0x34c334c2,
0x34c534c4,0x34c834c6,0x34ca34c9,0x34cc34cb,0x34cf34ce,0x34d134d0,0x34d434d2,0x34d634d5,0x34d834d7,0x34db34da,
0x34dd34dc,0x34e034de,0x34e234e1,0x34e434e3,0x34e734e6,0x34e934e8,0x34eb34ea,0x34ee34ed,0x34f034ef,0x34f334f1,
0x34f534f4,0x34f734f6,0x34fa34f9,0x34fc34fb,0x34ff34fd,0x35003500,0x35023501,0x35033502,0x35043503,0x35053505,
0x35063506,0x35083507,0x35093508,0x350a3509,0x350b350b,0x350c350c,0x350e350d,0x350f350e,0x3510350f,0x35113511,
0x35123512,0x35143513,0x35153514,0x35163515,0x35173517,0x35183518,0x351a3519,0x351d351b,0x351f351e,0x35213520,
0x35243523,0x35263525,0x35293527,0x352b352a,0x352d352c,0x3530352f,0x35323531,0x35343533,0x35373536,0x35393538,
0x353c353a,0x353e353d,0x3540353f,0x35433542,0x35453544,0x35483546,0x354a3549,0x354c354b,0x354f354e,0x35513550,
0x35543552,0x35563555,0x35583557,0x355b355a,0x355d355c,0x3560355e,0x35623561,0x35643563,0x35673566,0x35693568,
0x356b356a,0x356e356d,0x3570356f,0x35733571,0x35753574,0x35773576,0x357a3579,0x357c357b,0x357f357d,0x35803580,
0x35823581,0x35833582,0x35843583,0x35853585,0x35863586,0x35883587,0x35893588,0x358a3589,0x358b358b,0x358c358c,
0x358e358d,0x358f358e,0x3590358f,0x35913591,0x35923592,0x35943593,0x35953594,0x35963595,0x35973597,0x35983598,
0x359a3599,0x359d359b,0x359f359e,0x35a135a0,0x35a435a3,0x35a635a5,0x35a935a7,0x35ab35aa,0x35ad35ac,0x35b035af,
0x35b235b1,0x35b435b3,0x35b735b6,0x35b935b8,0x35bc35ba,0x35be35bd,0x35c035bf,0x35c335c2,0x35c535c4,0x35c835c6,
0x35ca35c9,0x35cc35cb,0x35cf35ce,0x35d135d0,0x35d435d2,0x35d635d5,0x35d835d7,0x35db35da,0x35dd35dc,0x35e035de,
0x35e235e1,0x35e435e3,0x35e735e6,0x35e935e8,0x35eb35ea,0x35ee35ed,0x35f035ef,0x35f335f1,0x35f535f4,0x35f735f6,
0x35fa35f9,0x35fc35fb,0x35ff35fd,0x36003600,0x36023601,0x36033602,0x36043603,0x36053605,0x36063606,0x36083607,
0x36093608,0x360a3609,0x360b360b,0x360c360c,0x360e360d,0x360f360e,0x3610360f,0x36113611,0x36123612,0x36143613,
0x36153614,0x36163615,0x36173617,0x36183618,0x361a3619,0x361d361b,0x361f361e,0x36213620,0x36243623,0x36263625,
0x36293627,0x362b362a,0x362d362c,0x3630362f,0x36323631,0x36343633,0x36373636,0x36393638,0x363c363a,0x363e363d,
0x3640363f,0x36433642,0x36453644,0x36483646,0x364a3649,0x364c364b,0x364f364e,0x36513650,0x36543652,0x36563655,
0x36583657,0x365b365a,0x365d365c,0x3660365e,0x36623661,0x36643663,0x36673666,0x36693668,0x366b366a,0x366e366d,
0x3670366f,0x36733671,0x36753674,0x36773676,0x367a3679,0x367c367b,0x367f367d,0x36803680,0x36823681,0x36833682,
0x36843683,0x36853685,0x36863686,0x36883687,0x36893688,0x368a3689,0x368b368b,0x368c368c,0x368e368d,0x368f368e,
0x3690368f,0x36913691,0x36923692,0x36943693,0x36953694,0x36963695,0x36973697,0x36983698,0x369a3699,0x369d369b,
0x369f369e,0x36a136a0,0x36a436a3,0x36a636a5,0x36a936a7,0x36ab36aa,0x36ad36ac,0x36b036af,0x36b236b1,0x36b436b3,
0x36b736b6,0x36b936b8,0x36bc36ba,0x36be36bd,0x36c036bf,0x36c336c2,0x36c536c4,0x36c836c6,0x36ca36c9,0x36cc36cb,
0x36cf36ce,0x36d136d0,0x36d436d2,0x36d636d5,0x36d836d7,0x36db36da,0x36dd36dc,0x36e036de,0x36e236e1,0x36e436e3,
0x36e736e6,0x36e936e8,0x36eb36ea,0x36ee36ed,0x36f036ef,0x36f336f1,0x36f536f4,0x36f736f6,0x36fa36f9,0x36fc36fb,
0x36ff36fd,0x37003700,0x37023701,0x37033702,0x37043703,0x37053705,0x37063706,0x37083707,0x37093708,0x370a3709,
0x370b370b,0x370c370c,0x370e370d,0x370f370e,0x3710370f,0x37113711,0x37123712,0x37143713,0x37153714,0x37163715,
0x37173717,0x37183718,0x371a3719,0x371d371b,0x371f371e,0x37213720,0x37243723,0x37263725,0x37293727,0x372b372a,
0x372d372c,0x3730372f,0x37323731,0x37343733,0x37373736,0x37393738,0x373c373a,0x373e373d,0x3740373f,0x37433742,
0x37453744,0x37483746,0x374a3749,0x374c374b,0x374f374e,0x37513750,0x37543752,0x37563755,0x37583757,0x375b375a,
0x375d375c,0x3760375e,0x37623761,0x37643763,0x37673766,0x37693768,0x376b376a,0x376e376d,0x3770376f,0x37733771,
0x37753774,0x37773776,0x377a3779,0x377c377b,0x377f377d,0x37803780,0x37823781,0x37833782,0x37843783,0x37853785,
0x37863786,0x37883787,0x37893788,0x378a3789,0x378b378b,0x378c378c,0x378e378d,0x378f378e,0x3790378f,0x37913791,
0x37923792,0x37943793,0x37953794,0x37963795,0x37973797,0x37983798,0x379a3799,0x379d379b,0x379f379e,0x37a137a0,
0x37a437a3,0x37a637a5,0x37a937a7,0x37ab37aa,0x37ad37ac,0x37b037af,0x37b237b1,0x37b437b3,0x37b737b6,0x37b937b8,
0x37bc37ba,0x37be37bd,0x37c037bf,0x37c337c2,0x37c537c4,0x37c837c6,0x37ca37c9,0x37cc37cb,0x37cf37ce,0x37d137d0,
0x37d437d2,0x37d637d5,0x37d837d7,0x37db37da,0x37dd37dc,0x37e037de,0x37e237e1,0x37e437e3,0x37e737e6,0x37e937e8,
0x37eb37ea,0x37ee37ed,0x37f037ef,0x37f337f1,0x37f537f4,0x37f737f6,0x37fa37f9,0x37fc37fb,0x37ff37fd,0x38003800,
0x38023801,0x38033802,0x38043803,0x38053805,0x38063806,0x38083807,0x38093808,0x380a3809,0x380b380b,0x380c380c,
0x380e380d,0x380f380e,0x3810380f,0x38113811,0x38123812,0x38143813,0x38153814,0x38163815,0x38173817,0x38183818,
0x381a3819,0x381d381b,0x381f381e,0x38213820,0x38243823,0x38263825,0x38293827,0x382b382a,0x382d382c,0x3830382f,
0x38323831,0x38343833,0x38373836,0x38393838,0x383c383a,0x383e383d,0x3840383f,0x38433842,0x38453844,0x38483846,
0x384a3849,0x384c384b,0x384f384e,0x38513850,0x38543852,0x38563855,0x38583857,0x385b385a,0x385d385c,0x3860385e,
0x38623861,0x38643863,0x38673866,0x38693868,0x386b386a,0x386e386d,0x3870386f,0x38733871,0x38753874,0x38773876,
0x387a3879,0x387c387b,0x387f387d,0x38803880,0x38823881,0x38833882,0x38843883,0x38853885,0x38863886,0x38883887,
0x38893888,0x388a3889,0x388b388b,0x388c388c,0x388e388d,0x388f388e,0x3890388f,0x38913891,0x38923892,0x38943893,
0x38953894,0x38963895,0x38973897,0x38983898,0x389a3899,0x389d389b,0x389f389e,0x38a138a0,0x38a438a3,0x38a638a5,
0x38a938a7,0x38ab38aa,0x38ad38ac,0x38b038af,0x38b238b1,0x38b438b3,0x38b738b6,0x38b938b8,0x38bc38ba,0x38be38bd,
0x38c038bf,0x38c338c2,0x38c538c4,0x38c838c6,0x38ca38c9,0x38cc38cb,0x38cf38ce,0x38d138d0,0x38d438d2,0x38d638d5,
0x38d838d7,0x38db38da,0x38dd38dc,0x38e038de,0x38e238e1,0x38e438e3,0x38e738e6,0x38e938e8,0x38eb38ea,0x38ee38ed,
0x38f038ef,0x38f338f1,0x38f538f4,0x38f738f6,0x38fa38f9,0x38fc38fb,0x38ff38fd,0x39003900,0x39023901,0x39033902,
0x39043903,0x39053905,0x39063906,0x39083907,0x39093908,0x390a3909,0x390b390b,0x390c390c,0x390e390d,0x390f390e,
0x3910390f,0x39113911,0x39123912,0x39143913,0x39153914,0x39163915,0x39173917,0x39183918,0x391a3919,0x391d391b,
0x391f391e,0x39213920,0x39243923,0x39263925,0x39293927,0x392b392a,0x392d392c,0x3930392f,0x39323931,0x39343933,
0x39373936,0x39393938,0x393c393a,0x393e393d,0x3940393f,0x39433942,0x39453944,0x39483946,0x394a3949,0x394c394b,
0x394f394e,0x39513950,0x39543952,0x39563955,0x39583957,0x395b395a,0x395d395c,0x3960395e,0x39623961,0x39643963,
0x39673966,0x39693968,0x396b396a,0x396e396d,0x3970396f,0x39733971,0x39753974,0x39773976,0x397a3979,0x397c397b,
0x397f397d,0x39803980,0x39823981,0x39833982,0x39843983,0x39853985,0x39863986,0x39883987,0x39893988,0x398a3989,
0x398b398b,0x398c398c,0x398e398d,0x398f398e,0x3990398f,0x39913991,0x39923992,0x39943993,0x39953994,0x39963995,
0x39973997,0x39983998,0x399a3999,0x399d399b,0x399f399e,0x39a139a0,0x39a439a3,0x39a639a5,0x39a939a7,0x39ab39aa,
0x39ad39ac,0x39b039af,0x39b239b1,0x39b439b3,0x39b739b6,0x39b939b8,0x39bc39ba,0x39be39bd,0x39c039bf,0x39c339c2,
0x39c539c4,0x39c839c6,0x39ca39c9,0x39cc39cb,0x39cf39ce,0x39d139d0,0x39d439d2,0x39d639d5,0x39d839d7,0x39db39da,
0x39dd39dc,0x39e039de,0x39e239e1,0x39e439e3,0x39e739e6,0x39e939e8,0x39eb39ea,0x39ee39ed,0x39f039ef,0x39f339f1,
0x39f539f4,0x39f739f6,0x39fa39f9,0x39fc39fb,0x39ff39fd,0x3a003a00,0x3a023a01,0x3a033a02,0x3a043a03,0x3a053a05,
0x3a063a06,0x3a083a07,0x3a093a08,0x3a0a3a09,0x3a0b3a0b,0x3a0c3a0c,0x3a0e3a0d,0x3a0f3a0e,0x3a103a0f,0x3a113a11,
0x3a123a12,0x3a143a13,0x3a153a14,0x3a163a15,0x3a173a17,0x3a183a18,0x3a1a3a19,0x3a1d3a1b,0x3a1f3a1e,0x3a213a20,
0x3a243a23,0x3a263a25,0x3a293a27,0x3a2b3a2a,0x3a2d3a2c,0x3a303a2f,0x3a323a31,0x3a343a33,0x3a373a36,0x3a393a38,
0x3a3c3a3a,0x3a3e3a3d,0x3a403a3f,0x3a433a42,0x3a453a44,0x3a483a46,0x3a4a3a49,0x3a4c3a4b,0x3a4f3a4e,0x3a513a50,
0x3a543a52,0x3a563a55,0x3a583a57,0x3a5b3a5a,0x3a5d3a5c,0x3a603a5e,0x3a623a61,0x3a643a63,0x3a673a66,0x3a693a68,
0x3a6b3a6a,0x3a6e3a6d,0x3a703a6f,0x3a733a71,0x3a753a74,0x3a773a76,0x3a7a3a79,0x3a7c3a7b,0x3a7f3a7d,0x3a803a80,
0x3a823a81,0x3a833a82,0x3a843a83,0x3a853a85,0x3a863a86,0x3a883a87,0x3a893a88,0x3a8a3a89,0x3a8b3a8b,0x3a8c3a8c,
0x3a8e3a8d,0x3a8f3a8e,0x3a903a8f,0x3a913a91,0x3a923a92,0x3a943a93,0x3a953a94,0x3a963a95,0x3a973a97,0x3a983a98,
0x3a9b3a9a,0x3a9e3a9c,0x3aa03a9f,0x3aa23aa1,0x3aa53aa4,0x3aa73aa6,0x3aaa3aa8,0x3aac3aab,0x3aae3aad,0x3ab13ab0,
0x3ab33ab2,0x3ab63ab4,0x3ab83ab7,0x3aba3ab9,0x3abd3abc,0x3abf3abe,0x3ac23ac0,0x3ac43ac3,0x3ac73ac5,0x3ac93ac8,
0x3acb3aca,0x3ace3acd,0x3ad03acf,0x3ad33ad1,0x3ad53ad4,0x3ad73ad6,0x3ada3ad9,0x3adc3adb,0x3adf3add,0x3ae13ae0,
0x3ae33ae2,0x3ae63ae5,0x3ae83ae7,0x3aeb3ae9,0x3aed3aec,0x3aef3aee,0x3af23af1,0x3af43af3,0x3af73af5,0x3af93af8,
0x3afb3afa,0x3afe3afd,0x3b003aff,0x3b013b01,0x3b033b02,0x3b043b03,0x3b053b04,0x3b063b06,0x3b073b07,0x3b093b08,
0x3b0a3b09,0x3b0b3b0a,0x3b0c3b0c,0x3b0d3b0d,0x3b0f3b0e,0x3b103b0f,0x3b113b10,0x3b123b12,0x3b133b13,0x3b153b14,
0x3b163b15,0x3b173b16,0x3b183b18,0x3b193b19,0x3b1b3b1a,0x3b1e3b1c,0x3b203b1f,0x3b223b21,0x3b253b24,0x3b273b26,
0x3b2a3b28,0x3b2c3b2b,0x3b2e3b2d,0x3b313b30,0x3b333b32,0x3b363b34,0x3b383b37,0x3b3a3b39,0x3b3d3b3c,0x3b3f3b3e,
0x3b423b40,0x3b443b43,0x3b473b45,0x3b493b48,0x3b4b3b4a,0x3b4e3b4d,0x3b503b4f,0x3b533b51,0x3b553b54,0x3b573b56,
0x3b5a3b59,0x3b5c3b5b,0x3b5f3b5d,0x3b613b60,0x3b633b62,0x3b663b65,0x3b683b67,0x3b6b3b69,0x3b6d3b6c,0x3b6f3b6e,
0x3b723b71,0x3b743b73,0x3b773b75,0x3b793b78,0x3b7b3b7a,0x3b7e3b7d,0x3b803b7f,0x3b813b81,0x3b833b82,0x3b843b83,
0x3b853b84,0x3b863b86,0x3b873b87,0x3b893b88,0x3b8a3b89,0x3b8b3b8a,0x3b8c3b8c,0x3b8d3b8d,0x3b8f3b8e,0x3b903b8f,
0x3b913b90,0x3b923b92,0x3b933b93,0x3b953b94,0x3b963b95,0x3b973b96,0x3b983b98,0x3b993b99,0x3b9b3b9a,0x3b9e3b9c,
0x3ba03b9f,0x3ba23ba1,0x3ba53ba4,0x3ba73ba6,0x3baa3ba8,0x3bac3bab,0x3bae3bad,0x3bb13bb0,0x3bb33bb2,0x3bb63bb4,
0x3bb83bb7,0x3bba3bb9,0x3bbd3bbc,0x3bbf3bbe,0x3bc33bc0,0x3bc53bc4,0x3bc83bc7,0x3bca3bc9,0x3bcd3bcb,0x3bcf3bce,
0x3bd13bd0,0x3bd43bd3,0x3bd63bd5,0x3bd93bd8,0x3bdb3bda,0x3bde3bdc,0x3be03bdf,0x3be23be1,0x3be53be4,0x3be73be6,
0x3bea3be8,0x3bec3beb,0x3bef3bed,0x3bf13bf0,0x3bf33bf2,0x3bf63bf5,0x3bf83bf7,0x3bfb3bf9,0x3bfd3bfc,0x3c003bfe,
0x3c013c00,0x3c023c02,0x3c033c03,0x3c053c04,0x3c063c05,0x3c073c06,0x3c083c08,0x3c093c09,0x3c0b3c0a,0x3c0c3c0b,
0x3c0d3c0c,0x3c0e3c0e,0x3c0f3c0f,0x3c113c10,0x3c123c11,0x3c133c13,0x3c143c14,0x3c163c15,0x3c173c16,0x3c183c17,
0x3c193c19,0x3c1a3c1a,0x3c1c3c1b,0x3c1f3c1d,0x3c213c20,0x3c233c22,0x3c263c25,0x3c283c27,0x3c2b3c2a,0x3c2d3c2c,
0x3c303c2e,0x3c323c31,0x3c343c33,0x3c373c36,0x3c393c38,0x3c3c3c3a,0x3c3e3c3d,0x3c413c3f,0x3c433c42,0x3c453c44,
0x3c483c47,0x3c4a3c49,0x3c4d3c4b,0x3c4f3c4e,0x3c513c50,0x3c543c53,0x3c583c55,0x3c593c58,0x3c5b3c5a,0x3c5e3c5c,
0x3c603c5f,0x3c623c61,0x3c653c64,0x3c673c66,0x3c6a3c68,0x3c6c3c6b,0x3c6f3c6d,0x3c713c70,0x3c733c72,0x3c763c75,
0x3c783c77,0x3c7b3c79,0x3c7f3c7c,0x3c813c80,0x3c823c81,0x3c833c82,0x3c843c84,0x3c853c85,0x3c873c86,0x3c883c87,
0x3c893c88,0x3c8a3c8a,0x3c8c3c8b,0x3c8d3c8c,0x3c8e3c8d,0x3c8f3c8f,0x3c903c90,0x3c923c91,0x3c933c92,0x3c943c93,
0x3c953c95,0x3c973c96,0x3c983c97,0x3c993c98,0x3c9a3c9a,0x3c9b3c9b,0x3c9d3c9c,0x3ca03c9e,0x3ca23ca1,0x3ca53ca3,
0x3ca73ca6,0x3ca93ca8,0x3cac3cab,0x3cae3cad,0x3cb13cb0,0x3cb33cb2,0x3cb63cb4,0x3cb83cb7,0x3cbc3cb9,0x3cbd3cbd,
0x3cbf3cbe,0x3cc23cc1,0x3cc43cc3,0x3cc73cc5,0x3cc93cc8,0x3ccc3cca,0x3ccf3ccd,0x3cd23cd1,0x3cd43cd3,0x3cd73cd5,
0x3cd93cd8,0x3cdc3cda,0x3cde3cdd,0x3ce03cdf,0x3ce43ce3,0x3ce73ce6,0x3ce83ce8,0x3cea3ce9,0x3ced3cec,0x3cef3cee,
0x3cf23cf0,0x3cf43cf3,0x3cf83cf7,0x3cfb3cf9,0x3cfd3cfc,0x3d003cfe,0x3d013d00,0x3d023d02,0x3d033d03,0x3d053d04,
0x3d063d05,0x3d073d07,0x3d083d08,0x3d0a3d09,0x3d0b3d0a,0x3d0c3d0b,0x3d0d3d0d,0x3d0f3d0e,0x3d103d0f,0x3d113d10,
0x3d123d12,0x3d143d13,0x3d153d14,0x3d163d15,0x3d173d17,0x3d183d18,0x3d1b3d1a,0x3d1c3d1b,0x3d1d3d1d,0x3d1e3d1e,
0x3d1f3d1e,0x3d223d20,0x3d253d24,0x3d283d26,0x3d2a3d29,0x3d2d3d2b,0x3d303d2e,0x3d333d32,0x3d353d34,0x3d373d35,
0x3d3a3d38,0x3d3d3d3c,0x3d3f3d3e,0x3d423d40,0x3d443d43,0x3d473d46,0x3d493d48,0x3d4c3d4a,0x3d4e3d4d,0x3d513d50,
0x3d533d52,0x3d563d54,0x3d5a3d57,0x3d5c3d5b,0x3d5f3d5d,0x3d613d60,0x3d643d61,0x3d663d65,0x3d693d67,0x3d6b3d6a,
0x3d6e3d6c,0x3d703d6f,0x3d733d72,0x3d753d74,0x3d793d77,0x3d7a3d79,0x3d7d3d7c,0x3d7f3d7e,0x3d823d80,0x3d833d82,
0x3d843d84,0x3d863d85,0x3d883d87,0x3d883d88,0x3d893d89,0x3d8b3d8a,0x3d8d3d8c,0x3d8e3d8d,0x3d8f3d8f,0x3d913d90,
0x3d923d91,0x3d933d92,0x3d943d94,0x3d963d95,0x3d973d96,0x3d983d98,0x3d993d99,0x3d9b3d9a,0x3d9d3d9c,0x3d9e3d9e,
0x3da03d9f,0x3da03da0,0x3da23da1,0x3da33da3,0x3da53da4,0x3da83da7,0x3daa3da9,0x3dad3dac,0x3db03dae,0x3db33db2,
0x3db53db3,0x3db83db7,0x3dbb3dba,0x3dbf3dbc,0x3dc03dc0,0x3dc43dc1,0x3dc63dc5,0x3dc93dc8,0x3dcc3dca,0x3dce3dcd,
0x3dd13dd0,0x3dd53dd2,0x3dd73dd6,0x3dda3dd9,0x3ddc3ddb,0x3ddf3dde,0x3de23de0,0x3de63de3,0x3de73de7,0x3deb3de8,
0x3dee3dec,0x3df03def,0x3df33df2,0x3df53df4,0x3df83df7,0x3dfb3df9,0x3dfd3dfc,0x3e013dff,0x3e023e01,0x3e043e03,
0x3e053e05,0x3e073e06,0x3e083e07,0x3e093e09,0x3e0b3e0a,0x3e0c3e0c,0x3e0d3e0d,0x3e103e0f,0x3e113e10,0x3e123e12,
0x3e143e13,0x3e153e14,0x3e163e16,0x3e193e18,0x3e1a3e19,0x3e1b3e1b,0x3e1d3e1c,0x3e1e3e1d,0x3e1f3e1f,0x3e213e20,
0x3e223e21,0x3e243e24,0x3e263e25,0x3e273e26,0x3e283e28,0x3e2a3e29,0x3e2b3e2a,0x3e2d3e2d,0x3e2f3e2d,0x3e323e31,
0x3e353e33,0x3e393e36,0x3e3b3e3a,0x3e3e3e3e,0x3e413e40,0x3e453e43,0x3e473e46,0x3e4a3e49,0x3e4e3e4d,0x3e513e4f,
0x3e543e52,0x3e583e56,0x3e5a3e59,0x3e5d3e5c,0x3e603e5e,0x3e643e63,0x3e673e65,0x3e6a3e68,0x3e6e3e6c,0x3e703e6f,
0x3e733e72,0x3e763e75,0x3e7a3e79,0x3e7d3e7c,0x3e803e7f,0x3e823e81,0x3e843e83,0x3e853e84,0x3e873e86,0x3e893e88,
0x3e8a3e89,0x3e8b3e8b,0x3e8e3e8d,0x3e8f3e8e,0x3e903e90,0x3e933e92,0x3e943e93,0x3e963e95,0x3e973e97,0x3e993e99,
0x3e9b3e9a,0x3e9c3e9c,0x3e9f3e9e,0x3ea03e9f,0x3ea13ea1,0x3ea43ea2,0x3ea53ea4,0x3ea73ea6,0x3ea83ea7,0x3eab3ea9,
0x3eac3eab,0x3ead3ead,0x3eb03eae,0x3eb13eb1,0x3eb33eb2,0x3eb43eb5,0x3eb73eb6,0x3eb83eb8,0x3eba3eb9,0x3ebc3ebc,
0x3ebe3ebd,0x3ebf3ebe,0x3ec23ec0,0x3ec63ec3,0x3ec93ec7,0x3ecd3ecb,0x3ed13ece,0x3ed43ed2,0x3ed83ed5,0x3edb3ed9,
0x3edf3ede,0x3ee23ee1,0x3ee53ee4,0x3eea3ee8,0x3eee3eeb,0x3ef13eef,0x3ef53ef4,0x3efa3ef7,0x3efd3efb,0x3f003efe,
0x3f023f01,0x3f043f03,0x3f063f05,0x3f083f07,0x3f0a3f09,0x3f0b3f0b,0x3f0d3f0c,0x3f0f3f0f,0x3f123f11,0x3f133f12,
0x3f153f14,0x3f173f16,0x3f193f18,0x3f1b3f1a,0x3f1e3f1d,0x3f1f3f1e,0x3f213f20,0x3f233f22,0x3f253f24,0x3f273f26,
0x3f293f28,0x3f2b3f2a,0x3f2d3f2c,0x3f2f3f2e,0x3f313f30,0x3f343f32,0x3f353f35,0x3f373f36,0x3f3a3f38,0x3f3b3f3a,
0x3f3d3f3c,0x3f403f3e,0x3f413f40,0x3f443f43,0x3f463f45,0x3f473f46,0x3f4a3f49,0x3f4c3f4b,0x3f4e3f4c,0x3f503f4f,
0x3f523f51,0x3f543f52,0x3f563f55,0x3f583f57,0x3f5a3f59,0x3f5c3f5b,0x3f603f5d,0x3f633f61,0x3f683f66,0x3f6c3f69,
0x3f713f6e,0x3f743f73,0x3f793f77,0x3f7e3f7b,0x3f813f7f,0x3f833f82,0x3f853f84,0x3f873f86,0x3f893f88,0x3f8b3f8a,
0x3f8e3f8d,0x3f903f8f,0x3f923f91,0x3f943f93,0x3f963f95,0x3f993f97,0x3f9a3f9a,0x3f9d3f9b,0x3f9f3f9e,0x3fa13fa0,
0x3fa33fa2,0x3fa63fa4,0x3fa83fa7,0x3fa93fa8,0x3fac3fab,0x3faf3fad,0x3fb03fb0,0x3fb23fb1,0x3fb53fb4,0x3fb73fb6,
0x3fb93fb8,0x3fbb3fba,0x3fbd3fbc,0x3fc03fbe,0x3fc23fc1,0x3fc43fc3,0x3fc63fc5,0x3fc83fc7,0x3fca3fc9,0x3fcd3fcb,
0x3fcf3fce,0x3fd13fd0,0x3fd43fd2,0x3fd53fd4,0x3fd73fd6,0x3fd93fd8,0x3fdc3fdb,0x3fde3fdd,0x3fe03fdf,0x3fe33fe1,
0x3fe53fe4,0x3fe73fe6,0x3fe93fe8,0x3fea3fe9,0x3fed3fec,0x3fef3fee,0x3ff13ff0,0x3ff33ff2,0x3ff63ff4,0x3ff83ff7,
0x3ffb3ff9,0x3fff3ffd,0x40024001,0x40044003,0x40064005,0x40084007,0x400a4009,0x400c400b,0x400e400d,0x40114010,
0x40134012,0x40154014,0x40174016,0x40194018,0x401b401a,0x401d401c,0x401f401e,0x40214020,0x40234022,0x40264025,
0x40284027,0x402a4029,0x402c402b,0x402e402d,0x4030402f,0x40324031,0x40344033,0x40364035,0x40384037,0x403a4039,
0x403c403b,0x403e403d,0x4040403f,0x40424041,0x40444043,0x40464045,0x40484047,0x404a4049,0x404c404b,0x404e404d,
0x4050404f,0x40524051,0x40544053,0x40564055,0x40584057,0x405a4059,0x405d405c,0x405f405e,0x40614060,0x40634062,
0x40654064,0x40674066,0x40694068,0x406b406a,0x406d406c,0x406f406e,0x40714070,0x40734072,0x40754074,0x40774076,
0x40794078,0x407b407a,0x407d407c,0x407f407e,0x40814080,0x40834082,0x40854084,0x40874086,0x40894088,0x408b408a,
0x408d408c,0x408f408e,0x40914090,0x40934092,0x40954094,0x40974096,0x40994098,0x409b409a,0x409d409c,0x409f409e,
0x40a140a0,0x40a340a2,0x40a540a4,0x40a740a6,0x40a940a8,0x40ab40aa,0x40ad40ac,0x40af40ae,0x40b140b0,0x40b340b2,
0x40b540b4,0x40b740b6,0x40b940b8,0x40bb40ba,0x40bd40bc,0x40bf40be,0x40c140c0,0x40c340c2,0x40c540c4,0x40c740c6,
0x40c940c8,0x40cb40ca,0x40cd40cc,0x40cf40ce,0x40d140d0,0x40d340d2,0x40d540d4,0x40d740d6,0x40d940d8,0x40db40da,
0x40dd40dc,0x40df40de,0x40e140e0,0x40e340e2,0x40e540e4,0x40e740e6,0x40e940e8,0x40eb40ea,0x40ed40ec,0x40ef40ee,
0x40f140f0,0x40f340f2,0x40f540f4,0x40f740f6,0x40f940f8,0x40fb40fa,0x40fd40fc,0x40ff40fe,0x41014100,0x41034102,
0x41054104,0x41074106,0x41094108,0x410b410a,0x410d410c,0x410f410e,0x41114110,0x41134112,0x41154114,0x41174116,
0x41194118,0x411b411a,0x411d411c,0x411f411e,0x41214120,0x41234122,0x41254124,0x41274126,0x41294128,0x412b412a,
0x412d412c,0x412f412e,0x41314130,0x41334132,0x41354134,0x41374136,0x41394138,0x413b413a,0x413d413c,0x413f413e,
0x41414140,0x41434142,0x41454144,0x41474146,0x41494148,0x414b414a,0x414d414c,0x414f414e,0x41514150,0x41534152,
0x41554154,0x41574156,0x41594158,0x415b415a,0x415d415c,0x415f415e,0x41614160,0x41634162,0x41654164,0x41674166,
0x41694168,0x416b416a,0x416d416c,0x416f416e,0x41714170,0x41734172,0x41754174,0x41774176,0x41794178,0x417b417a,
0x417d417c,0x417f417e,0x41814180,0x41834182,0x41854184,0x41874186,0x41894188,0x418b418a,0x418d418c,0x418f418e,
0x41914190,0x41934192,0x41954194,0x41974196,0x41994198,0x419b419a,0x419d419c,0x419f419e,0x41a141a0,0x41a341a2,
0x41a541a4,0x41a741a6,0x41a941a8,0x41ab41aa,0x41ad41ac,0x41af41ae,0x41b141b0,0x41b341b2,0x41b541b4,0x41b741b6,
0x41b941b8,0x41bb41ba,0x41bd41bc,0x41bf41be,0x41c141c0,0x41c341c2,0x41c541c4,0x41c741c6,0x41c941c8,0x41cb41ca,
0x41cd41cc,0x41cf41ce,0x41d141d0,0x41d341d2,0x41d541d4,0x41d741d6,0x41d941d8,0x41db41da,0x41dd41dc,0x41df41de,
0x41e141e0,0x41e341e2,0x41e541e4,0x41e741e6,0x41e941e8,0x41eb41ea,0x41ed41ec,0x41ef41ee,0x41f141f0,0x41f341f2,
0x41f541f4,0x41f741f6,0x41f941f8,0x41fb41fa,0x41fd41fc,0x41ff41fe,0x42014200,0x42034202,0x42054204,0x42074206,
0x42094208,0x420b420a,0x420d420c,0x420f420e,0x42114210,0x42134212,0x42154214,0x42174216,0x42194218,0x421b421a,
0x421d421c,0x421f421e,0x42214220,0x42234222,0x42254224,0x42274226,0x42294228,0x422b422a,0x422d422c,0x422f422e,
0x42314230,0x42334232,0x42354234,0x42374236,0x42394238,0x423b423a,0x423d423c,0x423f423e,0x42414240,0x42434242,
0x42454244,0x42474246,0x42494248,0x424b424a,0x424d424c,0x424f424e,0x42514250,0x42534252,0x42554254,0x42574256,
0x42594258,0x425b425a,0x425d425c,0x425f425e,0x42614260,0x42634262,0x42654264,0x42674266,0x42694268,0x426b426a,
0x426d426c,0x426f426e,0x42714270,0x42734272,0x42754274,0x42774276,0x42794278,0x427b427a,0x427d427c,0x427f427e,
0x42814280,0x42834282,0x42854284,0x42874286,0x42894288,0x428b428a,0x428d428c,0x428f428e,0x42914290,0x42934292,
0x42954294,0x42974296,0x42994298,0x429b429a,0x429d429c,0x429f429e,0x42a142a0,0x42a342a2,0x42a542a4,0x42a742a6,
0x42a942a8,0x42ab42aa,0x42ad42ac,0x42af42ae,0x42b142b0,0x42b342b2,0x42b542b4,0x42b742b6,0x42b942b8,0x42bb42ba,
0x42bd42bc,0x42bf42be,0x42c142c0,0x42c342c2,0x42c542c4,0x42c742c6,0x42c942c8,0x42cb42ca,0x42cd42cc,0x42cf42ce,
0x42d142d0,0x42d342d2,0x42d542d4,0x42d742d6,0x42d942d8,0x42db42da,0x42dd42dc,0x42df42de,0x42e142e0,0x42e342e2,
0x42e542e4,0x42e742e6,0x42e942e8,0x42eb42ea,0x42ed42ec,0x42ef42ee,0x42f142f0,0x42f342f2,0x42f542f4,0x42f742f6,
0x42f942f8,0x42fb42fa,0x42fd42fc,0x42ff42fe,0x43014300,0x43034302,0x43054304,0x43074306,0x43094308,0x430b430a,
0x430d430c,0x430f430e,0x43114310,0x43134312,0x43154314,0x43174316,0x43194318,0x431b431a,0x431d431c,0x431f431e,
0x43214320,0x43234322,0x43254324,0x43274326,0x43294328,0x432b432a,0x432d432c,0x432f432e,0x43314330,0x43334332,
0x43354334,0x43374336,0x43394338,0x433b433a,0x433d433c,0x433f433e,0x43414340,0x43434342,0x43454344,0x43474346,
0x43494348,0x434b434a,0x434d434c,0x434f434e,0x43514350,0x43534352,0x43554354,0x43574356,0x43594358,0x435b435a,
0x435d435c,0x435f435e,0x43614360,0x43634362,0x43654364,0x43674366,0x43694368,0x436b436a,0x436d436c,0x436f436e,
0x43714370,0x43734372,0x43754374,0x43774376,0x43794378,0x437b437a,0x437d437c,0x437f437e,0xb31ab319,0xb31db31b,
0xb31fb31e,0xb321b320,0xb324b323,0xb326b325,0xb329b327,0xb32bb32a,0xb32db32c,0xb330b32f,0xb332b331,0xb334b333,
0xb337b336,0xb339b338,0xb33cb33a,0xb33eb33d,0xb340b33f,0xb343b342,0xb345b344,0xb348b346,0xb34ab349,0xb34cb34b,
0xb34fb34e,0xb351b350,0xb354b352,0xb356b355,0xb358b357,0xb35bb35a,0xb35db35c,0xb360b35e,0xb362b361,0xb364b363,
0xb367b366,0xb369b368,0xb36bb36a,0xb36eb36d,0xb370b36f,0xb373b371,0xb375b374,0xb377b376,0xb37ab379,0xb37cb37b,
0xb37fb37d,0xb380b380,0xb382b381,0xb383b382,0xb384b383,0xb385b385,0xb386b386,0xb388b387,0xb389b388,0xb38ab389,
0xb38bb38b,0xb38cb38c,0xb38eb38d,0xb38fb38e,0xb390b38f,0xb391b391,0xb392b392,0xb394b393,0xb395b394,0xb396b395,
0xb397b397,0xb398b398,0xb39ab399,0xb39db39b,0xb39fb39e,0xb3a1b3a0,0xb3a4b3a3,0xb3a6b3a5,0xb3a9b3a7,0xb3abb3aa,
0xb3adb3ac,0xb3b0b3af,0xb3b2b3b1,0xb3b4b3b3,0xb3b7b3b6,0xb3b9b3b8,0xb3bcb3ba,0xb3beb3bd,0xb3c0b3bf,0xb3c3b3c2,
0xb3c5b3c4,0xb3c8b3c6,0xb3cab3c9,0xb3ccb3cb,0xb3cfb3ce,0xb3d1b3d0,0xb3d4b3d2,0xb3d6b3d5,0xb3d8b3d7,0xb3dbb3da,
0xb3ddb3dc,0xb3e0b3de,0xb3e2b3e1,0xb3e4b3e3,0xb3e7b3e6,0xb3e9b3e8,0xb3ebb3ea,0xb3eeb3ed,0xb3f0b3ef,0xb3f3b3f1,
0xb3f5b3f4,0xb3f7b3f6,0xb3fab3f9,0xb3fcb3fb,0xb3ffb3fd,0xb400b400,0xb402b401,0xb403b402,0xb404b403,0xb405b405,
0xb406b406,0xb408b407,0xb409b408,0xb40ab409,0xb40bb40b,0xb40cb40c,0xb40eb40d,0xb40fb40e,0xb410b40f,0xb411b411,
0xb412b412,0xb414b413,0xb415b414,0xb416b415,0xb417b417,0xb418b418,0xb41ab419,0xb41db41b,0xb41fb41e,0xb421b420,
0xb424b423,0xb426b425,0xb429b427,0xb42bb42a,0xb42db42c,0xb430b42f,0xb432b431,0xb434b433,0xb437b436,0xb439b438,
0xb43cb43a,0xb43eb43d,0xb440b43f,0xb443b442,0xb445b444,0xb448b446,0xb44ab449,0xb44cb44b,0xb44fb44e,0xb451b450,
0xb454b452,0xb456b455,0xb458b457,0xb45bb45a,0xb45db45c,0xb460b45e,0xb462b461,0xb464b463,0xb467b466,0xb469b468,
0xb46bb46a,0xb46eb46d,0xb470b46f,0xb473b471,0xb475b474,0xb477b476,0xb47ab479,0xb47cb47b,0xb47fb47d,0xb480b480,
0xb482b481,0xb483b482,0xb484b483,0xb485b485,0xb486b486,0xb488b487,0xb489b488,0xb48ab489,0xb48bb48b,0xb48cb48c,
0xb48eb48d,0xb48fb48e,0xb490b48f,0xb491b491,0xb492b492,0xb494b493,0xb495b494,0xb496b495,0xb497b497,0xb498b498,
0xb49ab499,0xb49db49b,0xb49fb49e,0xb4a1b4a0,0xb4a4b4a3,0xb4a6b4a5,0xb4a9b4a7,0xb4abb4aa,0xb4adb4ac,0xb4b0b4af,
0xb4b2b4b1,0xb4b4b4b3,0xb4b7b4b6,0xb4b9b4b8,0xb4bcb4ba,0xb4beb4bd,0xb4c0b4bf,0xb4c3b4c2,0xb4c5b4c4,0xb4c8b4c6,
0xb4cab4c9,0xb4ccb4cb,0xb4cfb4ce,0xb4d1b4d0,0xb4d4b4d2,0xb4d6b4d5,0xb4d8b4d7,0xb4dbb4da,0xb4ddb4dc,0xb4e0b4de,
0xb4e2b4e1,0xb4e4b4e3,0xb4e7b4e6,0xb4e9b4e8,0xb4ebb4ea,0xb4eeb4ed,0xb4f0b4ef,0xb4f3b4f1,0xb4f5b4f4,0xb4f7b4f6,
0xb4fab4f9,0xb4fcb4fb,0xb4ffb4fd,0xb500b500,0xb502b501,0xb503b502,0xb504b503,0xb505b505,0xb506b506,0xb508b507,
0xb509b508,0xb50ab509,0xb50bb50b,0xb50cb50c,0xb50eb50d,0xb50fb50e,0xb510b50f,0xb511b511,0xb512b512,0xb514b513,
0xb515b514,0xb516b515,0xb517b517,0xb518b518,0xb51ab519,0xb51db51b,0xb51fb51e,0xb521b520,0xb524b523,0xb526b525,
0xb529b527,0xb52bb52a,0xb52db52c,0xb530b52f,0xb532b531,0xb534b533,0xb537b536,0xb539b538,0xb53cb53a,0xb53eb53d,
0xb540b53f,0xb543b542,0xb545b544,0xb548b546,0xb54ab549,0xb54cb54b,0xb54fb54e,0xb551b550,0xb554b552,0xb556b555,
0xb558b557,0xb55bb55a,0xb55db55c,0xb560b55e,0xb562b561,0xb564b563,0xb567b566,0xb569b568,0xb56bb56a,0xb56eb56d,
0xb570b56f,0xb573b571,0xb575b574,0xb577b576,0xb57ab579,0xb57cb57b,0xb57fb57d,0xb580b580,0xb582b581,0xb583b582,
0xb584b583,0xb585b585,0xb586b586,0xb588b587,0xb589b588,0xb58ab589,0xb58bb58b,0xb58cb58c,0xb58eb58d,0xb58fb58e,
0xb590b58f,0xb591b591,0xb592b592,0xb594b593,0xb595b594,0xb596b595,0xb597b597,0xb598b598,0xb59ab599,0xb59db59b,
0xb59fb59e,0xb5a1b5a0,0xb5a4b5a3,0xb5a6b5a5,0xb5a9b5a7,0xb5abb5aa,0xb5adb5ac,0xb5b0b5af,0xb5b2b5b1,0xb5b4b5b3,
0xb5b7b5b6,0xb5b9b5b8,0xb5bcb5ba,0xb5beb5bd,0xb5c0b5bf,0xb5c3b5c2,0xb5c5b5c4,0xb5c8b5c6,0xb5cab5c9,0xb5ccb5cb,
0xb5cfb5ce,0xb5d1b5d0,0xb5d4b5d2,0xb5d6b5d5,0xb5d8b5d7,0xb5dbb5da,0xb5ddb5dc,0xb5e0b5de,0xb5e2b5e1,0xb5e4b5e3,
0xb5e7b5e6,0xb5e9b5e8,0xb5ebb5ea,0xb5eeb5ed,0xb5f0b5ef,0xb5f3b5f1,0xb5f5b5f4,0xb5f7b5f6,0xb5fab5f9,0xb5fcb5fb,
0xb5ffb5fd,0xb600b600,0xb602b601,0xb603b602,0xb604b603,0xb605b605,0xb606b606,0xb608b607,0xb609b608,0xb60ab609,
0xb60bb60b,0xb60cb60c,0xb60eb60d,0xb60fb60e,0xb610b60f,0xb611b611,0xb612b612,0xb614b613,0xb615b614,0xb616b615,
0xb617b617,0xb618b618,0xb61ab619,0xb61db61b,0xb61fb61e,0xb621b620,0xb624b623,0xb626b625,0xb629b627,0xb62bb62a,
0xb62db62c,0xb630b62f,0xb632b631,0xb634b633,0xb637b636,0xb639b638,0xb63cb63a,0xb63eb63d,0xb640b63f,0xb643b642,
0xb645b644,0xb648b646,0xb64ab649,0xb64cb64b,0xb64fb64e,0xb651b650,0xb654b652,0xb656b655,0xb658b657,0xb65bb65a,
0xb65db65c,0xb660b65e,0xb662b661,0xb664b663,0xb667b666,0xb669b668,0xb66bb66a,0xb66eb66d,0xb670b66f,0xb673b671,
0xb675b674,0xb677b676,0xb67ab679,0xb67cb67b,0xb67fb67d,0xb680b680,0xb682b681,0xb683b682,0xb684b683,0xb685b685,
0xb686b686,0xb688b687,0xb689b688,0xb68ab689,0xb68bb68b,0xb68cb68c,0xb68eb68d,0xb68fb68e,0xb690b68f,0xb691b691,
0xb692b692,0xb694b693,0xb695b694,0xb696b695,0xb697b697,0xb698b698,0xb69ab699,0xb69db69b,0xb69fb69e,0xb6a1b6a0,
0xb6a4b6a3,0xb6a6b6a5,0xb6a9b6a7,0xb6abb6aa,0xb6adb6ac,0xb6b0b6af,0xb6b2b6b1,0xb6b4b6b3,0xb6b7b6b6,0xb6b9b6b8,
0xb6bcb6ba,0xb6beb6bd,0xb6c0b6bf,0xb6c3b6c2,0xb6c5b6c4,0xb6c8b6c6,0xb6cab6c9,0xb6ccb6cb,0xb6cfb6ce,0xb6d1b6d0,
0xb6d4b6d2,0xb6d6b6d5,0xb6d8b6d7,0xb6dbb6da,0xb6ddb6dc,0xb6e0b6de,0xb6e2b6e1,0xb6e4b6e3,0xb6e7b6e6,0xb6e9b6e8,
0xb6ebb6ea,0xb6eeb6ed,0xb6f0b6ef,0xb6f3b6f1,0xb6f5b6f4,0xb6f7b6f6,0xb6fab6f9,0xb6fcb6fb,0xb6ffb6fd,0xb700b700,
0xb702b701,0xb703b702,0xb704b703,0xb705b705,0xb706b706,0xb708b707,0xb709b708,0xb70ab709,0xb70bb70b,0xb70cb70c,
0xb70eb70d,0xb70fb70e,0xb710b70f,0xb711b711,0xb712b712,0xb714b713,0xb715b714,0xb716b715,0xb717b717,0xb718b718,
0xb71ab719,0xb71db71b,0xb71fb71e,0xb721b720,0xb724b723,0xb726b725,0xb729b727,0xb72bb72a,0xb72db72c,0xb730b72f,
0xb732b731,0xb734b733,0xb737b736,0xb739b738,0xb73cb73a,0xb73eb73d,0xb740b73f,0xb743b742,0xb745b744,0xb748b746,
0xb74ab749,0xb74cb74b,0xb74fb74e,0xb751b750,0xb754b752,0xb756b755,0xb758b757,0xb75bb75a,0xb75db75c,0xb760b75e,
0xb762b761,0xb764b763,0xb767b766,0xb769b768,0xb76bb76a,0xb76eb76d,0xb770b76f,0xb773b771,0xb775b774,0xb777b776,
0xb77ab779,0xb77cb77b,0xb77fb77d,0xb780b780,0xb782b781,0xb783b782,0xb784b783,0xb785b785,0xb786b786,0xb788b787,
0xb789b788,0xb78ab789,0xb78bb78b,0xb78cb78c,0xb78eb78d,0xb78fb78e,0xb790b78f,0xb791b791,0xb792b792,0xb794b793,
0xb795b794,0xb796b795,0xb797b797,0xb798b798,0xb79ab799,0xb79db79b,0xb79fb79e,0xb7a1b7a0,0xb7a4b7a3,0xb7a6b7a5,
0xb7a9b7a7,0xb7abb7aa,0xb7adb7ac,0xb7b0b7af,0xb7b2b7b1,0xb7b4b7b3,0xb7b7b7b6,0xb7b9b7b8,0xb7bcb7ba,0xb7beb7bd,
0xb7c0b7bf,0xb7c3b7c2,0xb7c5b7c4,0xb7c8b7c6,0xb7cab7c9,0xb7ccb7cb,0xb7cfb7ce,0xb7d1b7d0,0xb7d4b7d2,0xb7d6b7d5,
0xb7d8b7d7,0xb7dbb7da,0xb7ddb7dc,0xb7e0b7de,0xb7e2b7e1,0xb7e4b7e3,0xb7e7b7e6,0xb7e9b7e8,0xb7ebb7ea,0xb7eeb7ed,
0xb7f0b7ef,0xb7f3b7f1,0xb7f5b7f4,0xb7f7b7f6,0xb7fab7f9,0xb7fcb7fb,0xb7ffb7fd,0xb800b800,0xb802b801,0xb803b802,
0xb804b803,0xb805b805,0xb806b806,0xb808b807,0xb809b808,0xb80ab809,0xb80bb80b,0xb80cb80c,0xb80eb80d,0xb80fb80e,
0xb810b80f,0xb811b811,0xb812b812,0xb814b813,0xb815b814,0xb816b815,0xb817b817,0xb818b818,0xb81ab819,0xb81db81b,
0xb81fb81e,0xb821b820,0xb824b823,0xb826b825,0xb829b827,0xb82bb82a,0xb82db82c,0xb830b82f,0xb832b831,0xb834b833,
0xb837b836,0xb839b838,0xb83cb83a,0xb83eb83d,0xb840b83f,0xb843b842,0xb845b844,0xb848b846,0xb84ab849,0xb84cb84b,
0xb84fb84e,0xb851b850,0xb854b852,0xb856b855,0xb858b857,0xb85bb85a,0xb85db85c,0xb860b85e,0xb862b861,0xb864b863,
0xb867b866,0xb869b868,0xb86bb86a,0xb86eb86d,0xb870b86f,0xb873b871,0xb875b874,0xb877b876,0xb87ab879,0xb87cb87b,
0xb87fb87d,0xb880b880,0xb882b881,0xb883b882,0xb884b883,0xb885b885,0xb886b886,0xb888b887,0xb889b888,0xb88ab889,
0xb88bb88b,0xb88cb88c,0xb88eb88d,0xb88fb88e,0xb890b88f,0xb891b891,0xb892b892,0xb894b893,0xb895b894,0xb896b895,
0xb897b897,0xb898b898,0xb89ab899,0xb89db89b,0xb89fb89e,0xb8a1b8a0,0xb8a4b8a3,0xb8a6b8a5,0xb8a9b8a7,0xb8abb8aa,
0xb8adb8ac,0xb8b0b8af,0xb8b2b8b1,0xb8b4b8b3,0xb8b7b8b6,0xb8b9b8b8,0xb8bcb8ba,0xb8beb8bd,0xb8c0b8bf,0xb8c3b8c2,
0xb8c5b8c4,0xb8c8b8c6,0xb8cab8c9,0xb8ccb8cb,0xb8cfb8ce,0xb8d1b8d0,0xb8d4b8d2,0xb8d6b8d5,0xb8d8b8d7,0xb8dbb8da,
0xb8ddb8dc,0xb8e0b8de,0xb8e2b8e1,0xb8e4b8e3,0xb8e7b8e6,0xb8e9b8e8,0xb8ebb8ea,0xb8eeb8ed,0xb8f0b8ef,0xb8f3b8f1,
0xb8f5b8f4,0xb8f7b8f6,0xb8fab8f9,0xb8fcb8fb,0xb8ffb8fd,0xb900b900,0xb902b901,0xb903b902,0xb904b903,0xb905b905,
0xb906b906,0xb908b907,0xb909b908,0xb90ab909,0xb90bb90b,0xb90cb90c,0xb90eb90d,0xb90fb90e,0xb910b90f,0xb911b911,
0xb912b912,0xb914b913,0xb915b914,0xb916b915,0xb917b917,0xb918b918,0xb91ab919,0xb91db91b,0xb91fb91e,0xb921b920,
0xb924b923,0xb926b925,0xb929b927,0xb92bb92a,0xb92db92c,0xb930b92f,0xb932b931,0xb934b933,0xb937b936,0xb939b938,
0xb93cb93a,0xb93eb93d,0xb940b93f,0xb943b942,0xb945b944,0xb948b946,0xb94ab949,0xb94cb94b,0xb94fb94e,0xb951b950,
0xb954b952,0xb956b955,0xb958b957,0xb95bb95a,0xb95db95c,0xb960b95e,0xb962b961,0xb964b963,0xb967b966,0xb969b968,
0xb96bb96a,0xb96eb96d,0xb970b96f,0xb973b971,0xb975b974,0xb977b976,0xb97ab979,0xb97cb97b,0xb97fb97d,0xb980b980,
0xb982b981,0xb983b982,0xb984b983,0xb985b985,0xb986b986,0xb988b987,0xb989b988,0xb98ab989,0xb98bb98b,0xb98cb98c,
0xb98eb98d,0xb98fb98e,0xb990b98f,0xb991b991,0xb992b992,0xb994b993,0xb995b994,0xb996b995,0xb997b997,0xb998b998,
0xb99ab999,0xb99db99b,0xb99fb99e,0xb9a1b9a0,0xb9a4b9a3,0xb9a6b9a5,0xb9a9b9a7,0xb9abb9aa,0xb9adb9ac,0xb9b0b9af,
0xb9b2b9b1,0xb9b4b9b3,0xb9b7b9b6,0xb9b9b9b8,0xb9bcb9ba,0xb9beb9bd,0xb9c0b9bf,0xb9c3b9c2,0xb9c5b9c4,0xb9c8b9c6,
0xb9cab9c9,0xb9ccb9cb,0xb9cfb9ce,0xb9d1b9d0,0xb9d4b9d2,0xb9d6b9d5,0xb9d8b9d7,0xb9dbb9da,0xb9ddb9dc,0xb9e0b9de,
0xb9e2b9e1,0xb9e4b9e3,0xb9e7b9e6,0xb9e9b9e8,0xb9ebb9ea,0xb9eeb9ed,0xb9f0b9ef,0xb9f3b9f1,0xb9f5b9f4,0xb9f7b9f6,
0xb9fab9f9,0xb9fcb9fb,0xb9ffb9fd,0xba00ba00,0xba02ba01,0xba03ba02,0xba04ba03,0xba05ba05,0xba06ba06,0xba08ba07,
0xba09ba08,0xba0aba09,0xba0bba0b,0xba0cba0c,0xba0eba0d,0xba0fba0e,0xba10ba0f,0xba11ba11,0xba12ba12,0xba14ba13,
0xba15ba14,0xba16ba15,0xba17ba17,0xba18ba18,0xba1aba19,0xba1dba1b,0xba1fba1e,0xba21ba20,0xba24ba23,0xba26ba25,
0xba29ba27,0xba2bba2a,0xba2dba2c,0xba30ba2f,0xba32ba31,0xba34ba33,0xba37ba36,0xba39ba38,0xba3cba3a,0xba3eba3d,
0xba40ba3f,0xba43ba42,0xba45ba44,0xba48ba46,0xba4aba49,0xba4cba4b,0xba4fba4e,0xba51ba50,0xba54ba52,0xba56ba55,
0xba58ba57,0xba5bba5a,0xba5dba5c,0xba60ba5e,0xba62ba61,0xba64ba63,0xba67ba66,0xba69ba68,0xba6bba6a,0xba6eba6d,
0xba70ba6f,0xba73ba71,0xba75ba74,0xba77ba76,0xba7aba79,0xba7cba7b,0xba7fba7d,0xba80ba80,0xba82ba81,0xba83ba82,
0xba84ba83,0xba85ba85,0xba86ba86,0xba88ba87,0xba89ba88,0xba8aba89,0xba8bba8b,0xba8cba8c,0xba8eba8d,0xba8fba8e,
0xba90ba8f,0xba91ba91,0xba92ba92,0xba94ba93,0xba95ba94,0xba96ba95,0xba97ba97,0xba98ba98,0xba9aba99,0xba9dba9b,
0xba9fba9e,0xbaa1baa0,0xbaa4baa3,0xbaa6baa5,0xbaa9baa7,0xbaabbaaa,0xbaadbaac,0xbab0baaf,0xbab2bab1,0xbab4bab3,
0xbab7bab6,0xbab9bab8,0xbabcbaba,0xbabebabd,0xbac0babf,0xbac3bac2,0xbac5bac4,0xbac8bac6,0xbacabac9,0xbaccbacb,
0xbacfbace,0xbad1bad0,0xbad4bad2,0xbad6bad5,0xbad8bad7,0xbadbbada,0xbaddbadc,0xbae0bade,0xbae2bae1,0xbae4bae3,
0xbae7bae6,0xbae9bae8,0xbaebbaea,0xbaeebaed,0xbaf0baef,0xbaf3baf1,0xbaf5baf4,0xbaf7baf6,0xbafabaf9,0xbafcbafb,
0xbaffbafd,0xbb00bb00,0xbb02bb01,0xbb03bb02,0xbb04bb03,0xbb05bb05,0xbb06bb06,0xbb08bb07,0xbb09bb08,0xbb0abb09,
0xbb0bbb0b,0xbb0cbb0c,0xbb0ebb0d,0xbb0fbb0e,0xbb10bb0f,0xbb11bb11,0xbb12bb12,0xbb14bb13,0xbb15bb14,0xbb16bb15,
0xbb17bb17,0xbb18bb18,0xbb1abb19,0xbb1dbb1b,0xbb1fbb1e,0xbb21bb20,0xbb24bb23,0xbb26bb25,0xbb29bb27,0xbb2bbb2a,
0xbb2dbb2c,0xbb30bb2f,0xbb32bb31,0xbb34bb33,0xbb37bb36,0xbb39bb38,0xbb3cbb3a,0xbb3ebb3d,0xbb40bb3f,0xbb43bb42,
0xbb45bb44,0xbb48bb46,0xbb4abb49,0xbb4cbb4b,0xbb4fbb4e,0xbb51bb50,0xbb54bb52,0xbb56bb55,0xbb58bb57,0xbb5bbb5a,
0xbb5dbb5c,0xbb60bb5e,0xbb62bb61,0xbb64bb63,0xbb67bb66,0xbb69bb68,0xbb6bbb6a,0xbb6ebb6d,0xbb70bb6f,0xbb73bb71,
0xbb75bb74,0xbb77bb76,0xbb7abb79,0xbb7cbb7b,0xbb7fbb7d,0xbb80bb80,0xbb82bb81,0xbb83bb82,0xbb84bb83,0xbb85bb85,
0xbb86bb86,0xbb88bb87,0xbb89bb88,0xbb8abb89,0xbb8bbb8b,0xbb8cbb8c,0xbb8ebb8d,0xbb8fbb8e,0xbb90bb8f,0xbb91bb91,
0xbb92bb92,0xbb94bb93,0xbb95bb94,0xbb96bb95,0xbb97bb97,0xbb98bb98,0xbb9abb99,0xbb9dbb9b,0xbb9fbb9e,0xbba1bba0,
0xbba4bba3,0xbba6bba5,0xbba9bba7,0xbbabbbaa,0xbbadbbac,0xbbb0bbaf,0xbbb2bbb1,0xbbb4bbb3,0xbbb7bbb6,0xbbb9bbb8,
0xbbbcbbba,0xbbbebbbd,0xbbc0bbbf,0xbbc3bbc2,0xbbc5bbc4,0xbbc8bbc6,0xbbcabbc9,0xbbccbbcb,0xbbcfbbce,0xbbd1bbd0,
0xbbd4bbd2,0xbbd6bbd5,0xbbd8bbd7,0xbbdbbbda,0xbbddbbdc,0xbbe0bbde,0xbbe2bbe1,0xbbe4bbe3,0xbbe7bbe6,0xbbe9bbe8,
0xbbebbbea,0xbbeebbed,0xbbf0bbef,0xbbf3bbf1,0xbbf5bbf4,0xbbf7bbf6,0xbbfabbf9,0xbbfcbbfb,0xbbffbbfd,0xbc00bc00,
0xbc02bc01,0xbc03bc02,0xbc04bc03,0xbc05bc05,0xbc06bc06,0xbc07bc06,0xbc08bc07,0xbc09bc09,0xbc0abc0a,0xbc0cbc0b,
0xbc0dbc0c,0xbc0ebc0d,0xbc0fbc0e,0xbc10bc10,0xbc11bc11,0xbc13bc12,0xbc14bc13,0xbc15bc14,0xbc16bc16,0xbc17bc17,
0xbc19bc18,0xbc1cbc1a,0xbc1ebc1d,0xbc20bc1f,0xbc23bc22,0xbc25bc24,0xbc27bc26,0xbc2abc29,0xbc2cbc2b,0xbc2fbc2d,
0xbc31bc30,0xbc33bc32,0xbc36bc34,0xbc38bc37,0xbc3abc39,0xbc3dbc3c,0xbc3fbc3e,0xbc42bc40,0xbc44bc43,0xbc46bc45,
0xbc49bc48,0xbc4bbc4a,0xbc4dbc4c,0xbc50bc4f,0xbc52bc51,0xbc53bc52,0xbc56bc54,0xbc58bc57,0xbc5abc59,0xbc5dbc5b,
0xbc5fbc5e,0xbc61bc60,0xbc64bc62,0xbc66bc65,0xbc68bc67,0xbc6bbc6a,0xbc6dbc6c,0xbc6fbc6e,0xbc72bc71,0xbc74bc73,
0xbc77bc75,0xbc79bc78,0xbc7bbc7a,0xbc7ebc7c,0xbc80bc7f,0xbc81bc81,0xbc82bc82,0xbc84bc83,0xbc85bc84,0xbc86bc85,
0xbc87bc86,0xbc88bc88,0xbc89bc89,0xbc8bbc8a,0xbc8cbc8b,0xbc8dbc8c,0xbc8ebc8e,0xbc8fbc8f,0xbc91bc90,0xbc92bc91,
0xbc93bc92,0xbc94bc93,0xbc95bc95,0xbc96bc96,0xbc98bc97,0xbc9bbc99,0xbc9dbc9c,0xbc9fbc9e,0xbca2bca0,0xbca4bca3,
0xbca6bca5,0xbca9bca8,0xbcabbcaa,0xbcadbcac,0xbcb0bcaf,0xbcb2bcb1,0xbcb4bcb3,0xbcb6bcb6,0xbcb8bcb7,0xbcbabcb9,
0xbcbdbcbc,0xbcbfbcbe,0xbcc1bcc0,0xbcc4bcc3,0xbcc6bcc5,0xbcc8bcc7,0xbccbbcca,0xbccdbccc,0xbccfbcce,0xbcd2bcd1,
0xbcd4bcd3,0xbcd6bcd5,0xbcd9bcd8,0xbcdbbcda,0xbcdcbcdc,0xbcdebcdd,0xbce1bce0,0xbce3bce2,0xbce5bce4,0xbce8bce6,
0xbceabce9,0xbcecbceb,0xbcefbced,0xbcf1bcf0,0xbcf3bcf2,0xbcf6bcf4,0xbcf8bcf7,0xbcfabcf9,0xbcfdbcfb,0xbcffbcfe,
0xbd01bd00,0xbd02bd01,0xbd03bd02,0xbd04bd04,0xbd05bd05,0xbd06bd06,0xbd08bd07,0xbd09bd08,0xbd0abd09,0xbd0bbd0b,
0xbd0cbd0c,0xbd0dbd0d,0xbd0fbd0e,0xbd10bd0f,0xbd11bd10,0xbd12bd12,0xbd13bd13,0xbd14bd14,0xbd15bd14,0xbd17bd16,
0xbd1abd19,0xbd1cbd1b,0xbd1ebd1d,0xbd21bd20,0xbd23bd22,0xbd25bd24,0xbd28bd26,0xbd29bd28,0xbd2bbd2a,0xbd2dbd2c,
0xbd30bd2f,0xbd32bd31,0xbd34bd33,0xbd37bd35,0xbd39bd38,0xbd3bbd3a,0xbd3dbd3c,0xbd40bd3f,0xbd42bd41,0xbd44bd43,
0xbd47bd46,0xbd49bd48,0xbd4bbd4a,0xbd4ebd4c,0xbd4ebd4d,0xbd51bd50,0xbd53bd52,0xbd55bd54,0xbd58bd56,0xbd5abd59,
0xbd5cbd5b,0xbd5ebd5d,0xbd61bd60,0xbd61bd60,0xbd64bd63,0xbd66bd65,0xbd68bd67,0xbd6abd69,0xbd6dbd6c,0xbd6fbd6e,
0xbd71bd70,0xbd74bd72,0xbd76bd75,0xbd78bd77,0xbd7abd79,0xbd7dbd7b,0xbd7fbd7e,0xbd81bd80,0xbd82bd81,0xbd83bd82,
0xbd83bd83,0xbd84bd84,0xbd85bd85,0xbd86bd86,0xbd88bd87,0xbd89bd88,0xbd8abd89,0xbd8bbd8a,0xbd8cbd8c,0xbd8cbd8d,
0xbd8dbd8d,0xbd8ebd8e,0xbd90bd8f,0xbd92bd91,0xbd95bd93,0xbd97bd96,0xbd99bd98,0xbd9bbd9a,0xbd9cbd9b,0xbd9fbd9e,
0xbda1bda0,0xbda3bda2,0xbda4bda4,0xbda6bda5,0xbda9bda7,0xbdabbdaa,0xbdadbdac,0xbdafbdae,0xbdb1bdb0,0xbdb4bdb2,
0xbdb6bdb5,0xbdb7bdb7,0xbdb9bdb8,0xbdbbbdba,0xbdbdbdbc,0xbdbfbdbe,0xbdc0bdc0,0xbdc2bdc1,0xbdc5bdc3,0xbdc7bdc6,
0xbdc9bdc8,0xbdcabdc9,0xbdccbdcb,0xbdcebdcd,0xbdd0bdcf,0xbdd2bdd1,0xbdd4bdd3,0xbdd7bdd5,0xbdd7bdd8,0xbdd9bdd8,
0xbddbbdda,0xbddebddc,0xbde0bddf,0xbde0bde1,0xbde2bde1,0xbde4bde3,0xbde7bde6,0xbde9bde8,0xbdebbdea,0xbdedbdec,
0xbdefbdee,0xbdf1bdf0,0xbdf3bdf2,0xbdf4bdf4,0xbdf6bdf5,0xbdf8bdf7,0xbdf8bdf7,0xbdfabdf9,0xbdfcbdfb,0xbdfebdfd,
0xbe00bdff,0xbe00be00,0xbe01be01,0xbe02be02,0xbe03be03,0xbe04be04,0xbe06be05,0xbe08be07,0xbe09be09,0xbe0bbe0a,
0xbe0dbe0c,0xbe0ebe0e,0xbe10be0f,0xbe12be11,0xbe14be13,0xbe15be14,0xbe17be16,0xbe19be18,0xbe1abe19,0xbe1cbe1b,
0xbe1dbe1d,0xbe1fbe1e,0xbe20be1f,0xbe22be21,0xbe24be23,0xbe26be25,0xbe28be27,0xbe28be29,0xbe2abe29,0xbe2cbe2b,
0xbe2ebe2d,0xbe2ebe2f,0xbe30be2f,0xbe32be31,0xbe32be33,0xbe34be33,0xbe36be35,0xbe38be37,0xbe39be38,0xbe3abe39,
0xbe3cbe3b,0xbe3ebe3d,0xbe3fbe3e,0xbe41be40,0xbe43be42,0xbe43be42,0xbe44be44,0xbe46be45,0xbe48be47,0xbe48be49,
0xbe4abe49,0xbe4cbe4b,0xbe4dbe4d,0xbe4ebe4e,0xbe4fbe4e,0xbe4fbe50,0xbe51be50,0xbe52be51,0xbe54be53,0xbe56be55,
0xbe57be56,0xbe59be58,0xbe59be59,0xbe59be5a,0xbe5bbe5a,0xbe5dbe5c,0xbe5ebe5e,0xbe60be5f,0xbe60be5f,0xbe62be61,
0xbe63be61,0xbe65be65,0xbe68be67,0xbe69be69,0xbe6dbe6b,0xbe6fbe6d,0xbe71be70,0xbe72be71,0xbe75be74,0xbe76be76,
0xbe78be77,0xbe7abe78,0xbe7dbe7c,0xbe7ebe7d,0xbe80be7f,0xbe81be80,0xbe82be81,0xbe83be82,0xbe83be83,0xbe84be84,
0xbe85be85,0xbe86be86,0xbe86be87,0xbe87be87,0xbe88be88,0xbe89be88,0xbe8abe8a,0xbe8bbe8a,0xbe8bbe8b,0xbe8cbe8c,
0xbe8cbe8c,0xbe8ebe8d,0xbe8ebe8e,0xbe8ebe8e,0xbe8fbe8e,0xbe8fbe8f,0xbe90be90,0xbe91be90,0xbe92be92,0xbe92be92,
0xbe92be92,0xbe94be93,0xbe93be93,0xbe94be93,0xbe94be94,0xbe96be95,0xbe95be96,0xbe96be96,0xbe96be96,0xbe97be96,
0xbe96be97,0xbe98be97,0xbe98be98,0xbe98be98,0xbe99be99,0xbe99be98,0xbe99be9a,0xbe99be99,0xbe9abe99,0xbe9abe9a,
0xbe9cbe9b,0xbe9bbe9b,0xbe9bbe9b,0xbe9bbe9c,0xbe9bbe9b,0xbe9cbe9b,0xbe9cbe9c,0xbe9dbe9d,0xbe9dbe9d,0xbe9dbe9d,
0xbe9ebe9e,0xbe9ebe9e,0xbe9dbe9e,0xbe9ebe9d,0xbe9ebe9e,0xbe9ebe9e,0xbe9ebe9e,0xbe9fbe9f,0xbe9ebe9d,0xbe9ebe9e,
0xbe9ebe9e,0xbe9dbe9e,0xbe9dbe9e,0xbe9dbe9e,0xbe9ebe9d,0xbe9ebe9d,0xbe9dbe9d,0xbe9cbe9c,0xbe9bbe9b,0xbe9bbe9c,
0xbe9cbe9b,0xbe9bbe9b,0xbe9abe9b,0xbe9abe9a,0xbe9abe99,0xbe98be99,0xbe98be98,0xbe98be98,0xbe98be97,0xbe97be98,
0xbe97be97,0xbe96be96,0xbe95be95,0xbe94be95,0xbe94be94,0xbe93be94,0xbe92be92,0xbe91be91,0xbe91be90,0xbe90be90,
0xbe90be90,0xbe8fbe90,0xbe8ebe8f,0xbe8ebe8e,0xbe8cbe8d,0xbe8cbe8c,0xbe8bbe8c,0xbe8abe8b,0xbe8abe89,0xbe88be8a,
0xbe88be88,0xbe87be87,0xbe86be86,0xbe85be86,0xbe83be85,0xbe83be84,0xbe82be83,0xbe81be82,0xbe80be81,0xbe7dbe7f,
0xbe79be7c,0xbe76be78,0xbe73be74,0xbe6fbe70,0xbe6bbe6d,0xbe67be6a,0xbe65be66,0xbe60be62,0xbe5dbe5f,0xbe59be5b,
0xbe57be58,0xbe53be55,0xbe4fbe52,0xbe4cbe4d,0xbe49be49,0xbe44be47,0xbe40be43,0xbe3ebe40,0xbe3abe3c,0xbe37be39,
0xbe34be34,0xbe30be32,0xbe2dbe2f,0xbe2abe2b,0xbe26be28,0xbe24be24,0xbe20be22,0xbe1cbe1e,0xbe1abe1b,0xbe17be18,
0xbe14be15,0xbe11be12,0xbe0ebe10,0xbe0bbe0d,0xbe08be0a,0xbe05be06,0xbe03be04,0xbe00be02,0xbdfabdfe,0xbdf6bdf8,
0xbdf0bdf2,0xbdebbded,0xbde7bde9,0xbde2bde4,0xbdddbddf,0xbdd7bdda,0xbdd3bdd5,0xbdcfbdd1,0xbdcabdcc,0xbdc6bdc7,
0xbdc1bdc3,0xbdbcbdbf,0xbdb9bdba,0xbdb5bdb7,0xbdb1bdb3,0xbdadbdaf,0xbda8bdab,0xbda5bda6,0xbda0bda3,0xbd9dbd9f,
0xbd9abd9c,0xbd96bd99,0xbd91bd95,0xbd8bbd8e,0xbd84bd87,0xbd7cbd81,0xbd71bd76,0xbd66bd6c,0xbd5bbd60,0xbd51bd56,
0xbd46bd4c,0xbd3dbd42,0xbd34bd39,0xbd2bbd30,0xbd23bd27,0xbd1bbd1f,0xbd14bd18,0xbd0dbd10,0xbd06bd0a,0xbcffbd02,
0xbcf2bcf9,0xbce7bcec,0xbcdcbce2,0xbcd2bcd7,0xbcc7bccc,0xbcbdbcc2,0xbcb4bcb8,0xbcabbcaf,0xbca2bca6,0xbc9abc9e,
0xbc92bc96,0xbc8bbc8e,0xbc83bc87,0xbc7bbc80,0xbc6dbc73,0xbc61bc68,0xbc56bc5b,0xbc4cbc51,0xbc40bc46,0xbc36bc3c,
0xbc2dbc32,0xbc24bc29,0xbc1cbc20,0xbc14bc18,0xbc0dbc10,0xbc05bc08,0xbbfdbc01,0xbbf0bbf5,0xbbe2bbe8,0xbbd7bbdc,
0xbbccbbd1,0xbbc1bbc7,0xbbb6bbbc,0xbbadbbb2,0xbba4bba9,0xbb9cbba0,0xbb93bb97,0xbb8cbb8f,0xbb85bb88,0xbb7bbb81,
0xbb6dbb74,0xbb61bb67,0xbb55bb5b,0xbb4abb4f,0xbb3fbb44,0xbb34bb3a,0xbb26bb30,0xbb15bb1d,0xbb06bb0d,0xbaefbafd,
0xbad6bae2,0xbac0bacb,0xbaacbab6,0xba9abaa3,0xba8aba91,0xba77ba82,0xba5cba6a,0xba45ba51,0xba30ba3a,0xba1dba27,
0xba0dba15,0xb9fcba05,0xb9e1b9ee,0xb9c9b9d5,0xb9b3b9be,0xb9a0b9aa,0xb98fb998,0xb97fb987,0xb964b972,0xb94cb958,
0xb937b940,0xb922b92c,0xb911b919,0xb901b909,0xb8e7b8f4,0xb8ceb8da,0xb8b8b8c2,0xb8a3b8ae,0xb892b89a,0xb882b88a,
0xb868b877,0xb84fb85c,0xb839b843,0xb824b82e,0xb813b81b,0xb802b80a,0xb7e8b7f7,0xb7d0b7dc,0xb7b9b7c3,0xb7a5b7af,
0xb793b79b,0xb783b78a,0xb769b776,0xb74fb75b,0xb739b743,0xb724b72e,0xb712b71b,0xb702b709,0xb6e7b6f5,0xb6ceb6da,
0xb6b7b6c3,0xb6a3b6ad,0xb691b69a,0xb681b689,0xb666b674,0xb64db659,0xb636b641,0xb622b62c,0xb610b619,0xb600b608,
0xb5d7b5f2,0xb5aab5bf,0xb586b598,0xb554b56f,0xb528b53d,0xb504b515,0xb4d1b4ec,0xb4a5b4ba,0xb483b493,0xb44fb469,
0xb423b438,0xb401b411,0xb3cbb3e5,0xb3a0b3b4,0xb37db38f,0xb348b361,0xb31db332,0xb2f8b30c,0xb2c4b2dc,0xb29ab2ae,
0xb273b288,0xb23fb257,0xb217b22a,0xb1eeb206,0xb1bbb1d2,0xb194b1a6,0xb168b183,0xb137b14e,0xb110b122,0xb0e2b100,
0xb0b3b0c9,0xb08cb09e,0xb05eb079,0xb02eb044,0xb009b01a,0xafd7aff3,0xafaaafbf,0xaf85af96,0xaf52af6c,0xaf25af3a,
0xaf02af12,0xaeccaee6,0xaea0aeb5,0xae7cae8e,0xae46ae5f,0xae1cae2f,0xadf5ae0a,0xadc1add9,0xad97adab,0xad6ead86,
0xad3aad52,0xad12ad25,0xace6ad02,0xacb5accc,0xac8eaca0,0xac5fac7b,0xac2fac46,0xac0aac1b,0xabd8abf4,0xabaaabbf,
0xab85ab96,0xab51ab6b,0xab24ab39,0xab00ab11,0xaab3aae4,0xaa5daa8c,0xaa08aa2d,0xa9a8a9d6,0xa94ea983,0xa8fda922,
0xa89ca8c7,0xa840a874,0xa7eca817,0xa791a7b9,0xa732a764,0xa6dba70c,0xa686a6ac,0xa626a654,0xa5cca602,0xa57aa59f,
0xa519a544,0xa4bca4f0,0xa468a494,0xa40ea436,0xa3aea3df,0xa356a389,0xa303a328,0xa2a1a2cd,0xa246a27d,0xa1f3a21b,
0xa195a1be,0xa136a169,0xa0e0a10f,0xa089a0af,0xa028a056,0x9fcea004,0x9f7d9fa1,0x9f1b9f47,0x9ebe9ef2,0x9e699e94,
0x9e0f9e36,0x9dae9ddf,0x9d579d89,0x9d039d27,0x9ca19ccd,0x9c449c7b,0x9bf19c1a,0x9b939bbc,0x9b359b67,0x9add9b0d,
0x9a889aad,0x9a269a54,0x99ca9a02,0x9978999e,0x99189942,0x98ba98ed,0x98649891,0x980b9831,0x97aa97da,0x97509785,
0x96fe9722,0x969c96c7,0x963e9673,0x95e99615,0x958e95b6,0x952e955f,0x94d49508,0x948294a7,0x93f9944b,0x93399398,
0x928b92e3,0x91cf922a,0x911b917d,0x906790bd,0x8fac900d,0x8f008f53,0x8e3f8e9e,0x8d8f8deb,0x8cd58d2f,0x8c1f8c83,
0x8b6e8bc3,0x8ab18b11,0x8a048a58,0x894489a0,0x889388f0,0x87da8832,0x87228785,0x867186c6,0x85b48614,0x8506855c,
0x844884a4,0x839583f3,0x82dd8336,0x82288286,0x816281ca,0x80b78108,0x805c805c,0x80008000,0x80008000,0x80008000,
0x80008000,0x80008000,0x80008000,0x80008000,0x80008000,0x80008000,0x80008000,0x80008000,0x80008000,0x80008000,
0x80008000,0x80008000,0x80008000,0x80008000,0x80008000,0x80008000,0x80008000,0x80008000,0x80008000,0x80008000,
0x80008000,0x80008000,0x80008000,0x80008000,0x80008000,0x80008000,0x80008000,0x80008000,0x80008000,0x80008000,
0x80008000,0x80008000,0x80008000,0x80008000,0x80008000,0x80008000,0x80008000,0x80008000,0x80008000,0x80008000,
0x80008000,0x80008000,0x80008000,0x80008000,0x80008000,0x80008000,0x80008000,0x80008000,0x80008000,0x80008000,
0x80008000,0x80008000,0x80008000,0x80008000,0x80008000,0x80008000,0x80008000,0x80008000,0x80008000,0x80008000,
0x80008000,0x80008000,0x80008000,0x80008000,0x80008000,0x80008000,0x80008000,0x80008000,0x80008000,0x80008000,
0x80008000,0x80008000,0x80008000,0x80008000,0x80008000,0x80008000,0x80008000,0x80008000,0x80008000,0x80008000,
0x80008000,0x80008000,0x80008000,0x80008000,0x80008000,0x80008000,0x80008000,0x80008000,0x80008000,0x80008000,
0x80008000,0x80008000,0x80008000,0x80008000,0x80008000,0x80008000,
};

template <int N, int I = 0, class Fn>
__device__ __forceinline__ void static_for(Fn&& f) {
  if constexpr (I < N) { f(std::integral_constant<int, I>{}); static_for<N, I + 1>(f); }
}

template <int CTRL>
__device__ __forceinline__ float dppf(float v) {
  return __builtin_bit_cast(float, __builtin_amdgcn_update_dpp(0, __builtin_bit_cast(int, v), CTRL, 0xF, 0xF, true));
}
__device__ __forceinline__ float rdl(float v, int l) {
  return __builtin_bit_cast(float, __builtin_amdgcn_readlane(__builtin_bit_cast(int, v), l));
}
__device__ __forceinline__ float wave_sum(float v) {
  v += dppf<0xB1>(v);
  v += dppf<0x4E>(v);
  v += dppf<0x141>(v);
  v += dppf<0x140>(v);
  return (rdl(v, 0) + rdl(v, 16)) + (rdl(v, 32) + rdl(v, 48));
}

typedef uint32_t u32x4 __attribute__((ext_vector_type(4)));
typedef uint32_t u32x2 __attribute__((ext_vector_type(2)));
typedef u32x4 u32x4_a4 __attribute__((aligned(4)));
typedef __attribute__((address_space(1))) uint32_t gu32;
#define RLX_AGENT __ATOMIC_RELAXED, __HIP_MEMORY_SCOPE_AGENT
__device__ __forceinline__ void st16wt(void* p, uint32_t a, uint32_t b, uint32_t c, uint32_t d) {
  u32x4 t = {a, b, c, d};
  asm volatile("global_store_dwordx4 %0, %1, off sc1\n\ts_nop 1" ::"v"(p), "v"(t) : "memory");
}
#define WT1 1
__device__ __forceinline__ void st16k1(void* p, uint32_t a, uint32_t b, uint32_t c, uint32_t d) {
  st16wt(p, a, b, c, d);
}

#define WTA 1
#define WTG 7
#define WTX 1
template <int WT>
__device__ __forceinline__ void st16m(void* p, uint32_t a, uint32_t b, uint32_t c, uint32_t d) {
  u32x4 t = {a, b, c, d};
  if (WT == 1) st16wt(p, a, b, c, d);
  else if (WT == 2) asm volatile("global_store_dwordx4 %0, %1, off nt\n\ts_nop 1" ::"v"(p), "v"(t) : "memory");
  else if (WT == 3) asm volatile("global_store_dwordx4 %0, %1, off sc0 sc1\n\ts_nop 1" ::"v"(p), "v"(t) : "memory");
  else if (WT == 4) asm volatile("global_store_dwordx4 %0, %1, off sc1 nt\n\ts_nop 1" ::"v"(p), "v"(t) : "memory");
  else *(u32x4*)p = t;
}
constexpr int SY_CLC = 0, SY_WORDS = 512;

constexpr int KC_ = 2688;
constexpr uint32_t WS_XNT = L_ * D_ * 2, WS_K = WS_XNT + L_ * D_ * 2, WS_W2 = WS_K + D_ * 4096 * 2,
                   WS_ACT = WS_W2 + D_ * 1792 * 2, WS_AO = WS_ACT + D_ * F_ * 2 + L_ * F_ * 2, WS_SY = WS_AO + L_ * D_ * 2 + 16384;
template <int AUX>
__device__ __forceinline__ bf16x8 bld(__amdgpu_buffer_rsrc_t rs, uint32_t vo, uint32_t so) {
  return __builtin_bit_cast(bf16x8, __builtin_amdgcn_raw_buffer_load_b128(rs, (int)vo, (int)so, AUX));
}

typedef __attribute__((address_space(3))) const char* lds_cptr;
typedef short v4i16_t __attribute__((ext_vector_type(4)));
typedef unsigned short u16x2 __attribute__((ext_vector_type(2)));
__device__ __forceinline__ v4i16_t trd(lds_cptr p) {
  return __builtin_amdgcn_ds_read_tr16_b64_v4i16((__attribute__((address_space(3))) v4i16_t*)p);
}
__device__ __forceinline__ bf16x8 rd16(lds_cptr p) { return *(const __attribute__((address_space(3))) bf16x8*)p; }
#define SBAR() __builtin_amdgcn_sched_barrier(0)

template <int OFF>
__device__ __forceinline__ void garrive(uint32_t addr, int lane) {
  asm volatile("" ::: "memory");
  __builtin_amdgcn_s_waitcnt(0xC07F);
  asm volatile("" ::: "memory");
  if (lane == 0) asm volatile("ds_add_u32 %0, %1 offset:%2" ::"v"(addr), "v"(1u), "n"(OFF) : "memory");
}
template <int OFF>
__device__ __forceinline__ uint32_t garrive_rtn(uint32_t addr, int lane) {
  uint32_t old = 0;
  if (lane == 0) asm volatile("ds_add_rtn_u32 %0, %1, %2 offset:%3\n\ts_waitcnt lgkmcnt(0)" : "=v"(old) : "v"(addr), "v"(1u), "n"(OFF) : "memory");
  return (uint32_t)__builtin_amdgcn_readfirstlane(old);
}
template <int OFF>
__device__ __forceinline__ void gset(uint32_t addr, uint32_t v, int lane) {
  if (lane == 0) asm volatile("ds_write_b32 %0, %1 offset:%2" ::"v"(addr), "v"(v), "n"(OFF) : "memory");
}
template <int OFF, int SL = 1>
__device__ __forceinline__ uint32_t gwait(uint32_t addr, uint32_t target) {
  while (true) {
    uint32_t c;
    asm volatile("ds_read_b32 %0, %1 offset:%2\n\ts_waitcnt lgkmcnt(0)" : "=v"(c) : "v"(addr), "n"(OFF) : "memory");
    c = (uint32_t)__builtin_amdgcn_readfirstlane(c);
    if (c >= target) return c;
    __builtin_amdgcn_s_sleep(SL);
  }
}
template <int OFF>
__device__ __forceinline__ void gpoll(uint32_t& c, uint32_t addr) {
  uint32_t t;
  asm volatile("v_readfirstlane_b32 %1, %0\n\ts_cmp_gt_u32 %1, 1\n\ts_cbranch_scc1 1f\n0:\n\ts_sleep 1\n\t"
               "ds_read_b32 %0, %2 offset:%3\n\ts_waitcnt lgkmcnt(0)\n\tv_readfirstlane_b32 %1, %0\n\ts_cmp_lt_u32 %1, 2\n\t"
               "s_cbranch_scc1 0b\n1:" : "+v"(c), "=&s"(t) : "v"(addr), "n"(OFF) : "scc", "memory");
}
template <int MASK, class Hook>
__device__ __forceinline__ void conv_mfma(f32x4 (&acc)[8], lds_cptr ka, lds_cptr ub, Hook&& hook) {
  constexpr int T = (MASK & 0x80) ? 7 : (MASK & 0x40) ? 6 : (MASK & 0x20) ? 5 : 4;
  bf16x8 ah[2][8], al[2][8];
  v4i16_t bx[2][8], by[2][8];
#define CAT(x, y) __builtin_shufflevector(x, y, 0, 1, 2, 3, 4, 5, 6, 7)
  asm volatile("; conv %0" ::"n"(MASK) : "memory");
  bx[0][0] = trd(ub + 8); by[0][0] = trd(ub);
#pragma unroll
  for (int b = 0; b <= T; ++b) { ah[0][b] = rd16(ka + 256 + 512 * b); al[0][b] = rd16(ka + 256 + 512 * b + 4608); }
#pragma unroll
  for (int a = 1; a <= T; ++a) { bx[0][a] = trd(ub + 512 * a + 8); by[0][a] = trd(ub + 512 * a); }
  SBAR();
  static_for<2>([&](auto RR) {
    constexpr int rr = decltype(RR)::value;
    static_for<T + 1>([&](auto AA) {
      constexpr int a = decltype(AA)::value;
      const bf16x8 bf = CAT(bx[rr][a], by[rr][a]);
      int cnt = 0, pend = -1;
#pragma unroll
      for (int bb = 0; bb <= T - a; ++bb) {
        const int b = (rr == 1 && a == 0) ? T - bb : bb;
        if (!((MASK >> (a + b)) & 1)) continue;
        acc[a + b] = __builtin_amdgcn_mfma_f32_16x16x32_bf16(bf, ah[rr][b], acc[a + b], 0, 0, 0);
        if (cnt == 0 && rr == 0) { bx[1][a] = trd(ub + 256 + 512 * a + 8); by[1][a] = trd(ub + 256 + 512 * a); }
        if (rr == 1 && cnt == (a == 0 ? (MASK == 0xFF ? 2 : 1) : 0)) hook(AA, std::integral_constant<int, MASK>{});
        SBAR();
        if (pend >= 0) {
          acc[a + pend] = __builtin_amdgcn_mfma_f32_16x16x32_bf16(bf, al[rr][pend], acc[a + pend], 0, 0, 0);
          SBAR();
        }
        pend = b; ++cnt;
      }
      acc[a + pend] = __builtin_amdgcn_mfma_f32_16x16x32_bf16(bf, al[rr][pend], acc[a + pend], 0, 0, 0);
      if (rr == 0) { ah[1][T - a] = rd16(ka + 512 * (T - a)); al[1][T - a] = rd16(ka + 512 * (T - a) + 4608); }
      SBAR();
    });
  });
#undef CAT
}

constexpr int K1_ONE = 192;
constexpr int K1_PRM = 1104;
constexpr int K1_CS = 7264;
constexpr int K1_P0 = 50944, K1_P1 = 82432, K1_P2 = 8784;
constexpr int K1_W2 = 8784;
constexpr int K1_ST = 60160;
constexpr int K1_B = K1_ST + 1024;
constexpr int K1_W = 100096, K1_WS = 15360;
constexpr int K1_X0 = 34560, K1_X1 = 66048, K1_X2 = K1_W + 3 * K1_WS;
constexpr int K1_LDS = K1_X2 + 16384;

__device__ __forceinline__ float fmul(float a, float b) { float r; asm("v_mul_f32 %0, %1, %2" : "=v"(r) : "v"(a), "v"(b)); return r; }
__device__ __forceinline__ float fsub(float a, float b) { float r; asm("v_sub_f32 %0, %1, %2" : "=v"(r) : "v"(a), "v"(b)); return r; }
__device__ __forceinline__ float fadd(float a, float b) { float r; asm("v_add_f32 %0, %1, %2" : "=v"(r) : "v"(a), "v"(b)); return r; }
__device__ __forceinline__ float fnma(float a, float b, float c) { float r; asm("v_fma_f32 %0, %1, %2, -%3" : "=v"(r) : "v"(a), "v"(b), "v"(c)); return r; }
__device__ __forceinline__ float ffma(float a, float b, float c) { float r; asm("v_fma_f32 %0, %1, %2, %3" : "=v"(r) : "v"(a), "v"(b), "v"(c)); return r; }
__device__ __forceinline__ f32x4 add4(const f32x4& a, const f32x4& b, const f32x4& c, const f32x4& d) {
  f32x4 r;
#pragma unroll
  for (int i = 0; i < 4; ++i) r[i] = fadd(fadd(a[i], b[i]), fadd(c[i], d[i]));
  return r;
}
__device__ __forceinline__ void cexp(const f32x4& P, float p, float& re, float& im) {
  const float m = __builtin_amdgcn_exp2f(fmul(P[0], p));
  const float rv = __builtin_amdgcn_fractf(fmul(P[1], p));
  re = fmul(m, __builtin_amdgcn_cosf(rv));
  im = fmul(m, __builtin_amdgcn_sinf(rv));
}
__device__ __forceinline__ void split2(float a, float b, uint32_t& h, uint32_t& l) {
  h = pack2(a, b);
  l = pack2(fsub(a, bflo(h)), fsub(b, bfhi(h)));
}
__device__ __forceinline__ uint32_t duplo(uint32_t v) { return __builtin_amdgcn_perm(v, v, 0x01000100u); }
__device__ __forceinline__ uint32_t duphi(uint32_t v) { return __builtin_amdgcn_perm(v, v, 0x03020302u); }

#define CODE_BASE(a, sym) asm volatile("s_getpc_b64 vcc\n\ts_add_u32 vcc_lo, vcc_lo, " sym "@rel32@lo+4\n\ts_addc_u32 vcc_hi, vcc_hi, " sym "@rel32@hi+12\n\ts_mov_b64 %0, vcc" : "=s"(a) :: "vcc", "scc")
#define K1_SYM "_ZN12_GLOBAL__N_17k1_mainEPKtS1_S1_S1_S1_S1_S1_S1_S1_Pc"
#define K2_SYM "_ZN12_GLOBAL__N_17k2_mainEPKtS1_PcPt"
constexpr uint32_t K1_CODE = 39040, K2_CODE = 17408;
__device__ __forceinline__ void code_touch(uint64_t a, uint32_t q, uint32_t lim, int lane, uint32_t& t0, uint32_t& t1) {
  const uint32_t o = min(q * 8192u + (uint32_t)lane * 128u, lim - 128u);
  typedef const __attribute__((address_space(1))) uint32_t* gptr;
  gptr p = (gptr)(a + o);
  t0 = p[0]; t1 = p[16];
}

#define CODE_DMA(sym, off, lds) asm volatile("s_getpc_b64 vcc\n\ts_add_u32 vcc_lo, vcc_lo, " sym "@rel32@lo+4\n\ts_addc_u32 vcc_hi, vcc_hi, " sym "@rel32@hi+12\n\ts_mov_b32 m0, %1\n\ts_nop 0\n\tglobal_load_lds_dword %0, vcc" :: "v"(off), "s"(lds) : "vcc", "scc", "m0", "memory")
#define ENT 3
#define NOCP1
#define NOCP3
#define SP1 2

__device__ __forceinline__ void k1_body(
    const u16* __restrict__ x, const u16* __restrict__ logAr, const u16* __restrict__ Aim,
    const u16* __restrict__ Cre, const u16* __restrict__ Cim, const u16* __restrict__ logdt,
    const u16* __restrict__ w1, const u16* __restrict__ b1, const u16* __restrict__ w2, char* __restrict__ ws) {
  __shared__ __attribute__((aligned(16))) char smem[K1_LDS + 256];
  const int tid = threadIdx.x, lane = tid & 63;
  const int wave = __builtin_amdgcn_readfirstlane(tid >> 6);
  const int bid = blockIdx.x;
  const uint32_t sbase = (uint32_t)(size_t)(lds_cptr)smem;
  {
    constexpr int NL = 52 / SP1;
    const uint32_t o = min((uint32_t)((((bid >> 3) % SP1) * 12 + wave) * NL + min(lane, NL - 1)) * 64u, K1_CODE - 64u);
    CODE_DMA(K1_SYM, o, sbase + K1_LDS);
  }
  constexpr int cF = 16, cE = 64, cS = 104, cCs = 112, cP = 116, cC = 120, cW = 124;
  const int xc = bid & 7, jj = bid >> 3;
  const int tm = (xc & 3) * 4 + (jj & 3), tn = (xc >> 2) * 8 + (jj >> 2);
  const int ch0 = 5 * ((bid >> 7) * 8 + (tn & 3) + 4 * (tn >> 3));
  const char* gb = (const char*)w1 + tn * 216;
  const char* gx = (const char*)x + (size_t)tm * (128 * 1280);
#define ROT 0
#define PA 3
#define W2S 2
#define PAO 4
#define PW2 6
  const int rot = (ROT == 0) ? 0 : (ROT == 1) ? (jj >> 2) : (ROT == 2) ? ((jj >> 2) & 1) : (ROT == 3) ? (jj % 10) : (ROT == 4) ? ((jj & 3) * 2 + ((jj >> 2) & 1)) : ((jj >> 2) & 1) * 5;
  auto wpiece = [&](int kt, int p, uint32_t vo, int slot) {
    kt += rot; if (kt >= 10) kt -= 10;
    __builtin_amdgcn_global_load_lds((const uint32_t*)(gb + (size_t)(kt * 64 + p * 4) * (F_ * 2) + vo),
                                     (uint32_t*)(smem + slot + (p >> 1) * 1920 + (p & 1) * 896), 16, 0, 0);
  };
  auto xpiece = [&](int kt, int p, uint32_t vo, int slot) {
    kt += rot; if (kt >= 10) kt -= 10;
    __builtin_amdgcn_global_load_lds((const uint32_t*)(gx + kt * 128 + p * 10240 + vo), (uint32_t*)(smem + slot + p * 1024), 16, 0, 0);
  };
  auto wvo = [&](int ln) -> uint32_t { const int r = ln / 14, c = ln - r * 14; return (uint32_t)(r * (F_ * 2) + c * 16 - (c == 13 ? 8 : 0)); };
  auto xvo = [&](int ln) -> uint32_t { return (uint32_t)((ln >> 3) * 1280 + (((ln & 7) ^ (ln >> 3)) * 16)); };
  if (wave >= 4 && wave < 8) {
    const int cw = wave - 4;
    STAMP1(16)
    uint32_t pl[2][5];
    {
      const int d0 = bid + 256 * (lane >> 5), i0 = d0 * NS_ + cw * 32 + (lane & 31);
      pl[0][0] = logAr[i0]; pl[0][1] = Aim[i0]; pl[0][2] = Cre[i0]; pl[0][3] = Cim[i0]; pl[0][4] = logdt[d0];
      const int d1 = 512 + (bid & 127), i1 = d1 * NS_ + cw * 32 + (lane & 31);
      pl[1][0] = logAr[i1]; pl[1][1] = Aim[i1]; pl[1][2] = Cre[i1]; pl[1][3] = Cim[i1]; pl[1][4] = logdt[d1];
    }
    __builtin_amdgcn_s_barrier();
    STAMP1(17)
    char* prm = smem + K1_PRM + cw * 1536;
#pragma unroll
    for (int t = 0; t < 2; ++t) {
      const float dt = bfr(expf(bf2f((u16)pl[t][4])));
      const float ar = -bfr(expf(bf2f((u16)pl[t][0]))) * dt;
      const float ai = bf2f((u16)pl[t][1]) * dt;
      const float cr = bf2f((u16)pl[t][2]) * dt;
      const float ci = bf2f((u16)pl[t][3]) * dt;
      if (t == 0 || lane < 32) *(f32x4*)(prm + t * 1024 + lane * 16) = (f32x4){ar * LOG2E, ai * INV2PI, 2.f * cr, -2.f * ci};
    }
    STAMP1(18)
    STAMP1(19)
    const int r = lane & 15, kq = lane >> 4;
    const float p0 = (float)r;
    const float p1 = (r < 8) ? (float)(256 + 2 * r) : (float)(512 + 4 * ((r - 8) & 3) + 256 * ((r - 8) >> 2));
    const float p2 = (r < 8) ? (float)(1024 + 8 * (r & 1) + 256 * (r >> 1)) : 2048.f;
    const float pb = (float)(16 * r);
    const int nch = (bid < 128) ? 3 : 2;
    f32x4 acc[3];
#pragma unroll 1
    for (int cc = 0; cc < nch; ++cc) {
      int c = cc;
      if (cw < nch) { c += cw + 1; if (c >= nch) c -= nch; }
#pragma unroll
      for (int t = 0; t < 3; ++t) acc[t] = (f32x4){0.f, 0.f, 0.f, 0.f};
#pragma unroll 1
      for (int it = 0; it < 2; ++it) {
        u32x4 ah[3], al[3], bh, bl;
#pragma unroll
        for (int m = 0; m < 4; ++m) {
          const f32x4 P = *(const f32x4*)(prm + c * 512 + it * 256 + kq * 64 + m * 16);
          float fr_, fi_, er, ei;
          cexp(P, pb, fr_, fi_);
          const float br = ffma(P[2], fr_, fmul(P[3], fi_));
          const float bi = fnma(P[3], fr_, fmul(P[2], fi_));
          uint32_t h, l;
          split2(br, bi, h, l); bh[m] = h; bl[m] = l;
          cexp(P, p0, er, ei); split2(er, ei, h, l); ah[0][m] = h; al[0][m] = l;
          cexp(P, p1, er, ei); split2(er, ei, h, l); ah[1][m] = h; al[1][m] = l;
          cexp(P, p2, er, ei); split2(er, ei, h, l); ah[2][m] = h; al[2][m] = l;
        }
        const bf16x8 fbh = __builtin_bit_cast(bf16x8, bh), fbl = __builtin_bit_cast(bf16x8, bl);
#pragma unroll
        for (int t = 0; t < 3; ++t) acc[t] = __builtin_amdgcn_mfma_f32_16x16x32_bf16(__builtin_bit_cast(bf16x8, al[t]), fbh, acc[t], 0, 0, 0);
#pragma unroll
        for (int t = 0; t < 3; ++t) acc[t] = __builtin_amdgcn_mfma_f32_16x16x32_bf16(__builtin_bit_cast(bf16x8, ah[t]), fbl, acc[t], 0, 0, 0);
#pragma unroll
        for (int t = 0; t < 3; ++t) acc[t] = __builtin_amdgcn_mfma_f32_16x16x32_bf16(__builtin_bit_cast(bf16x8, ah[t]), fbh, acc[t], 0, 0, 0);
      }
      if (c != cw) {
        char* pt = smem + ((c == 0) ? K1_P0 : (c == 1) ? K1_P1 : K1_P2) + (cw > c ? cw - 1 : cw) * 3072 + lane * 16;
#pragma unroll
        for (int t = 0; t < 3; ++t) *(f32x4*)(pt + t * 1024) = acc[t];
      }
    }
    garrive<cP>(sbase, lane);
    STAMP1(20)
    if (cw < nch) {
      const int d = (cw == 0) ? bid : (cw == 1) ? bid + 256 : bid + 512;
      char* pt = smem + ((cw == 0) ? K1_P0 : (cw == 1) ? K1_P1 : K1_P2);
      gwait<cP>(sbase, 4);
      STAMP1(21)
      f32x4 s[3];
#pragma unroll
      for (int t = 0; t < 3; ++t) {
        const char* ps = pt + t * 1024 + lane * 16;
        s[t] = add4(acc[t], *(const f32x4*)(ps), *(const f32x4*)(ps + 3072), *(const f32x4*)(ps + 6144));
      }
      asm volatile("" ::: "memory");
      float* kp = (float*)pt;
      *(f32x4*)(kp + 16 * r + 4 * kq) = s[0];
      *(f32x4*)(kp + ((kq < 2) ? 256 + 8 * r + 4 * kq : 256 + 64 * kq + 4 * r)) = s[1];
      if (kq < 2) {
        *(f32x2*)(kp + 512 + 64 * kq + 2 * r) = (f32x2){s[2][0], s[2][1]};
        *(f32x2*)(kp + 544 + 64 * kq + 2 * r) = (f32x2){s[2][2], s[2][3]};
      }
      if (lane == 32) kp[640] = s[2][0];
      char* dst = ws + WS_K + (size_t)d * KC_ + lane * 16;
#pragma unroll
      for (int t = 0; t < 3; ++t) {
        const f32x4 f = *(const f32x4*)(kp + 256 * t + 4 * lane);
        uint32_t h0, l0_, h1, l1;
        split2(f[0], f[1], h0, l0_);
        split2(f[2], f[3], h1, l1);
        if (t < 2 || lane < 40)
          st16k1(dst + 1024 * t, __builtin_amdgcn_perm(l0_, h0, 0x05040100u), __builtin_amdgcn_perm(l0_, h0, 0x07060302u),
                 __builtin_amdgcn_perm(l1, h1, 0x05040100u), __builtin_amdgcn_perm(l1, h1, 0x07060302u));
      }
    }
    else if (bid >= 128) {
      const int hw = cw - 2, q = bid & 127, t2 = q >> 3, n2 = q & 7;
      gwait<cW, 2>(sbase, 2);
      const int rr = (lane >> 2) & 3, c4 = lane & 3;
      const int ub = 7 * (t2 >> 1) + 3 * (t2 & 1), us = 7 * (t2 >> 1) + 6;
      lds_cptr bf_ = (lds_cptr)(smem + K1_W2 + (4 * kq + rr) * 160 + c4 * 8);
      lds_cptr bh_ = (lds_cptr)(smem + K1_W2 + (96 + 4 * (kq & 1) + rr) * 160 + c4 * 8);
      char* wd = ws + WS_W2 + (size_t)n2 * 56 * 5120 + lane * 16;
#pragma unroll
      for (int i = 0; i < 10; ++i) {
        const int jb = hw + 2 * i;
        if (jb < 15) {
          const int p = jb / 5, r16 = jb - p * 5;
          const u32x2 a = __builtin_bit_cast(u32x2, trd(bf_ + p * 5120 + r16 * 32));
          const u32x2 b = __builtin_bit_cast(u32x2, trd(bf_ + p * 5120 + 2560 + r16 * 32));
          st16k1(wd + ((ub + p) * 5 + r16) * 1024, a.x, a.y, b.x, b.y);
        } else {
          const int r16 = jb - 15;
          const u32x2 a = __builtin_bit_cast(u32x2, trd(bh_ + r16 * 32));
          const u32x2 b = __builtin_bit_cast(u32x2, trd(bh_ + 640 + r16 * 32));
          if (lane < 32) st16k1(wd + (us * 5 + r16) * 1024 + (t2 & 1) * 512, (kq & 1) ? 0u : a.x, (kq & 1) ? 0u : a.y, b.x, b.y);
        }
        if (PW2) __builtin_amdgcn_s_sleep(PW2);
      }
    }
    if (cw == 0) { uint32_t ct0, ct1; uint64_t ca; CODE_BASE(ca, K2_SYM); code_touch(ca, (uint32_t)jj % 3u, K2_CODE, lane, ct0, ct1); asm volatile("" :: "v"(ct0), "v"(ct1)); }
    STAMP1(22)
    STAMP_DUMP1(16, 24)
    return;
  }
  if (wave >= 10) {
    const int hw = wave - 10, kq = lane >> 4;
    __builtin_amdgcn_s_barrier();
    __builtin_amdgcn_s_setprio(PA);
#define AOE 1
    auto prefill = [&]() {
#pragma unroll
      for (int i = 0; i < 5; ++i) {
        st16k1(ws + WS_AO + (size_t)bid * 10240 + (hw * 5 + i) * 1024 + lane * 16, ~0u, ~0u, ~0u, ~0u);
        if (PAO) __builtin_amdgcn_s_sleep(PAO);
      }
      if (bid == 0) {
#pragma unroll
        for (int i = 0; i < SY_WORDS / 512; ++i) ((uint4*)(ws + WS_SY))[(hw * (SY_WORDS / 512) + i) * 64 + lane] = make_uint4(0, 0, 0, 0);
      }
    };
    if (AOE == 1) gwait<cF, 4>(sbase, 4);
    if (AOE < 2) prefill();
    if (bid >= 128) {
      const int q = bid & 127, t2 = q >> 3, n2 = q & 7;
      gwait<cF + 4 * W2S, 4>(sbase, 2);
#pragma unroll
      for (int i = 0; i < 9; ++i) {
        const int idx = (hw + 2 * i) * 64 + lane;
        const int r = idx / 10, c = idx - r * 10;
        if (i < 8 || (hw == 0 && lane < 56))
          __builtin_amdgcn_global_load_lds((const uint32_t*)(w2 + (size_t)(t2 * 108 + r) * D_ + n2 * 80 + c * 8),
                                           (uint32_t*)(smem + K1_W2 + (hw + 2 * i) * 1024), 16, 0, 0);
      }
      asm volatile("s_waitcnt vmcnt(0)" ::: "memory");
      garrive<cW>(sbase, lane);
    }
    u32x2 v[5][2];
    {
      const int j = (lane >> 2) & 3, p = lane & 3;
      const uint32_t rb = (uint32_t)((64 * hw + 16 * kq + 8 * (p >> 1) + j) * 128 + (p & 1) * 8);
      int n = 0;
#pragma unroll
      for (int i = 0; i < 5; ++i) {
        const int g = ch0 + i, st = g >> 3, c = g & 7;
        if (i == 0 || c == 0) {
          if (i) { garrive<cC>(sbase, lane); n = 1; }
          gwait<cF, 2>(sbase + st * 4, st ? 2 : 4);
        }
        const int s3 = st % 3;
        lds_cptr xs = (lds_cptr)smem + ((s3 == 0) ? K1_X0 : (s3 == 1) ? K1_X1 : K1_X2) + rb;
        v[i][0] = __builtin_bit_cast(u32x2, trd(xs + ((c ^ j) << 4)));
        v[i][1] = __builtin_bit_cast(u32x2, trd(xs + 512 + ((c ^ j ^ 4) << 4)));
      }
      garrive<cC>(sbase, lane);
      if (!n) garrive<cC>(sbase, lane);
    }
    __builtin_amdgcn_s_setprio(0);
    if (hw == 0) {
      if (lane < 14)
        __builtin_amdgcn_global_load_lds((const uint32_t*)((const char*)b1 + tn * 216 + lane * 16 - (lane == 13 ? 8 : 0)), (uint32_t*)(smem + K1_B), 16, 0, 0);
      asm volatile("s_waitcnt vmcnt(0)" ::: "memory");
      garrive<cCs>(sbase, lane);
    }
    if (AOE == 2) prefill();
    gwait<cS, 4>(sbase, 4);
    const char* sp = smem + K1_ST + (64 * hw + 16 * kq + (lane & 8)) * 4;
    f32x4 mb[2], rc[2];
#pragma unroll
    for (int t = 0; t < 2; ++t) { mb[t] = *(const f32x4*)(sp + t * 16); rc[t] = *(const f32x4*)(sp + 512 + t * 16); }
    char* tb = smem + K1_W + hw * 5760;
#pragma unroll
    for (int i = 0; i < 5; ++i) {
      u32x4 o;
#pragma unroll
      for (int k = 0; k < 4; ++k) {
        const uint32_t w = v[i][k >> 1][k & 1];
        const uint32_t c2 = pack2(bflo(w) - mb[k >> 1][2 * (k & 1)], bfhi(w) - mb[k >> 1][2 * (k & 1) + 1]);
        o[k] = pack2(bflo(c2) * rc[k >> 1][2 * (k & 1)], bfhi(c2) * rc[k >> 1][2 * (k & 1) + 1]);
      }
      *(u32x4*)(tb + i * 1152 + (lane & 7) * 144 + (2 * kq + ((lane >> 3) & 1)) * 16) = o;
    }
    char* xd = ws + WS_XNT + (size_t)(ch0 * 8 + (lane >> 3)) * 4096 + (tm * 128 + 64 * hw) * 2 + (lane & 7) * 16;
#pragma unroll
    for (int i = 0; i < 5; ++i) {
      const u32x4 o = *(const u32x4*)(tb + i * 1152 + (lane >> 3) * 144 + (lane & 7) * 16);
      st16m<WTX>(xd + i * 32768, o[0], o[1], o[2], o[3]);
    }
    return;
  }
  if (wave >= 8) {
    const int lw = wave - 8;
    STAMP1(8)
    __builtin_amdgcn_s_setprio(3);
    const uint32_t vo = lw ? wvo(lane) : xvo(lane);
    auto stage = [&](int kt, int sl) {
      if (lw) {
        if (lane < 56) {
#pragma unroll
          for (int p = 0; p < 16; ++p) wpiece(kt, p, vo, K1_W + sl * K1_WS);
        }
      } else {
        const int slot = (sl == 0) ? K1_X0 : (sl == 1) ? K1_X1 : K1_X2;
#pragma unroll
        for (int p = 0; p < 16; ++p) xpiece(kt, p, vo, slot);
      }
    };
    __builtin_amdgcn_s_barrier();
    STAMP1(9)
    stage(1, 1);
    stage(2, 2);
#pragma unroll
    for (int i = 0; i < 8; ++i)
      __builtin_amdgcn_global_load_lds(mish_tab + lw * 2048 + i * 256 + lane * 4, (uint32_t*)(smem + TB_LO * 2 + lw * 65536 + i * 1024), 16, 0, 0);
    STAMP1(10)
    asm volatile("s_waitcnt vmcnt(24)" ::: "memory"); garrive<cF + 4>(sbase, lane);
    const int s0 = ch0 >> 3, s1 = (ch0 + 4) >> 3;
    gwait<cE>(sbase, 4);
    if (!lw && s0 == 0) gwait<cC>(sbase, s1 == 0 ? 4 : 2);
    stage(3, 0);
    asm volatile("s_waitcnt vmcnt(24)" ::: "memory"); garrive<cF + 8>(sbase, lane);
    STAMP1(11)
    int sl = 1;
#pragma unroll 1
    for (int kt = 4; kt < 10; ++kt) {
      gwait<cE - 12>(sbase + kt * 4, 4);
      if (!lw && (kt - 3 == s0 || kt - 3 == s1)) gwait<cC>(sbase, kt - 3 == s1 ? 4 : 2);
      stage(kt, sl);
      sl = (sl == 2) ? 0 : sl + 1;
      asm volatile("s_waitcnt vmcnt(16)" ::: "memory");
      garrive<cF - 4>(sbase + kt * 4, lane);
    }
    asm volatile("s_waitcnt vmcnt(0)" ::: "memory");
    garrive<cF + 36>(sbase, lane);
    garrive<cF + 40>(sbase, lane);
    STAMP1(12)
    STAMP_DUMP1(8, 16)
    return;
  }
  STAMP1(0)
#define PG 2
#define PE 2
  f32x4 acc[7][2], G[2];
#define CAT(x, y) __builtin_shufflevector(x, y, 0, 1, 2, 3, 4, 5, 6, 7)
#define WFRAG(a, off) CAT(trd((lds_cptr)(uintptr_t)(a) + (off)), trd((lds_cptr)(uintptr_t)(a) + (off) + 896))
  auto loop = [&](auto WW) {
    constexpr int W = decltype(WW)::value;
    int ln = lane;
    asm volatile("" : "+v"(ln));
    {
      const uint32_t vx = xvo(ln), vw = wvo(ln);
#pragma unroll
      for (int p = 0; p < 4; ++p) xpiece(0, W * 4 + p, vx, K1_X0);
      if (ln < 56) {
#pragma unroll
        for (int p = 0; p < 4; ++p) wpiece(0, W * 4 + p, vw, K1_W);
      }
    }
    bf16x8 af[7], bq[2][2];
    STAMP1(5)
    if (W == 0) {
      if (ln < 28) *(uint32_t*)(smem + 16 + ln * 4) = 0;
      else if (ln < 30) *(uint32_t*)(smem + (ln - 28) * 65536) = (ln - 28) * 0x8000u;
      else if (ln < 34) *(uint2*)(smem + K1_ONE + (ln & 1) * 896 + ((ln >> 1) & 1) * 7680) = make_uint2(0x3F803F80u, 0x3F803F80u);
      asm volatile("s_waitcnt lgkmcnt(0)" ::: "memory");
    }
    __builtin_amdgcn_s_barrier();
    __builtin_amdgcn_s_setprio(PG);
    f32x4 cs[2];
#pragma unroll
    for (int i = 0; i < 7; ++i)
#pragma unroll
      for (int j = 0; j < 2; ++j) acc[i][j] = (f32x4){0.f, 0.f, 0.f, 0.f};
    cs[0] = cs[1] = G[0] = G[1] = (f32x4){0.f, 0.f, 0.f, 0.f};
    bf16x8 ones = {0x3F80, 0x3F80, 0x3F80, 0x3F80, 0x3F80, 0x3F80, 0x3F80, 0x3F80};
    uint32_t one = 1;
    asm volatile("" : "+v"(ones), "+v"(one));
    const uint32_t ab = sbase + K1_W + (uint32_t)((8 * (ln >> 4) + ((ln >> 2) & 3)) * 224 + (ln >> 4) * 128 + (ln & 3) * 8);
    const bool on = (ln & 3) == 2;
    const uint32_t xb = sbase + (uint32_t)((W * 32 + (ln & 15)) * 128 + (((ln >> 4) ^ (ln & 7)) * 16));
    asm volatile("" ::: "memory");
    __builtin_amdgcn_s_waitcnt(0x0070);
    garrive<cF>(sbase, ln);
    gwait<cF>(sbase, 4);
    STAMP1(1)
#pragma unroll
    for (int j = 0; j < 2; ++j) bq[0][j] = rd16((lds_cptr)(uintptr_t)(xb + K1_X0) + j * 2048);
#pragma unroll
    for (int i = 0; i < 6; ++i) af[i] = WFRAG(ab, i * 32);
    af[6] = af[5];
    bq[1][0] = bq[1][1] = af[5];
    uint32_t a1 = ab;
    uint32_t xs0 = K1_X0, xs1 = K1_X1, xs2 = K1_X2, ws0 = 0, ws1 = K1_WS, ws2 = 2 * K1_WS;
#pragma unroll 1
    for (int kt = 0; kt < 10; ++kt) {
      const uint32_t a0 = a1;
      const uint32_t a6 = on ? sbase + K1_ONE - 192 : a0;
      a1 = ab + ws1;
      const uint32_t x0 = (xb + xs0) ^ 64u, x1 = xb + xs1;
      { const uint32_t t = xs0; xs0 = xs1; xs1 = xs2; xs2 = t; }
      { const uint32_t t = ws0; ws0 = ws1; ws1 = ws2; ws2 = t; }
      uint32_t pc = 0;
      const uint32_t ca = sbase + kt * 4;
      asm volatile("ds_read_b32 %0, %1 offset:%2" : "+v"(pc) : "v"(ca), "n"(cF + 4) : "memory");
      SBAR();
#pragma unroll
      for (int ks = 0; ks < 2; ++ks) {
#pragma unroll
        for (int i = 0; i < 7; ++i) {
          acc[i][0] = __builtin_amdgcn_mfma_f32_16x16x32_bf16(af[i], bq[ks][0], acc[i][0], 0, 0, 0);
          SBAR();
          acc[i][1] = __builtin_amdgcn_mfma_f32_16x16x32_bf16(af[i], bq[ks][1], acc[i][1], 0, 0, 0);
          if ((i & 3) == W) cs[i >> 2] = __builtin_amdgcn_mfma_f32_16x16x32_bf16(af[i], ones, cs[i >> 2], 0, 0, 0);
          SBAR();
          if (i == 0) {
            af[6] = WFRAG(a6, ks * 7680 + 192);
            if (ks == 1) asm volatile("s_mov_b64 exec, 1\n\tds_add_u32 %0, %1 offset:%2\n\ts_mov_b64 exec, -1" ::"v"(ca), "v"(one), "n"(cE) : "memory");
          } else {
            if (ks == 1 && i == 1) gpoll<cF + 4>(pc, ca);
            af[i - 1] = ks ? WFRAG(a1, (i - 1) * 32) : WFRAG(a0, 7680 + (i - 1) * 32);
          }
          if (i == 2 || i == 3) bq[ks ^ 1][i - 2] = rd16((lds_cptr)(uintptr_t)(ks ? x1 : x0) + (i - 2) * 2048);
          if (i == 1 || i == 4) G[i >> 2] = __builtin_amdgcn_mfma_f32_16x16x32_bf16(bq[ks][i >> 2], bq[ks][i >> 2], G[i >> 2], 0, 0, 0);
          SBAR();
        }
      }
    }
    if ((ln & 15) == 0) {
      *(f32x4*)(smem + K1_CS + W * 64 + (ln >> 4) * 16) = cs[0];
      if (W < 3) *(f32x4*)(smem + K1_CS + (W + 4) * 64 + (ln >> 4) * 16) = cs[1];
    }
  };
  if (wave < 2) { if (wave == 0) loop(std::integral_constant<int, 0>{}); else loop(std::integral_constant<int, 1>{}); }
  else { if (wave == 2) loop(std::integral_constant<int, 2>{}); else loop(std::integral_constant<int, 3>{}); }
#undef WFRAG
#undef CAT
  garrive<cCs>(sbase, lane);
  STAMP1(2)
  int ln = lane;
  asm volatile("" : "+v"(ln));
  const int fr = ln & 15, fq = ln >> 4;
  float rs[2], c1[2], mm[2];
#pragma unroll
  for (int j = 0; j < 2; ++j) {
    const float g0 = (fr & 2) ? ((fr & 1) ? G[j][3] : G[j][2]) : ((fr & 1) ? G[j][1] : G[j][0]);
    const float g = __builtin_bit_cast(float, __builtin_amdgcn_ds_bpermute((fr + 16 * (fr >> 2)) * 4, __builtin_bit_cast(int, g0)));
    const float sm = __builtin_bit_cast(float, __builtin_amdgcn_ds_bpermute((fr + 32) * 4, __builtin_bit_cast(int, acc[6][j][0])));
    const float mean = sm * (1.f / D_);
    const float var = fmaxf(__builtin_fmaf(-mean, mean, g * (1.f / D_)), 0.f);
    const float sd = bfr(__builtin_amdgcn_sqrtf(bfr(bfr(var) + 1e-5f)));
    rs[j] = __builtin_amdgcn_rcpf(sd);
    mm[j] = bfr(mean);
    c1[j] = mm[j] * rs[j];
  }
  *(float*)(smem + K1_ST + (fq >> 1) * 512 + (wave * 32 + (fq & 1) * 16 + fr) * 4) = (fq & 2) ? ((fq & 1) ? rs[1] : rs[0]) : ((fq & 1) ? mm[1] : mm[0]);
  garrive<cS>(sbase, lane);
  __builtin_amdgcn_s_setprio(PE);
  gwait<cCs>(sbase, 5);
  f32x4 cv[7];
  uint2 bb[7];
#pragma unroll
  for (int i = 0; i < 7; ++i) {
    cv[i] = *(const f32x4*)(smem + K1_CS + i * 64 + fq * 16);
    bb[i] = *(const uint2*)(smem + K1_B + i * 32 + fq * 8);
  }
  STAMP1(3)
  const int ub = 7 * (tn >> 1) + 3 * (tn & 1), us = 7 * (tn >> 1) + 6;
  char* abase = ws + WS_ACT + (size_t)((tm * 2 + (wave >> 1)) * 28) * 8192 + (wave & 1) * 2048;
  u16x2 vmax = {0, 0}, vmin = {0xffff, 0xffff};
  auto epi = [&](auto slow_tag) {
    constexpr bool SLOW = decltype(slow_tag)::value;
    auto act2 = [&](uint32_t h) -> uint32_t {
      if (SLOW) {
        return pack2(bflo(h) * tanh_sp_bf(bflo(h)), bfhi(h) * tanh_sp_bf(bfhi(h)));
      } else {
        const u16x2 m = __builtin_bit_cast(u16x2, h & 0x7fff7fffu);
        vmax = __builtin_elementwise_max(vmax, m);
        vmin = __builtin_elementwise_min(vmin, (u16x2)(m - (u16x2){1, 1}));
        uint32_t alo, ahi;
        asm("v_lshlrev_b32_sdwa %0, %1, %2 dst_sel:DWORD dst_unused:UNUSED_PAD src0_sel:DWORD src1_sel:WORD_0" : "=v"(alo) : "v"(1u), "v"(h));
        asm("v_lshlrev_b32_sdwa %0, %1, %2 dst_sel:DWORD dst_unused:UNUSED_PAD src0_sel:DWORD src1_sel:WORD_1" : "=v"(ahi) : "v"(1u), "v"(h));
        const uint32_t lo = *(const u16*)(smem + alo);
        const uint32_t hi = *(const u16*)(smem + ahi);
        return lo | (hi << 16);
      }
    };
    auto pre = [&](int g, uint32_t (&h)[4]) {
#pragma unroll
      for (int k = 0; k < 4; ++k) {
        const int j = (g == 6) ? (k >> 1) : g / 3, i = (g == 6) ? 6 : 2 * (g % 3) + (k >> 1), e = k & 1;
        const float v0 = __builtin_fmaf(acc[i][j][2 * e], rs[j], -(cv[i][2 * e] * c1[j]));
        const float v1 = __builtin_fmaf(acc[i][j][2 * e + 1], rs[j], -(cv[i][2 * e + 1] * c1[j]));
        const uint32_t r = pack2(v0, v1), bias = e ? bb[i].y : bb[i].x;
        h[k] = pack2(bflo(r) + bflo(bias), bfhi(r) + bfhi(bias));
        if (i == 6 && fq == 2) h[k] = 0;
      }
    };
    uint32_t h[2][4];
    pre(0, h[0]);
#pragma unroll
    for (int g = 0; g < 7; ++g) {
      uint32_t (&o)[4] = h[g & 1];
#pragma unroll
      for (int k = 0; k < 4; ++k) o[k] = act2(o[k]);
      if (g < 6) pre(g + 1, h[(g + 1) & 1]);
      if (g < 6) {
        const int u = ub + g % 3;
        char* ap = abase + (u >> 1) * 8192 + (u & 1) * 4096 + (g / 3) * 1024 + ln * 16;
        if (g < WTG) st16m<WTA>(ap, o[0], o[1], o[2], o[3]); else st16m<0>(ap, o[0], o[1], o[2], o[3]);
      } else {
        const auto s0 = __builtin_amdgcn_permlane16_swap(o[0], o[2], false, false);
        const auto s1 = __builtin_amdgcn_permlane16_swap(o[1], o[3], false, false);
        st16m<(WTG > 6) ? WTA : 0>(abase + (us >> 1) * 8192 + (us & 1) * 4096 + (fq & 1) * 1024 + (((tn & 1) * 2 + (fq >> 1)) * 16 + fr) * 16,
                                   s0[0], s1[0], s0[1], s1[1]);
      }
      SBAR();
    }
  };
  epi(std::false_type{});
  {
    const uint32_t mx = max((uint32_t)vmax.x, (uint32_t)vmax.y), mn = min((uint32_t)vmin.x, (uint32_t)vmin.y);
    if (__builtin_amdgcn_ballot_w64(mx > (uint32_t)TB_HI || mn < (uint32_t)(TB_LO - 1)) != 0) epi(std::true_type{});
  }
  STAMP1(4)
  STAMP_DUMP1(0, 8)
  STAMP_DUMP1(24, 32)
}

#define K1T 768
__global__ __launch_bounds__(K1T, 1) void k1_main(
    const u16* __restrict__ x, const u16* __restrict__ logAr, const u16* __restrict__ Aim,
    const u16* __restrict__ Cre, const u16* __restrict__ Cim, const u16* __restrict__ logdt,
    const u16* __restrict__ w1, const u16* __restrict__ b1, const u16* __restrict__ w2, char* __restrict__ ws) {
  TS_BEGIN(0)
  k1_body(x, logAr, Aim, Cre, Cim, logdt, w1, b1, w2, ws);
  TS_END(0)
}

constexpr int K2_X = 1024;
constexpr int K2_AO = K2_X + 68096;
constexpr int K2_C0 = K2_AO + 10240;
constexpr int K2_IMG = 4 * 4160 + 2 * 4608;
constexpr int K2_KM = 4 * 4160;
constexpr int K2_Q = K2_IMG;
constexpr int K2_US = K2_Q + 8192;
constexpr int K2_CG = K2_US + 4096;
constexpr int K2_LDS = K2_C0 + 2 * K2_CG;
constexpr int K2_SEC = K2_IMG + 8192;

__device__ __forceinline__ void k2_body(const u16* __restrict__ x, const u16* __restrict__ Dskip, char* __restrict__ ws,
                                        u16* __restrict__ out) {
  __shared__ __attribute__((aligned(16))) char smem[K2_LDS + 256];
  const int tid = threadIdx.x, lane = tid & 63;
  const int wave = __builtin_amdgcn_readfirstlane(tid >> 6);
  const int bid = blockIdx.x;
  {
    const uint32_t o = min((uint32_t)(wave * 23 + min(lane, 22)) * 64u, K2_CODE - 64u);
    CODE_DMA(K2_SYM, o, (uint32_t)(size_t)(lds_cptr)smem + K2_LDS);
  }
  uint32_t* sy = (uint32_t*)(ws + WS_SY);
  constexpr int cRed = 16, cX = 20, cGo = 24, cC = 32, cB = 36, cS = 40, cCd = 44, cCo = 48;
  const int fr = lane & 15, fq = lane >> 4;
  const int xc = bid & 7, jj = bid >> 3;
  const int rb = (xc & 3) * 8 + (jj & 7), nt = (xc >> 2) * 4 + (jj >> 3);
  if (wave >= 4) {
    const int grp = (wave - 4) >> 2, cw = wave & 3;
    const uint32_t sb0 = (uint32_t)(size_t)(lds_cptr)smem, sbase = sb0 + grp * 32;
    char* img = smem + K2_C0 + grp * K2_CG;
    char* sec = smem + K2_X + grp * K2_SEC;
    uint32_t cT = 0, bT = 0;
    auto cbar = [&]() { cT += 4; garrive<cC>(sbase, lane); gwait<cC>(sbase, cT); };
    const int kg = fq;
    const int ubo = ((lane >> 2) & 3) * 4160 + 32 * (kg >> 1) - 16 * (kg & 1) + (lane & 3) * 8 + 56 + 64 * cw;
    const int kao = K2_KM + 512 + 32 * fr - 32 * (kg >> 1) + 16 * (kg & 1) - 64 * cw - 256;
    const int pwo = fr * 64 + ((kg ^ ((fr >> 1) & 3) ^ (fr >> 3)) * 16);
    const int mine = grp * 256 + bid;
    int bun = mine;
    auto fetch = [&](int d, u32x4& uv, u32x2& up, u32x4& k0, u32x4& k1, uint32_t& dw) {
      int c2 = tid & 255;
      asm volatile("" : "+v"(c2));
      dw = ((const uint32_t*)Dskip)[d >> 1];
      const char* us = ws + WS_XNT + (size_t)d * 4096 + c2 * 16;
      uv = *(const u32x4*)us;
      up = *(const u32x2*)(c2 ? us - 8 : us);
      const int i0 = (c2 < 32) ? 8 * c2 : (c2 < 64) ? 128 + 4 * c2 : (c2 < 128) ? 256 + 2 * c2 : 384 + c2;
      const u32x4_a4* kp = (const u32x4_a4*)(ws + WS_K + (size_t)d * KC_ + i0 * 4);
      k0 = kp[0];
      k1 = kp[c2 < 64 ? 1 : 0];
    };
    auto build = [&](char* im, const u32x4& uv, const u32x2& up, const u32x4& k0, const u32x4& k1, int ct) {
      {
        uint32_t a[4], b[4];
        a[0] = k0[0];
        if (cw == 0) {
          const bool lo_ = ct < 32;
          b[0] = lo_ ? k0[1] : k0[0];
          a[1] = lo_ ? k0[2] : k0[1]; b[1] = lo_ ? k0[3] : k0[2];
          a[2] = lo_ ? k1[0] : k0[2]; b[2] = lo_ ? k1[1] : k0[2];
          a[3] = lo_ ? k1[2] : k0[3]; b[3] = lo_ ? k1[3] : k1[0];
        } else if (cw == 1) {
          b[0] = a[1] = k0[0]; b[1] = a[2] = b[2] = k0[1]; a[3] = b[3] = k0[2];
        } else {
          b[0] = a[1] = b[1] = k0[0]; a[2] = (ct & 1) ? k0[1] : k0[0]; b[2] = a[3] = b[3] = k0[1];
        }
        u32x4 h, l;
#pragma unroll
        for (int i = 0; i < 4; ++i) { h[i] = __builtin_amdgcn_perm(b[i], a[i], 0x05040100u); l[i] = __builtin_amdgcn_perm(b[i], a[i], 0x07060302u); }
        *(u32x4*)(im + K2_KM + 512 + ct * 16) = h;
        *(u32x4*)(im + K2_KM + 4608 + 512 + ct * 16) = l;
      }
      const uint32_t pm2 = ct ? up.x : 0u, pm1 = ct ? up.y : 0u;
      const uint32_t am = __builtin_amdgcn_alignbit(pm1, pm2, 16), a0 = __builtin_amdgcn_alignbit(uv.x, pm1, 16);
      const uint32_t a1 = __builtin_amdgcn_alignbit(uv.y, uv.x, 16), a2 = __builtin_amdgcn_alignbit(uv.z, uv.y, 16);
      const uint32_t a3 = __builtin_amdgcn_alignbit(uv.w, uv.z, 16);
      char* ud = im + 64 + ct * 16;
      *(u32x4*)(ud) = uv;
      *(uint4*)(ud + 4160) = make_uint4(a0, a1, a2, a3);
      *(uint4*)(ud + 2 * 4160) = make_uint4(pm1, uv.x, uv.y, uv.z);
      *(uint4*)(ud + 3 * 4160) = make_uint4(am, a0, a1, a2);
      if (ct < 64) *(uint4*)(im + K2_KM + (ct >> 5) * 4608 + (ct & 31) * 16) = make_uint4(0, 0, 0, 0);
      else if (ct < 80) *(uint4*)(im + ((ct >> 2) & 3) * 4160 + (ct & 3) * 16) = make_uint4(0, 0, 0, 0);
    };
    auto reduce = [&](int d, uint32_t dw, int mask, const char* p0, int ps, const char* p3, const char* usrc, int ct) {
      if ((mask >> (ct >> 5)) & 1) {
        const int rsw = ((ct >> 2) & 3) ^ ((ct >> 4) & 1);
        const int rc0 = ((mask == 0xFF) ? (ct >> 5) : (ct >> 7)) * 1024 + ((ct >> 1) & 15) * 64 + (((ct & 1) * 2) ^ rsw) * 16, rc1 = rc0 ^ 16;
        const f32x4 s0 = add4(*(const f32x4*)(p0 + rc0), *(const f32x4*)(p0 + ps + rc0), *(const f32x4*)(p0 + 2 * ps + rc0), *(const f32x4*)(p3 + rc0));
        const f32x4 s1 = add4(*(const f32x4*)(p0 + rc1), *(const f32x4*)(p0 + ps + rc1), *(const f32x4*)(p0 + 2 * ps + rc1), *(const f32x4*)(p3 + rc1));
        const u32x4 uu = *(const u32x4*)(usrc + ct * 16);
        const float y[8] = {s0[0], s0[1], s0[2], s0[3], s1[0], s1[1], s1[2], s1[3]};
        const float dsk = (d & 1) ? bfhi(dw) : bflo(dw);
        uint32_t o4[4];
#pragma unroll
        for (int i = 0; i < 4; ++i) {
          const uint32_t yb = pack2(y[2 * i], y[2 * i + 1]), sb = pack2(fmul(bflo(uu[i]), dsk), fmul(bfhi(uu[i]), dsk));
          o4[i] = min(pack2(fadd(bflo(yb), bflo(sb)), fadd(bfhi(yb), bfhi(sb))), 0xfffffffeu);
        }
        const int dn = d / 80;
        st16wt(ws + WS_AO + (size_t)(dn * 327680 + (d - dn * 80) * 128 + (ct >> 3) * 10240 + (ct & 7) * 16), o4[0], o4[1], o4[2], o4[3]);
      }
    };
#define NP 1
#define PSL 2
#define PST 10
    auto poll = [&]() -> bool {
      const __amdgpu_buffer_rsrc_t ra = __builtin_amdgcn_make_buffer_rsrc((void*)(ws + WS_AO), 0, L_ * D_ * 2, 0x00020000);
      const uint32_t vo = (uint32_t)((nt * 32 + rb) * 10240 + lane * 16);
      uint32_t done = gwait<cGo>(sb0, 0), got = 0;
      if (done == 1023) return true;
      u32x4 t[10];
#pragma unroll
      for (int i = 0; i < 10; ++i)
        if (!((done >> i) & 1)) t[i] = __builtin_bit_cast(u32x4, bld<16>(ra, vo, i * 1024));
#pragma unroll
      for (int i = 0; i < 10; ++i)
        if (!((done >> i) & 1)) {
          const uint32_t m = max(max(t[i][0], t[i][1]), max(t[i][2], t[i][3]));
          if (__builtin_amdgcn_ballot_w64(m == 0xffffffffu) == 0) {
            *(u32x4*)(smem + K2_AO + i * 1024 + lane * 16) = t[i];
            got |= 1u << i;
          }
        }
      asm volatile("s_waitcnt lgkmcnt(0)" ::: "memory");
      if (got && lane == 0) asm volatile("ds_or_b32 %0, %1 offset:%2" ::"v"(sb0), "v"(got), "n"(cGo) : "memory");
      return (done | got) == 1023;
    };
    u32x4 uv, k0, k1, uv2, k02, k12; u32x2 up, up2;
    uint32_t dwn, dw2;
    fetch(bun, uv, up, k0, k1, dwn);
    fetch(512 + (bid & 127), uv2, up2, k02, k12, dw2);
    if ((tid & 255) == 192) __hip_atomic_store((gu32*)(sy + SY_CLC + mine), 1u, RLX_AGENT);
    __builtin_amdgcn_s_barrier();
#define PC 2
#define PR 2
    __builtin_amdgcn_s_setprio(PR);
#pragma unroll 1
    for (;;) {
#pragma unroll 1
      for (int task = 0; task < 2; ++task) {
        STAMP(8 + grp * 12 + task * 5)
#define SWP 1
        const bool qk = (task != 0) != (bT < 8 && (SWP == 2 || (SWP == 1 && grp == 1)));
        const bool us = qk && bT < 8;
        const int d = qk ? 512 + (bun & 127) : bun;
        int ct = tid & 255;
        asm volatile("" : "+v"(ct));
        const uint32_t dw = dwn;
        const bool s2 = (bT == 4);
        if (!s2) {
          if (task) cbar();
          build(img, uv, up, k0, k1, ct);
          *(u32x4*)(img + K2_US + ct * 16) = uv;
          STAMP(9 + grp * 12 + task * 5)
          if (bT == 0) build(sec, uv2, up2, k02, k12, ct);
          __builtin_amdgcn_s_waitcnt(0x0F70);
          cbar();
        } else {
          __builtin_amdgcn_s_waitcnt(0x0F70);
        }
        STAMP(10 + grp * 12 + task * 5)
        f32x4 acc[8];
#pragma unroll
        for (int i = 0; i < 8; ++i) acc[i] = (f32x4){0.f, 0.f, 0.f, 0.f};
        const int qt = bun >> 7;
        const int mq = (1 << qt) | (0x80 >> qt), mask = qk ? mq : 0xFF;
        lds_cptr ub = (lds_cptr)((us ? sec : img) + ubo);
        lds_cptr ka = (lds_cptr)((us ? sec : img) + kao);
        char* Pw = (us ? sec + K2_IMG + cw * 2048 : img + ((cw == 3) ? K2_Q : cw * 8192)) + pwo;
        bT += 4;
        uint32_t pb = 0;
        bool early = us;
        auto hook = [&](auto AA, auto MK) {
          constexpr int a = decltype(AA)::value, mk = decltype(MK)::value;
          constexpr int sh = (mk == 0xFF) ? 0 : 2;
          if constexpr (a == 0) garrive<cB>(sbase, lane);
          if constexpr (a == 1) asm volatile("ds_read_b32 %0, %1 offset:%2" : "+v"(pb) : "v"(sbase), "n"(cB) : "memory");
          if constexpr (a == 2) {
            __builtin_amdgcn_s_waitcnt(0xC07F);
            asm volatile("" : "+v"(pb)::"memory");
            early = early || (uint32_t)__builtin_amdgcn_readfirstlane(pb) >= bT;
            if constexpr (mk & 1) { if (early) *(f32x4*)(Pw) = acc[0]; }
          }
          if constexpr (a > 2) {
            if (!early) {
              uint32_t c0;
              asm volatile("ds_read_b32 %0, %1 offset:%2\n\ts_waitcnt lgkmcnt(0)" : "=v"(c0) : "v"(sbase), "n"(cB) : "memory");
              early = (uint32_t)__builtin_amdgcn_readfirstlane(c0) >= bT;
              if (early) {
#pragma unroll
                for (int i = 0; i < a - 1; ++i)
                  if ((mk >> i) & 1) *(f32x4*)(Pw + (i >> sh) * 1024) = acc[i];
              }
            }
          }
          if constexpr (a >= 2) {
            if constexpr ((mk >> (a - 1)) & 1) { if (early) *(f32x4*)(Pw + ((a - 1) >> sh) * 1024) = acc[a - 1]; }
          }
        };
        __builtin_amdgcn_s_setprio(PC);
        if (mask == 0xFF) conv_mfma<0xFF>(acc, ka, ub, hook);
        else if (mask == 0x81) conv_mfma<0x81>(acc, ka, ub, hook);
        else if (mask == 0x42) conv_mfma<0x42>(acc, ka, ub, hook);
        else if (mask == 0x24) conv_mfma<0x24>(acc, ka, ub, hook);
        else conv_mfma<0x18>(acc, ka, ub, hook);
        __builtin_amdgcn_s_setprio(PR);
        STAMP(11 + grp * 12 + task * 5)
        if (early) {
          if (mask == 0xFF) *(f32x4*)(Pw + 7 * 1024) = acc[7];
          else if (mask & 0x80) *(f32x4*)(Pw + 1024) = acc[7];
          else if (mask & 0x40) *(f32x4*)(Pw + 1024) = acc[6];
          else if (mask & 0x20) *(f32x4*)(Pw + 1024) = acc[5];
          else *(f32x4*)(Pw + 1024) = acc[4];
        } else {
          gwait<cB>(sbase, bT);
#pragma unroll
          for (int i = 0; i < 8; ++i)
            if ((mask >> i) & 1) *(f32x4*)(Pw + ((mask == 0xFF) ? i : (i >> 2)) * 1024) = acc[i];
        }
        uv2 = k02 = k12 = (u32x4){0u, 0u, 0u, 0u}; up2 = (u32x2){0u, 0u};
        if (bT == 4) { uv = k0 = k1 = (u32x4){0u, 0u, 0u, 0u}; up = (u32x2){0u, 0u}; continue; }
        if (task == 0) fetch(512 + (bun & 127), uv, up, k0, k1, dwn);
        else { uv = k0 = k1 = (u32x4){0u, 0u, 0u, 0u}; up = (u32x2){0u, 0u}; }
        cbar();
        STAMP(19 + grp * 12)
        asm volatile("" : "+v"(ct));
        reduce(s2 ? bun : d, dw, s2 ? 0xFF : mask, img, 8192, img + K2_Q, img + K2_US, ct);
        if (s2) {
          reduce(512 + (bun & 127), dw2, mq, sec + K2_IMG, 2048, sec + K2_IMG + 6144, sec + 64, ct);
          garrive<cX>(sb0, lane);
        }
        STAMP(12 + grp * 12 + task * 5)
      }
      if (cw == 0) {
        uint32_t dec = 1;
        for (;;) {
          uint32_t c[8];
#pragma unroll
          for (int i = 0; i < 8; ++i) c[i] = __hip_atomic_load((gu32*)(sy + SY_CLC + i * 64 + lane), RLX_AGENT);
          int t = -1;
#pragma unroll
          for (int i = 7; i >= 0; --i) {
            const unsigned long long m = __builtin_amdgcn_ballot_w64(c[i] == 0u);
            if (m) t = i * 64 + __builtin_ctzll(m);
          }
          if (t < 0) break;
          uint32_t o = 1;
          if (lane == 0) o = __hip_atomic_exchange((gu32*)(sy + SY_CLC + t), 1u, RLX_AGENT);
          if (__builtin_amdgcn_readfirstlane(o) == 0) { dec = 2 + t; break; }
        }
        gset<cCd>(sbase, dec + (bT << 10), lane);
      }
      const uint32_t dec = gwait<cCd>(sbase, bT << 10) & 1023u;
      if (dec == 1) break;
      bun = dec - 2;
      fetch(bun, uv, up, k0, k1, dwn);
      cbar();
    }
    STAMP(18 + grp * 12)
    if (grp == 0 && cw >= 4 - NP) {
      gwait<cX, 2>(sb0, 8);
      for (;;) {
        if (poll()) break;
        __builtin_amdgcn_s_sleep(PSL);
      }
    }
    STAMP_DUMP(8 + grp * 12, 20 + grp * 12)
    return;
  }
  STAMP(0)
  const uint32_t sbase = (uint32_t)(size_t)(lds_cptr)smem;
  const __amdgpu_buffer_rsrc_t rw = __builtin_amdgcn_make_buffer_rsrc((void*)ws, 0, (int)WS_AO, 0x00020000);
  const uint32_t gw = WS_W2 + (uint32_t)(nt * 56 + wave) * 5120u;
  const uint32_t gA = WS_ACT + (uint32_t)(rb * 56 + wave) * 4096u;
  const uint32_t vo = (uint32_t)lane * 16u;
  bf16x8 wf[2][5], xf[2][4];
#define R2 0
  const int rot = (R2 == 0) ? 0 : (R2 == 1) ? ((jj & 7) * 7 / 4 + (jj >> 3) * 7 / 2) % 14 : (R2 == 2) ? ((jj >> 3) * 7 / 2) : ((jj & 7) * 7 / 4);
  if (tid < 24) *(uint32_t*)(smem + 16 + tid * 4) = 0;
  asm volatile("s_waitcnt lgkmcnt(0)" ::: "memory");
  __builtin_amdgcn_s_barrier();
  SBAR();
#pragma unroll
  for (int s2 = 0; s2 < 2; ++s2) {
#pragma unroll
    for (int i = 0; i < 5; ++i) { wf[s2][i] = bld<0>(rw, vo + (i & 3) * 1024, gw + ((rot + s2) % 14) * 20480 + (i >> 2) * 4096); SBAR(); }
#pragma unroll
    for (int j = 0; j < 4; ++j) { xf[s2][j] = bld<0>(rw, vo + j * 1024, gA + ((rot + s2) % 14) * 16384); SBAR(); }
  }
#define PG2 1
  __builtin_amdgcn_s_setprio(PG2);
  f32x4 acc[5][4];
#pragma unroll
  for (int i = 0; i < 5; ++i)
#pragma unroll
    for (int j = 0; j < 4; ++j) acc[i][j] = (f32x4){0.f, 0.f, 0.f, 0.f};
  STAMP(1)
#pragma unroll 1
  for (int r = 0; r < 7; ++r) {
#pragma unroll
    for (int s2 = 0; s2 < 2; ++s2) {
      const int un = (r * 2 + s2 + 2) * 4 + wave;
      const uint32_t vn = (un < 56) ? vo : 0x40000000u;
      int ii = r * 2 + s2 + 2 + rot;
      if (ii >= 14) ii -= 14;
      const uint32_t pw = gw + (uint32_t)ii * 20480u, pa = gA + (uint32_t)ii * 16384u;
#pragma unroll
      for (int i = 0; i < 5; ++i) {
#pragma unroll
        for (int j = 0; j < 4; ++j)
          acc[i][j] = __builtin_amdgcn_mfma_f32_16x16x32_bf16(wf[s2][i], xf[s2][j], acc[i][j], 0, 0, 0);
        SBAR();
        wf[s2][i] = bld<0>(rw, vn + (i & 3) * 1024, pw + (i >> 2) * 4096);
        SBAR();
      }
#pragma unroll
      for (int j = 0; j < 4; ++j) { xf[s2][j] = bld<0>(rw, vn + j * 1024, pa); SBAR(); }
    }
  }
  STAMP(2)
  int ln = lane;
  asm volatile("" : "+v"(ln));
  f32x4 own[5];
  gwait<cX>(sbase, 8);
  static_for<4>([&](auto JJ) {
    constexpr int j = decltype(JJ)::value;
    if (wave == j) {
#pragma unroll
      for (int i = 0; i < 5; ++i) own[i] = acc[i][j];
    } else {
      char* xd = smem + K2_X + (j * 3 + ((wave - j - 1) & 3)) * 5120 + ln * 16;
#pragma unroll
      for (int i = 0; i < 5; ++i) *(f32x4*)(xd + i * 1024) = acc[i][j];
    }
  });
  garrive<cRed>(sbase, lane);
  const int m0 = rb * 64 + wave * 16, n0 = nt * 80;
  const size_t eo = (size_t)(m0 + (ln & 15)) * D_ + n0 + (ln >> 4) * 4;
  uint2 xv[5];
#pragma unroll
  for (int i = 0; i < 5; ++i) xv[i] = *(const uint2*)(x + eo + i * 16);
  STAMP(3)
  gwait<cRed>(sbase, 4);
  {
    const char* xs = smem + K2_X + wave * 15360 + ln * 16;
#pragma unroll
    for (int i = 0; i < 5; ++i)
      own[i] = (own[i] + *(const f32x4*)(xs + i * 1024)) + (*(const f32x4*)(xs + 5120 + i * 1024) + *(const f32x4*)(xs + 10240 + i * 1024));
  }
  gwait<cGo>(sbase, 1023);
  STAMP(4)
  lds_cptr aq = (lds_cptr)(smem + K2_AO + ((ln >> 4) * 4 + ((ln >> 2) & 3)) * 128 + wave * 32 + (ln & 3) * 8);
#pragma unroll
  for (int i = 0; i < 5; ++i) {
    const u32x2 a = __builtin_bit_cast(u32x2, trd(aq + i * 2048));
    const float v0 = bfr(bflo(xv[i].x) + bflo(a.x)) + bfr(own[i][0]);
    const float v1 = bfr(bfhi(xv[i].x) + bfhi(a.x)) + bfr(own[i][1]);
    const float v2 = bfr(bflo(xv[i].y) + bflo(a.y)) + bfr(own[i][2]);
    const float v3 = bfr(bfhi(xv[i].y) + bfhi(a.y)) + bfr(own[i][3]);
    *(uint2*)(out + eo + i * 16) = make_uint2(pack2(v0, v1), pack2(v2, v3));
  }
  STAMP(5)
  STAMP_DUMP(0, 8)
}

__global__ __launch_bounds__(768, 1) void k2_main(const u16* __restrict__ x, const u16* __restrict__ Dskip,
                                                   char* __restrict__ ws, u16* __restrict__ out) {
  TS_BEGIN(1)
  k2_body(x, Dskip, ws, out);
  TS_END(1)
}

}

extern "C" void kernel_launch(void* const* d_in, const int* in_sizes, int n_in,
                              void* d_out, int out_size, void* d_ws, size_t ws_size,
                              hipStream_t stream) {
  const u16* x = (const u16*)d_in[0];
  k1_main<<<256, K1T, 0, stream>>>(x, (const u16*)d_in[1], (const u16*)d_in[2], (const u16*)d_in[3], (const u16*)d_in[4],
                                   (const u16*)d_in[5], (const u16*)d_in[8], (const u16*)d_in[9], (const u16*)d_in[10],
                                   (char*)d_ws);
  k2_main<<<256, 768, 0, stream>>>(x, (const u16*)d_in[6], (char*)d_ws, (u16*)d_out);
}
```
